```python
import torch

def _softmax(x, axis=-1):
    return torch.softmax(x, dim=axis)
def _gelu(x):
    return torch.nn.functional.gelu(x, approximate="tanh")
def _silu(x):
    return torch.nn.functional.silu(x)
def _sigmoid(x):
    return torch.sigmoid(x)
def _layernorm(x, axis=-1, eps=1e-5):
    m = x.mean(dim=axis, keepdim=True)
    v = x.var(dim=axis, keepdim=True, unbiased=False)
    return (x - m) / torch.sqrt(v + eps)
def _scatter_add(tgt, idx, src, axis=0):
    return tgt.clone().index_add_(axis, idx, src)
def _segment_sum(src, seg, n):
    out = torch.zeros((n,) + src.shape[1:], dtype=src.dtype)
    return out.index_add_(0, seg, src)
def _pad(x, pw):
    flat = tuple(v for pair in reversed(pw) for v in pair)
    return torch.nn.functional.pad(x, flat)

kernel_name = 'sequential_preLN__gqa_block_cached__swiglu_ffn__decode'

def setup_inputs():

    _w = (lambda a: (lambda t: t.to(torch.bfloat16) if t.is_floating_point() else t)(torch.as_tensor(a)))
    def _A_setup():
        B, Sq, D = 4, 1, 512
        Hq, Hkv, Sk, Dh = 32, 8, 16384, 128
        assert Hq % Hkv == 0, (Hq, Hkv)
        return {
            "x": _w((lambda *s: torch.randn(*s, dtype=torch.bfloat16) * 0.05)(B, Sq, D)),
            "k_cache": _w((lambda *s: torch.randn(*s, dtype=torch.bfloat16) * 3.2)(B, Hkv, Sk, Dh)),
            "v_cache": _w((lambda *s: torch.randn(*s, dtype=torch.bfloat16) * 0.4)(B, Hkv, Sk, Dh)),
            "wq": _w((lambda *s: torch.randn(*s, dtype=torch.bfloat16) * 3.2)(D, Hq * Dh) * (D ** -0.5)),
            "wo": _w((lambda *s: torch.randn(*s, dtype=torch.bfloat16) * 0.4)(Hq * Dh, D) * ((Hq * Dh) ** -0.5)),
        }
    _A_in = _A_setup()

    def _B_setup():
        B, S, D, F = 4, 1, 512, 1792
        return {
            "x": _w((lambda *s: torch.randn(*s, dtype=torch.bfloat16) * 0.05)(B, S, D)),
            "w_gate": _w((lambda *s: torch.randn(*s, dtype=torch.bfloat16) * 0.05)(D, F)),
            "w_up": _w((lambda *s: torch.randn(*s, dtype=torch.bfloat16) * 0.05)(D, F)),
            "w_down": _w((lambda *s: torch.randn(*s, dtype=torch.bfloat16) * 0.05)(F, D)),
        }
    _B_in = _B_setup()

    return {
        "A_x": _A_in["x"],
        "A_k_cache": _A_in["k_cache"],
        "A_v_cache": _A_in["v_cache"],
        "A_wq": _A_in["wq"],
        "A_wo": _A_in["wo"],
        "B_x": _B_in["x"],
        "B_w_gate": _B_in["w_gate"],
        "B_w_up": _B_in["w_up"],
        "B_w_down": _B_in["w_down"],
    }

def reference(A_x, A_k_cache, A_v_cache, A_wq, A_wo, B_x, B_w_gate, B_w_up, B_w_down):
    def _A_ref(x, k_cache, v_cache, wq, wo):
        B, Sq, D = x.shape
        Hq, Dh = 32, 128
        Hkv = k_cache.shape[1]
        rep = Hq // Hkv
        q = (lambda x, *d: x.permute(*d))((x @ wq).reshape(B, Sq, Hq, Dh), 0, 2, 1, 3)
        k = (lambda x, n, axis: torch.repeat_interleave(x, n, dim=axis))(k_cache, rep, axis=1)
        v = (lambda x, n, axis: torch.repeat_interleave(x, n, dim=axis))(v_cache, rep, axis=1)
        scores = torch.einsum("bhqd,bhkd->bhqk", q, k) * (Dh ** -0.5)
        p = _softmax(scores, axis=-1)
        o = torch.einsum("bhqk,bhkd->bhqd", p, v)
        return (lambda x, *d: x.permute(*d))(o, 0, 2, 1, 3).reshape(B, Sq, Hq * Dh) @ wo

    def _B_ref(x, w_gate, w_up, w_down):
        g = _silu(x @ w_gate)
        u = x @ w_up
        return (g * u) @ w_down

    x = A_x
    h = x + _A_ref(_layernorm(x, axis=-1), A_k_cache, A_v_cache, A_wq, A_wo)
    h2 = h + _B_ref(_layernorm(h, axis=-1), B_w_gate, B_w_up, B_w_down)
    return h2
```

```cpp
#include <hip/hip_runtime.h>
#include <hip/hip_bf16.h>
#include <stdint.h>
#define MIMIC 1
typedef unsigned short u16;
__device__ __forceinline__ float bf2f(u16 b){ return __uint_as_float(((unsigned)b)<<16); }
__device__ __forceinline__ u16 f2bf(float f){ __hip_bfloat16 h = __float2bfloat16(f); return *(u16*)&h; }
__device__ __forceinline__ float rb(float f){ return MIMIC ? bf2f(f2bf(f)) : f; }
constexpr int B=4,D=512,HQ=32,HKV=8,SK=16384,DH=128,F=1792;
constexpr int NS=32;

__device__ float block_sum(float v, float* sh){
  for(int o=32;o>0;o>>=1) v += __shfl_xor(v,o);
  int w=threadIdx.x>>6; __syncthreads(); if((threadIdx.x&63)==0) sh[w]=v; __syncthreads();
  float s=0; for(int i=0;i<(int)(blockDim.x>>6);i++) s+=sh[i]; return s;
}
__device__ void layernorm(const float* sx, float* sy, float* sh){
  int t=threadIdx.x;
  float a=sx[t], b=sx[t+256];
  float m=block_sum(a+b,sh)/512.f;
  float da=a-m, db=b-m;
  float v=block_sum(da*da+db*db,sh)/512.f;
  float mr=rb(m), vr=rb(v);
  float den=rb(sqrtf(rb(vr+1e-5f)));
  sy[t]=rb(rb(a-mr)/den); sy[t+256]=rb(rb(b-mr)/den);
  __syncthreads();
}
__global__ void k_q(const u16* x,const u16* wq,float* q){
  __shared__ float sx[512], sy[512], sh[8];
  int b=blockIdx.y, t=threadIdx.x;
  sx[t]=bf2f(x[b*D+t]); sx[t+256]=bf2f(x[b*D+t+256]); __syncthreads();
  layernorm(sx,sy,sh);
  int n=blockIdx.x*256+t; float acc=0;
  for(int k=0;k<D;k++) acc+=sy[k]*bf2f(wq[(size_t)k*4096+n]);
  q[b*4096+n]=rb(acc);
}
__global__ void k_attn(const u16* K,const u16* V,const float* q,float* pm,float* pl,float* po){
  __shared__ float sq[512], sc[4][SK/NS], red[8];
  int s=blockIdx.x,h=blockIdx.y,b=blockIdx.z,t=threadIdx.x;
  constexpr int NK=SK/NS;
  sq[t]=q[b*4096+h*512+t]; sq[t+256]=q[b*4096+h*512+t+256]; __syncthreads();
  const u16* kb=K+((size_t)(b*HKV+h)*SK+s*NK)*DH; const u16* vb=V+((size_t)(b*HKV+h)*SK+s*NK)*DH;
  for(int k=t;k<NK;k+=256){ float a[4]={0,0,0,0};
    const uint4* kr=(const uint4*)(kb+(size_t)k*DH);
    for(int c=0;c<16;c++){ uint4 u=kr[c]; unsigned w[4]={u.x,u.y,u.z,u.w};
      for(int j=0;j<4;j++){ float lo=__uint_as_float(w[j]<<16), hi=__uint_as_float(w[j]&0xffff0000u);
        for(int g=0;g<4;g++){ a[g]+=lo*sq[g*128+c*8+2*j]; a[g]+=hi*sq[g*128+c*8+2*j+1]; } } }
    for(int g=0;g<4;g++) sc[g][k]=rb(rb(a[g])*0.08838834764831845f);
  }
  __syncthreads();
  int w=t>>6,l=t&63;
  float m=-INFINITY; for(int k=l;k<NK;k+=64) m=fmaxf(m,sc[w][k]);
  for(int o=32;o>0;o>>=1) m=fmaxf(m,__shfl_xor(m,o));
  float ls=0; for(int k=l;k<NK;k+=64){ float p=__expf(sc[w][k]-m); sc[w][k]=p; ls+=p; }
  for(int o=32;o>0;o>>=1) ls+=__shfl_xor(ls,o);
  __syncthreads();
  float o0=0,o1=0;
  for(int k=0;k<NK;k++){ unsigned u=*(const unsigned*)(vb+(size_t)k*DH+2*l); float p=sc[w][k];
    o0+=p*__uint_as_float(u<<16); o1+=p*__uint_as_float(u&0xffff0000u);}
  size_t idx=((size_t)(b*HQ+h*4+w)*NS+s);
  po[idx*128+2*l]=o0; po[idx*128+2*l+1]=o1; if(l==0){pm[idx]=m;pl[idx]=ls;}
}
__global__ void k_comb(const float* pm,const float* pl,const float* po,float* o){
  int r=blockIdx.x,t=threadIdx.x; float M=-INFINITY; for(int s=0;s<NS;s++) M=fmaxf(M,pm[r*NS+s]);
  float L=0,a=0; for(int s=0;s<NS;s++){ float w=__expf(pm[r*NS+s]-M); L+=w*pl[r*NS+s]; a+=w*po[((size_t)r*NS+s)*128+t]; }
  o[r*128+t]=rb(a/L);
}
__global__ void k_wo(const float* o,const u16* wo,const u16* x,float* h){
  __shared__ float sh[256];
  int b=blockIdx.y,t=threadIdx.x,n=blockIdx.x*64+(t&63),ks=t>>6; float acc=0;
  for(int k=ks*1024;k<ks*1024+1024;k++) acc+=o[b*4096+k]*bf2f(wo[(size_t)k*512+n]);
  sh[t]=acc; __syncthreads();
  if(t<64){ float a=sh[t]+sh[t+64]+sh[t+128]+sh[t+192]; h[b*512+n]=rb(bf2f(x[b*512+n])+rb(a)); }
}
__global__ void k_gu(const float* h,const u16* wg,const u16* wu,float* act){
  __shared__ float sx[512], sy[512], sh[8];
  int b=blockIdx.y,t=threadIdx.x; sx[t]=h[b*512+t]; sx[t+256]=h[b*512+t+256]; __syncthreads();
  layernorm(sx,sy,sh);
  int n=blockIdx.x*256+t; float g=0,u=0;
  for(int k=0;k<D;k++){ g+=sy[k]*bf2f(wg[(size_t)k*F+n]); u+=sy[k]*bf2f(wu[(size_t)k*F+n]); }
  g=rb(g);u=rb(u); float sg=rb(g/(1.f+__expf(-g))); act[b*F+n]=rb(sg*u);
}
__global__ void k_dn(const float* act,const u16* wd,const float* h,u16* out){
  __shared__ float sh[256];
  int b=blockIdx.y,t=threadIdx.x,n=blockIdx.x*64+(t&63),ks=t>>6; float acc=0;
  for(int k=ks*448;k<ks*448+448;k++) acc+=act[b*F+k]*bf2f(wd[(size_t)k*512+n]);
  sh[t]=acc; __syncthreads();
  if(t<64){ float a=sh[t]+sh[t+64]+sh[t+128]+sh[t+192]; out[b*512+n]=f2bf(h[b*512+n]+rb(a)); }
}
extern "C" void kernel_launch(void* const* d_in, const int* in_sizes, int n_in, void* d_out, int out_size, void* d_ws, size_t ws_size, hipStream_t stream){
  const u16* x=(const u16*)d_in[0]; const u16* K=(const u16*)d_in[1]; const u16* V=(const u16*)d_in[2];
  const u16* wq=(const u16*)d_in[3]; const u16* wo=(const u16*)d_in[4];
  const u16* wg=(const u16*)d_in[6]; const u16* wu=(const u16*)d_in[7]; const u16* wd=(const u16*)d_in[8];
  float* ws=(float*)d_ws; float* q=ws; float* pm=q+B*4096; float* pl=pm+B*HQ*NS; float* po=pl+B*HQ*NS; float* o=po+(size_t)B*HQ*NS*128; float* h=o+B*4096; float* act=h+B*512;
  k_q<<<dim3(16,B),256,0,stream>>>(x,wq,q);
  k_attn<<<dim3(NS,HKV,B),256,0,stream>>>(K,V,q,pm,pl,po);
  k_comb<<<B*HQ,128,0,stream>>>(pm,pl,po,o);
  k_wo<<<dim3(8,B),256,0,stream>>>(o,wo,x,h);
  k_gu<<<dim3(7,B),256,0,stream>>>(h,wg,wu,act);
  k_dn<<<dim3(8,B),256,0,stream>>>(act,wd,h,(u16*)d_out);
}
```

```cpp
#include <hip/hip_runtime.h>
#include <stdint.h>

#define FK_W 5
#define FK_WNT 1
#define FK_W0 120
#define FK_P1 18
#define FK_P2 22
#define FK_ORD 1
#define FK_G0 0
#define FK_WB 7
#define FK_WC 10
#define FK_LG 60
#define FK_T0 80
#define FK_TAU 1870
#define FK_D0 40
#define FK_D1 12
#define FK_D2 10
#define FK_D3 14
#define FK_STAMPS 0

namespace fk {
#define GAS __attribute__((address_space(1)))
#define LAS __attribute__((address_space(3)))
typedef unsigned u32;
typedef unsigned long long u64;
typedef unsigned short u16;
typedef u32 v2u __attribute__((ext_vector_type(2)));
typedef u32 v4u __attribute__((ext_vector_type(4)));
typedef int v4i __attribute__((ext_vector_type(4)));
typedef float f2 __attribute__((ext_vector_type(2)));
typedef float f4 __attribute__((ext_vector_type(4)));
typedef short s4 __attribute__((ext_vector_type(4)));
typedef __bf16 b2 __attribute__((ext_vector_type(2)));
typedef __bf16 b8 __attribute__((ext_vector_type(8)));

constexpr int DM = 512, SK = 16384, FF = 1792, NQ = 4096;
constexpr int NCH = 32;
constexpr float L2E = 1.4426950408889634f, SCALE = 0.08838834764831845f, EPS = 1e-5f;
constexpr u32 WS_E = 0, WS_C = 1024  , WS_QG = 4096, WS_O = WS_QG + 32 * 16384, WS_ML = WS_O + 256 * 2048, WS_Y = WS_ML + 256 * 64,
              WS_D = WS_Y + 8 * 65536, WS_END = WS_D + 4 * 56 * 2048, WS_DBG = WS_END;
constexpr u32 OOB = 0x80000000u;
constexpr u32 L_RING = 0, L_FLAG = 131072  , L_QRED = L_FLAG + 256  , L_QS = L_QRED + 4096,
              L_PRIV = L_QS + 1024  , L_OM = L_PRIV + 8192, L_MLS = L_OM + 8192, L_YRED = L_MLS + 128, L_HS = L_YRED + 1024,
              L_GU = L_HS + 1024, L_END = L_GU + 1024;
constexpr u32 SPIN_LDS = 1u << 19, SPIN_MEM = 1u << 16;

struct Params {
  const u16 *x, *k, *v, *wq, *wo, *wg, *wu, *wd;
  u16* out;
  unsigned char* ws;
};

#define CB() asm volatile("" ::: "memory")
__device__ __forceinline__ void wg_barrier() {
  asm volatile("s_waitcnt lgkmcnt(0)\n\ts_barrier" ::: "memory");
  __builtin_amdgcn_sched_barrier(0);
}
__device__ __forceinline__ float asf(u32 x) { return __builtin_bit_cast(float, x); }
__device__ __forceinline__ u32 asu(float x) { return __builtin_bit_cast(u32, x); }
__device__ __forceinline__ u32 pk(float lo, float hi) { f2 z = {lo, hi}; return __builtin_bit_cast(u32, __builtin_convertvector(z, b2)); }
__device__ __forceinline__ float blo(u32 p) { return asf(p << 16); }
__device__ __forceinline__ float bhi(u32 p) { return asf(p & 0xffff0000u); }
__device__ __forceinline__ float rb(float x) { return blo(pk(x, x)); }
template <int CTRL, int RM = 0xf> __device__ __forceinline__ float dpp0(float x) {
  return asf(__builtin_amdgcn_update_dpp(0, asu(x), CTRL, RM, 0xf, true));
}
template <int CTRL> __device__ __forceinline__ float dppm(float x) { return asf(__builtin_amdgcn_mov_dpp(asu(x), CTRL, 0xf, 0xf, true)); }
__device__ __forceinline__ float wsum63(float v) {
  v += dpp0<0x111>(v); v += dpp0<0x112>(v); v += dpp0<0x114>(v); v += dpp0<0x118>(v);
  v += asf(__builtin_amdgcn_update_dpp(0, asu(v), 0x142, 0xa, 0xf, false));
  v += asf(__builtin_amdgcn_update_dpp(0, asu(v), 0x143, 0xc, 0xf, false));
  return v;
}
__device__ __forceinline__ float wsum(float v) { return asf(__builtin_amdgcn_readlane(asu(wsum63(v)), 63)); }
__device__ __forceinline__ float xg_max(float x) {
  auto s = __builtin_amdgcn_permlane16_swap(asu(x), asu(x), false, false);
  x = fmaxf(asf(s[0]), asf(s[1]));
  auto t = __builtin_amdgcn_permlane32_swap(asu(x), asu(x), false, false);
  return fmaxf(asf(t[0]), asf(t[1]));
}
__device__ __forceinline__ float xg_sum(float x) {
  auto s = __builtin_amdgcn_permlane16_swap(asu(x), asu(x), false, false);
  x = asf(s[0]) + asf(s[1]);
  auto t = __builtin_amdgcn_permlane32_swap(asu(x), asu(x), false, false);
  return asf(t[0]) + asf(t[1]);
}
__device__ __forceinline__ f4 mfma32(v4u a, v4u b, f4 c) {
  return __builtin_amdgcn_mfma_f32_16x16x32_bf16(__builtin_bit_cast(b8, a), __builtin_bit_cast(b8, b), c, 0, 0, 0);
}
__device__ __forceinline__ f4 mfma16(s4 a, v2u b, f4 c) {
  return __builtin_amdgcn_mfma_f32_16x16x16bf16_1k(a, __builtin_bit_cast(s4, b), c, 0, 0, 0);
}
__device__ __forceinline__ u32 plo(u32 r1, u32 r0) { return __builtin_amdgcn_perm(r1, r0, 0x05040100u); }
__device__ __forceinline__ u32 phi(u32 r1, u32 r0) { return __builtin_amdgcn_perm(r1, r0, 0x07060302u); }
__device__ __forceinline__ void tr84(const v2u (&r)[8], v4u (&f)[4]) {
#pragma unroll
  for (int t = 0; t < 4; ++t) {
    f[0][t] = plo(r[2 * t + 1].x, r[2 * t].x); f[1][t] = phi(r[2 * t + 1].x, r[2 * t].x);
    f[2][t] = plo(r[2 * t + 1].y, r[2 * t].y); f[3][t] = phi(r[2 * t + 1].y, r[2 * t].y);
  }
}
typedef __amdgpu_buffer_rsrc_t rsrc_t;
__device__ __forceinline__ rsrc_t mkr(const void* p, u32 bytes) { return __builtin_amdgcn_make_buffer_rsrc((void*)p, 0, bytes, 0x00020000); }
__device__ __forceinline__ v4i mkr_s(const void* p, u32 bytes) {
  const u64 a = (u64)p; v4i r;
  r.x = __builtin_amdgcn_readfirstlane((u32)a); r.y = __builtin_amdgcn_readfirstlane((u32)(a >> 32)); r.z = (int)bytes; r.w = 0x00020000;
  return r;
}
constexpr int SC1 = 16, WAUX = FK_WNT ? 2 : 0;
__device__ __forceinline__ v2u ld2(rsrc_t r, u32 vo, u32 so = 0) { return __builtin_amdgcn_raw_buffer_load_b64(r, vo, so, SC1); }
__device__ __forceinline__ v4u ld4(rsrc_t r, u32 vo, u32 so = 0) { return __builtin_amdgcn_raw_buffer_load_b128(r, vo, so, SC1); }
__device__ __forceinline__ void st2(rsrc_t r, u32 vo, v2u v) { __builtin_amdgcn_raw_buffer_store_b64(v, r, vo, 0, SC1); }
__device__ __forceinline__ void st4(rsrc_t r, u32 vo, v4u v) { __builtin_amdgcn_raw_buffer_store_b128(v, r, vo, 0, SC1); }

#define STAMP(i) do {} while (0)
#define FLUSH() do {} while (0)

template <int N> __device__ __forceinline__ void pause() { if (N > 0) asm volatile("s_sleep %0" ::"i"(N) : "memory"); }
__device__ __forceinline__ float rsum16(float v) {
  v += dppm<0x128>(v); v += dppm<0x124>(v); v += dppm<0x122>(v); v += dppm<0x121>(v);
  return v;
}
struct Tag { u32 b[4]; };
__device__ __forceinline__ Tag mktag(u32 t) { return Tag{{t & 0xffu, (t >> 8) & 0xffu, (t >> 16) & 0xffu, t >> 24}}; }
__device__ __forceinline__ u32 enc(float x, u32 tb) { return ((asu(x) + 0x80u) & 0xffffff00u) | tb; }
__device__ __forceinline__ v4u enc4(f4 x, const Tag& t) { return v4u{enc(x[0], t.b[0]), enc(x[1], t.b[1]), enc(x[2], t.b[2]), enc(x[3], t.b[3])}; }
__device__ __forceinline__ float dec(u32 y) { return asf(y & 0xffffff00u); }
__device__ __forceinline__ bool bad2(v2u y, u32 t16) { return (__builtin_amdgcn_perm(y.y, y.x, 0x0c0c0400u)) != t16; }
__device__ __forceinline__ bool bad4(v4u y, u32 t32) {
  return __builtin_amdgcn_perm(__builtin_amdgcn_perm(y.w, y.z, 0x0c0c0400u), __builtin_amdgcn_perm(y.y, y.x, 0x0c0c0400u), 0x05040100u) != t32;
}
__device__ __forceinline__ f2 ln_stats(float piv, float s1, float s2) {
  const float md = s1 * (1.f / DM), e2 = s2 * (1.f / DM);
  const float var = rb(fmaxf(fmaf(-md, md, e2), 0.f));
  return f2{rb(piv + md), __builtin_amdgcn_rcpf(rb(__builtin_amdgcn_sqrtf(rb(var + EPS))))};
}
__device__ __forceinline__ v4u ln_apply(v4u xr, f2 st) {
  v4u o;
#pragma unroll
  for (int i = 0; i < 4; ++i) { const u32 d = pk(blo(xr[i]) - st.x, bhi(xr[i]) - st.x); o[i] = pk(blo(d) * st.y, bhi(d) * st.y); }
  return o;
}
__device__ __forceinline__ v4u layernorm8(v4u xr) {
  const float piv = asf(__builtin_amdgcn_readfirstlane(xr[0] << 16));
  float s1 = 0.f, s2 = 0.f;
#pragma unroll
  for (int i = 0; i < 4; ++i) { const float d0 = blo(xr[i]) - piv, d1 = bhi(xr[i]) - piv; s1 += d0 + d1; s2 = fmaf(d0, d0, fmaf(d1, d1, s2)); }
  s1 = wsum63(s1); s2 = wsum63(s2);
  return ln_apply(xr, ln_stats(piv, asf(__builtin_amdgcn_readlane(asu(s1), 63)), asf(__builtin_amdgcn_readlane(asu(s2), 63))));
}

#define FK_PIECE(T, VO) \
  "s_waitcnt vmcnt(%c[wm])\n\t" \
  ".if %c[pt] == " #T "\n\tds_write_b32 %[fa], %[fv]\n\t.endif\n\t" \
  "buffer_load_dwordx4 " VO ", %[rs], %[so] offen offset:" #T "*1024 nt lds\n\t"
template <int W>
__device__ __forceinline__ void dma_tile(v4i rs, u32 so, u32 la, u32 v0, u32 v1, u32 v2, u32 v3, u32 fa, int tn) {
  const int landed = tn - 1 - (W - 5) / 4;
  asm volatile("s_mov_b32 m0, %[la]\n\t" FK_PIECE(0, "%[v0]") FK_PIECE(1, "%[v1]") FK_PIECE(2, "%[v2]") FK_PIECE(3, "%[v3]")
               :
               : [rs] "s"(rs), [so] "s"(so), [la] "s"(la), [v0] "v"(v0), [v1] "v"(v1), [v2] "v"(v2), [v3] "v"(v3), [fa] "v"(fa),
                 [fv] "v"(landed > 0 ? landed : 0), [wm] "i"(W - 1), [pt] "i"((W - 5) % 4)
               : "memory");
}
__device__ __forceinline__ void dma_adapt(int lag, v4i rs, u32 so, u32 la, u32 v0, u32 v1, u32 v2, u32 v3, u32 fa, int tn) {
  if (lag <= 0) dma_tile<FK_W>(rs, so, la, v0, v1, v2, v3, fa, tn);
  else if (lag <= FK_LG) dma_tile<FK_WB>(rs, so, la, v0, v1, v2, v3, fa, tn);
  else dma_tile<FK_WC>(rs, so, la, v0, v1, v2, v3, fa, tn);
}
__device__ __forceinline__ bool lds_wait_ge(volatile LAS u32* w, u32 need, bool& dead) {
  for (u32 n = 0;; ++n) {
    if ((u32)__builtin_amdgcn_readfirstlane(*w) >= need) break;
    if (dead || n > SPIN_LDS) { dead = true; break; }
    __builtin_amdgcn_s_sleep(1);
  }
  CB();
  return true;
}

__global__ void __launch_bounds__(1024, 4) fused_block(Params p) {
  __shared__ __attribute__((aligned(1024))) unsigned char lds_raw[L_END];
  LAS unsigned char* L = (LAS unsigned char*)lds_raw;
  const int tid = threadIdx.x, lane = tid & 63, wave = __builtin_amdgcn_readfirstlane(tid >> 6), w = wave & 3, role = wave >> 2;
  const int g = lane >> 4, c = lane & 15;
  const int kvh = blockIdx.x & 7, b = blockIdx.x >> 6, s = (blockIdx.x >> 3) & 7, grp = b * 8 + kvh;
  const rsrc_t rw = mkr(p.ws, WS_END);
  volatile LAS u32* FLAG = (volatile LAS u32*)(L + L_FLAG);
  const bool ffn = s < 7;
  const int j = kvh * 7 + s;
  bool dead = false;
  STAMP(0);

  if (role == 1) {
    __builtin_amdgcn_s_setprio(3);
    const u32 tb = (u32)__builtin_amdgcn_s_memrealtime() + FK_T0;
    const v4i rk = mkr_s(p.k + (size_t)grp * SK * 128, SK * 256), rv = mkr_s(p.v + (size_t)grp * SK * 128, SK * 256);
    const u32 k0 = g * 256 + ((c ^ g) << 4), k1 = g * 256 + ((c ^ (4 + g)) << 4), k2 = g * 256 + ((c ^ (8 + g)) << 4), k3 = g * 256 + ((c ^ (12 + g)) << 4);
    const u32 u0 = g * 256 + ((c ^ (g << 1)) << 4), u1 = g * 256 + ((c ^ ((4 + g) << 1)) << 4);
    const u32 ring = (u32)(size_t)(L + L_RING) + w * 32768, fa = (u32)(size_t)(FLAG + w);
    STAMP(5);
    if (lane < 4) FLAG[w + 4 * lane] = 0u;
    wg_barrier();
    STAMP(7);
    dma_tile<FK_W>(rk, (u32)(s * 4 + w) * 4096u, ring, k0, k1, k2, k3, fa, 0);
    STAMP(6);
    dma_tile<FK_W>(rv, (u32)(s * 4 + w) * 4096u, ring + 4096, u0, u1, u0, u1, fa, 1);
    STAMP(8);
#pragma unroll 1
    for (int i = 1; i < NCH; ++i) {
      const u32 so = (u32)((i * 8 + s) * 4 + w) * 4096u, la = ring + (i & 3) * 8192;
      if (i >= 4) lds_wait_ge(FLAG + 4 + w, i - 3, dead);
      const int lag = (int)((u32)__builtin_amdgcn_s_memrealtime() - tb) - ((i * FK_TAU) >> 4);
      dma_adapt(lag, rk, so, la, k0, k1, k2, k3, fa, 2 * i);
      dma_adapt(lag, rv, so, la + 4096, u0, u1, u0, u1, fa, 2 * i + 1);
    }
    asm volatile("s_waitcnt vmcnt(8)\n\tds_write_b32 %0, %1\n\ts_waitcnt vmcnt(4)\n\tds_write_b32 %0, %2\n\ts_waitcnt vmcnt(0)\n\tds_write_b32 %0, %3" ::"v"(fa),
                 "v"(2 * NCH - 2), "v"(2 * NCH - 1), "v"(2 * NCH) : "memory");
    STAMP(1);
    wg_barrier();
    wg_barrier();
    wg_barrier();
    wg_barrier();
    if (w == 0) {
      const int jj = kvh * 8 + s;
      const u32 off = lane < 56 ? WS_D + (u32)(b * 56 + lane) * 2048 + jj * 32 : OOB;
      const v4u hv = *(const LAS v4u*)(L + L_HS + jj * 16);
      const u32 tg = lane < 56 ? FLAG[12] : 0u;
      pause<FK_D3>();
      STAMP(2);
      v4u d0 = ld4(rw, off), d1 = ld4(rw, off + 16);
      for (u32 n = 0; __any(bad4(d0, tg) || bad4(d1, tg)); ++n) {
        if (dead || n > SPIN_MEM) { dead = true; break; }
        CB();
        if (bad4(d0, tg) || bad4(d1, tg)) { d0 = ld4(rw, off); d1 = ld4(rw, off + 16); }
      }
      STAMP(3);
      v4u ov;
#pragma unroll
      for (int i = 0; i < 2; ++i) {
        const u32 a = pk(wsum63(dec(d0[2 * i])), wsum63(dec(d0[2 * i + 1]))), bq = pk(wsum63(dec(d1[2 * i])), wsum63(dec(d1[2 * i + 1])));
        ov[i] = pk(blo(hv[i]) + blo(a), bhi(hv[i]) + bhi(a));
        ov[2 + i] = pk(blo(hv[2 + i]) + blo(bq), bhi(hv[2 + i]) + bhi(bq));
      }
      if (lane == 63) *(GAS v4u*)(p.out + b * DM + jj * 8) = ov;
      STAMP(4);
    }
    FLUSH();
    return;
  }

  if (role == 2) {
    LAS unsigned char* priv = L + L_PRIV + w * 1024;
    wg_barrier();
    pause<FK_W0>(); pause<FK_W0>();
    v2u wor[4][8]; v4u dr[8];
    const rsrc_t ro = mkr(p.wo, NQ * DM * 2), rd = mkr(p.wd, FF * DM * 2);
#pragma unroll
    for (int u = 0; u < 4; ++u)
#pragma unroll
      for (int r = 0; r < 8; ++r) {
        wor[u][r] = __builtin_amdgcn_raw_buffer_load_b64(ro, g * 8 * 1024 + 8 * c, (kvh * 512 + 128 * w + 32 * u + r) * 1024 + 128 * s, WAUX);
        pause<FK_P1>();
      }
#pragma unroll
    for (int r = 0; r < 8; ++r) {
      dr[r] = __builtin_amdgcn_raw_buffer_load_b128(rd, ffn ? (8 * g) * 1024 + 16 * c : OOB, (32 * j + r) * 1024 + 256 * w, WAUX);
      pause<FK_P1>();
    }
    v4u wf[4][4], df[8];
#pragma unroll
    for (int u = 0; u < 4; ++u) { tr84(wor[u], wf[u]); asm volatile("" : "+v"(wf[u][0]), "+v"(wf[u][1]), "+v"(wf[u][2]), "+v"(wf[u][3])); }
#pragma unroll
    for (int t = 0; t < 4; ++t)
#pragma unroll
      for (int q = 0; q < 4; ++q) { df[2 * q][t] = plo(dr[2 * t + 1][q], dr[2 * t][q]); df[2 * q + 1][t] = phi(dr[2 * t + 1][q], dr[2 * t][q]); }
    asm volatile("" : "+v"(df[0]), "+v"(df[1]), "+v"(df[2]), "+v"(df[3]), "+v"(df[4]), "+v"(df[5]), "+v"(df[6]), "+v"(df[7]));
    lds_wait_ge(FLAG + 9, 4, dead);
    const u32 tg = FLAG[12], t16 = (lane & 1) ? tg >> 16 : tg & 0xffffu;
    const Tag T = mktag(tg);
    STAMP(1);
    wg_barrier();
    {
      const u32 oo = WS_O + grp * 8 * 2048 + w * 512 + lane * 8, om = WS_ML + grp * 512 + (lane & 7) * 64 + w * 16;
      pause<FK_D1>();
      STAMP(2);
      v2u o[8]; v4u ml;
#pragma unroll
      for (int k = 0; k < 8; ++k) o[k] = ld2(rw, oo, k * 2048);
      ml = ld4(rw, om);
      for (u32 n = 0;; ++n) {
        bool bad = bad4(ml, tg);
#pragma unroll
        for (int k = 0; k < 8; ++k) bad = bad || bad2(o[k], t16);
        if (!__any(bad)) break;
        if (dead || n > SPIN_MEM) { dead = true; break; }
        CB();
#pragma unroll
        for (int k = 0; k < 8; ++k) if (__any(bad2(o[k], t16))) o[k] = ld2(rw, oo, k * 2048);
        if (__any(bad4(ml, tg))) ml = ld4(rw, om);
      }
      STAMP(3);
      float M = dec(ml.x);
      M = fmaxf(M, dppm<0xB1>(M)); M = fmaxf(M, dppm<0x4E>(M)); M = fmaxf(M, dppm<0x141>(M));
      const float e = __builtin_amdgcn_exp2f((dec(ml.x) - M) * L2E);
      float Ls = e * dec(ml.y);
      Ls += dppm<0xB1>(Ls); Ls += dppm<0x4E>(Ls); Ls += dppm<0x141>(Ls);
      float o0 = 0.f, o1 = 0.f;
#pragma unroll
      for (int k = 0; k < 8; ++k) { const float ek = asf(__builtin_amdgcn_readlane(asu(e), k)); o0 = fmaf(ek, dec(o[k].x), o0); o1 = fmaf(ek, dec(o[k].y), o1); }
      const float inv = __builtin_amdgcn_rcpf(Ls);
      ((LAS u32*)priv)[lane] = pk(o0 * inv, o1 * inv);
      f4 y[4];
#pragma unroll
      for (int i = 0; i < 4; ++i) y[i] = f4{0.f, 0.f, 0.f, 0.f};
#pragma unroll
      for (int u = 0; u < 4; ++u) {
        const v4u a = *(const LAS v4u*)(priv + (32 * u + 8 * g) * 2);
#pragma unroll
        for (int i = 0; i < 4; ++i) y[i] = mfma32(a, wf[u][i], y[i]);
      }
      if (lane < 16) *(LAS f4*)(L + L_YRED + w * 256 + c * 16) = f4{y[0][0], y[1][0], y[2][0], y[3][0]};
    }
    wg_barrier();
    {
      const LAS f4* yr = (const LAS f4*)(L + L_YRED) + c;
      const f4 y = (yr[0] + yr[16]) + (yr[32] + yr[48]);
      st4(rw, lane < 32 ? WS_Y + (u32)(2 * w + g) * 65536 + grp * 2048 + s * 256 + c * 16 : OOB, enc4(y, T));
    }
    STAMP(4);
    wg_barrier();
    wg_barrier();
    STAMP(5);
    if (ffn) {
      const LAS float* gu = (const LAS float*)(L + L_GU) + (lane & 31);
      const float gt = rb((gu[0] + gu[64]) + (gu[128] + gu[192])), up = rb((gu[32] + gu[96]) + (gu[160] + gu[224]));
      const float sg = rb(gt * __builtin_amdgcn_rcpf(1.f + __builtin_amdgcn_exp2f(-gt * L2E)));
      ((LAS u16*)priv)[lane & 31] = (u16)pk(sg * up, 0.f);
      const v4u a = *(const LAS v4u*)(priv + 16 * g);
      f4 o[8];
#pragma unroll
      for (int i = 0; i < 8; ++i) o[i] = mfma32(a, df[i], f4{0.f, 0.f, 0.f, 0.f});
      const bool hi = g & 1;
      const v4u ov = enc4(f4{hi ? o[4][0] : o[0][0], hi ? o[5][0] : o[1][0], hi ? o[6][0] : o[2][0], hi ? o[7][0] : o[3][0]}, T);
      st4(rw, lane < 32 ? WS_D + (u32)(b * 56 + j) * 2048 + (u32)(128 * w + 8 * c + 4 * g) * 4 : OOB, ov);
    }
    STAMP(6);
    FLUSH();
    return;
  }

  if (role == 3) {
    LAS unsigned char* priv = L + L_PRIV + 4096 + w * 1024;
    const int t = tid - 768;
    wg_barrier();
    pause<FK_W0>(); pause<FK_W0>();
    const u32 xp = *(const GAS u32*)(p.x + b * DM + 2 * t);
    v2u gr[4][8];
    v4u gf[4][4];
    if (ffn) {
      const GAS unsigned char* gp = (const GAS unsigned char*)(c < 8 ? p.wg : p.wu) + (size_t)(128 * w + 8 * g) * (FF * 2) + 64 * j + 8 * (c & 7);
#pragma unroll
      for (int u = 0; u < 4; ++u)
#pragma unroll
        for (int r = 0; r < 8; ++r) {
          const GAS v2u* a = (const GAS v2u*)(gp + (size_t)(32 * u + r) * (FF * 2));
          gr[u][r] = FK_WNT ? __builtin_nontemporal_load(a) : *a;
          pause<FK_P2>();
        }
#pragma unroll
      for (int u = 0; u < 4; ++u) { tr84(gr[u], gf[u]); asm volatile("" : "+v"(gf[u][0]), "+v"(gf[u][1]), "+v"(gf[u][2]), "+v"(gf[u][3])); }
    }
    lds_wait_ge(FLAG + 9, 4, dead);
    const u32 tg = FLAG[12];
    if (blockIdx.x == 0) {
      const u32 tlo = FLAG[10];
      for (u32 n = 0;; ++n) {
        const v2u gv = ld2(rw, WS_QG + (u32)(lane >> 3) * 16384 + w * 4096 + (lane & 7) * 512);
        if (__all(gv.y == tlo)) break;
        if (dead || n > SPIN_MEM) { dead = true; break; }
        pause<8>();
      }
    }
    STAMP(1);
    wg_barrier();
    wg_barrier();
    {
      const u32 off = WS_Y + (u32)kvh * 65536 + (u32)b * 8 * 2048 + (u32)t * 8, t16 = (lane & 1) ? tg >> 16 : tg & 0xffffu;
      pause<FK_D2>();
      STAMP(2);
      v2u y[8];
#pragma unroll
      for (int k = 0; k < 8; ++k) y[k] = ld2(rw, off, k * 2048);
      for (u32 n = 0;; ++n) {
        bool bad = false;
#pragma unroll
        for (int k = 0; k < 8; ++k) bad = bad || bad2(y[k], t16);
        if (!__any(bad)) break;
        if (dead || n > SPIN_MEM) { dead = true; break; }
        CB();
#pragma unroll
        for (int k = 0; k < 8; ++k) if (__any(bad2(y[k], t16))) y[k] = ld2(rw, off, k * 2048);
      }
      STAMP(3);
      const u32 at = pk(((dec(y[0].x) + dec(y[1].x)) + (dec(y[2].x) + dec(y[3].x))) + ((dec(y[4].x) + dec(y[5].x)) + (dec(y[6].x) + dec(y[7].x))),
                        ((dec(y[0].y) + dec(y[1].y)) + (dec(y[2].y) + dec(y[3].y))) + ((dec(y[4].y) + dec(y[5].y)) + (dec(y[6].y) + dec(y[7].y))));
      ((LAS u32*)(L + L_HS))[t] = pk(blo(xp) + blo(at), bhi(xp) + bhi(at));
    }
    wg_barrier();
    STAMP(4);
    if (ffn) {
      const v4u hn = layernorm8(*(const LAS v4u*)(L + L_HS + lane * 16));
      *(LAS v4u*)(priv + lane * 16) = hn;
      f4 acc[4];
#pragma unroll
      for (int i = 0; i < 4; ++i) acc[i] = f4{0.f, 0.f, 0.f, 0.f};
#pragma unroll
      for (int u = 0; u < 4; ++u) {
        const v4u a = *(const LAS v4u*)(priv + (128 * w + 32 * u + 8 * g) * 2);
#pragma unroll
        for (int i = 0; i < 4; ++i) acc[i] = mfma32(a, gf[u][i], acc[i]);
      }
      if (lane < 16) *(LAS f4*)(L + L_GU + w * 256 + c * 16) = f4{acc[0][0], acc[1][0], acc[2][0], acc[3][0]};
    }
    STAMP(5);
    wg_barrier();
    if (blockIdx.x == 0 && w == 0) st2(rw, lane == 0 ? WS_E : OOB, v2u{FLAG[10], FLAG[11]});
    FLUSH();
    return;
  }

  v4u qf[4];
  u32 tg;
  {
    STAMP(6);
    v4u x4[4];
#pragma unroll
    for (int t = 0; t < 4; ++t) x4[t] = *(const GAS v4u*)(p.x + g * DM + 128 * t + 8 * c);
    const v4u xa = *(const GAS v4u*)(p.x + (c & 3) * DM + 128 * b + 32 * w + 8 * g);
    const rsrc_t rq = mkr(p.wq, DM * NQ * 2);
    v2u wr[8];
#pragma unroll
    for (int r = 0; r < 8; ++r)
      wr[r] = __builtin_amdgcn_raw_buffer_load_b64(rq, g * 8 * 8192 + 8 * c, (128 * b + 32 * w + r) * 8192 + (kvh * 512 + 64 * s) * 2, 2);
    const v2u e = ld2(rw, WS_E);
    STAMP(7);
    wg_barrier();
    STAMP(10);
    const float piv = dppm<0x150>(blo(x4[0][0]));
    float s1 = 0.f, s2 = 0.f;
#pragma unroll
    for (int t = 0; t < 4; ++t)
#pragma unroll
      for (int i = 0; i < 4; ++i) { const float d0 = blo(x4[t][i]) - piv, d1 = bhi(x4[t][i]) - piv; s1 += d0 + d1; s2 = fmaf(d0, d0, fmaf(d1, d1, s2)); }
    const f2 st = ln_stats(piv, rsum16(s1), rsum16(s2));
    const f2 sa = {asf(__builtin_amdgcn_ds_bpermute((c & 3) * 64, asu(st.x))), asf(__builtin_amdgcn_ds_bpermute((c & 3) * 64, asu(st.y)))};
    const v4u a = ln_apply(xa, sa);
    v4u f[4];
    tr84(wr, f);
    f4 acc[4];
#pragma unroll
    for (int i = 0; i < 4; ++i) acc[i] = mfma32(a, f[i], f4{0.f, 0.f, 0.f, 0.f});
    if (lane < 16) {
#pragma unroll
      for (int r = 0; r < 4; ++r) *(LAS f4*)(L + L_QRED + w * 1024 + r * 256 + c * 16) = f4{acc[0][r], acc[1][r], acc[2][r], acc[3][r]};
    }
    const u64 epoch = ((u64)__builtin_amdgcn_readfirstlane(e.y) << 32 | __builtin_amdgcn_readfirstlane(e.x)) + 1;
    const u32 tlo = (u32)epoch, thi = (u32)(epoch >> 32) + tlo * 0x9E3779B1u;
    const u32 dl = tlo - (u32)(epoch >> 32);
    tg = ((tlo + 1u) & 0xffu) | (((tlo >> 8) + 3u * dl + 1u) & 0xffu) << 8 | (((tlo >> 16) + 5u * dl + 1u) & 0xffu) << 16 | ((tlo >> 24) + 7u * dl + 1u) << 24;
    if (w == 0 && lane == 0) { FLAG[10] = tlo; FLAG[11] = (u32)(epoch >> 32); FLAG[12] = tg; }
    asm volatile("s_waitcnt lgkmcnt(0)" ::: "memory");
    STAMP(11);
    u32 old = 0;
    if (lane == 0) old = __hip_atomic_fetch_add((LAS u32*)(L + L_FLAG) + 8, 1u, __ATOMIC_RELAXED, __HIP_MEMORY_SCOPE_WORKGROUP);
    old = __builtin_amdgcn_readfirstlane(old);
    CB();
    if (old == 3) {
      const LAS f2* qr = (const LAS f2*)(L + L_QRED) + g * 32 + c;
      const f2 q0 = (qr[0] + qr[128]) + (qr[256] + qr[384]), q1 = (qr[16] + qr[144]) + (qr[272] + qr[400]);
      const u32 off = WS_QG + (u32)(g * 8 + kvh) * 16384 + b * 4096 + s * 512 + c * 16;
      st4(rw, off, v4u{asu(q0.x), tlo, asu(q0.y), thi});
      st4(rw, off + 256, v4u{asu(q1.x), tlo, asu(q1.y), thi});
    }
    STAMP(1);
    pause<FK_D0>();
    v4u gq[4];
    const u32 qo = WS_QG + grp * 16384 + w * 1024 + lane * 16;
#pragma unroll
    for (int t = 0; t < 4; ++t) gq[t] = ld4(rw, qo, t * 4096);
    for (u32 n = 0;; ++n) {
      bool bad = false;
#pragma unroll
      for (int t = 0; t < 4; ++t) bad = bad || gq[t].y != tlo || gq[t].w != thi;
      if (!__any(bad)) break;
      if (dead || n > SPIN_MEM) { dead = true; break; }
      pause<2>();
#pragma unroll
      for (int t = 0; t < 4; ++t) if (__any(gq[t].y != tlo || gq[t].w != thi)) gq[t] = ld4(rw, qo, t * 4096);
    }
    STAMP(12);
    ((LAS u32*)(L + L_QS))[64 * w + lane] = pk((asf(gq[0].x) + asf(gq[1].x)) + (asf(gq[2].x) + asf(gq[3].x)), (asf(gq[0].z) + asf(gq[1].z)) + (asf(gq[2].z) + asf(gq[3].z)));
    asm volatile("s_waitcnt lgkmcnt(0)" ::: "memory");
    if (lane == 0) __hip_atomic_fetch_add((LAS u32*)(L + L_FLAG) + 9, 1u, __ATOMIC_RELAXED, __HIP_MEMORY_SCOPE_WORKGROUP);
    lds_wait_ge(FLAG + 9, 4, dead);
#pragma unroll
    for (int u = 0; u < 4; ++u) qf[u] = *(const LAS v4u*)(L + L_QS + ((c & 3) * 128 + 32 * u + 8 * g) * 2);
    STAMP(2);
  }
  float m = -INFINITY, l = 0.f;
  f4 acc[8];
#pragma unroll
  for (int i = 0; i < 8; ++i) acc[i] = f4{0.f, 0.f, 0.f, 0.f};
  const LAS unsigned char* ringk = L + L_RING + w * 32768 + c * 256;
  const u32 vrow = 4 * g + (c >> 2);
  const u32 va = (u32)(size_t)(L + L_RING) + w * 32768 + 4096 + vrow * 256 + (vrow & 7) * 32 + 8 * (c & 3);
  u32 seen = 0;
  auto need = [&](u32 n) {
    for (u32 k = 0; seen < n; ++k) {
      seen = __builtin_amdgcn_readfirstlane(FLAG[w]);
      if (seen >= n) break;
      if (dead || k > SPIN_LDS) { dead = true; break; }
      __builtin_amdgcn_s_sleep(1);
    }
    CB();
  };
#pragma unroll 1
  for (int i = 0; i < NCH; ++i) {
    const u32 sl = (i & 3) * 8192;
    need(2 * i + 1);
    v4u kf[4];
#pragma unroll
    for (int u = 0; u < 4; ++u) kf[u] = *(const LAS v4u*)(ringk + sl + (((4 * u + g) ^ c) << 4));
    f4 s0 = mfma32(kf[0], qf[0], f4{0.f, 0.f, 0.f, 0.f}), s1 = mfma32(kf[2], qf[2], f4{0.f, 0.f, 0.f, 0.f});
    s0 = mfma32(kf[1], qf[1], s0); s1 = mfma32(kf[3], qf[3], s1);
    const u32 a01 = pk(s0[0] + s1[0], s0[1] + s1[1]), a23 = pk(s0[2] + s1[2], s0[3] + s1[3]);
    const u32 b01 = pk(blo(a01) * SCALE, bhi(a01) * SCALE), b23 = pk(blo(a23) * SCALE, bhi(a23) * SCALE);
    const float t0 = blo(b01), t1 = bhi(b01), t2 = blo(b23), t3 = bhi(b23);
    const float mx = xg_max(fmaxf(fmaxf(t0, t1), fmaxf(t2, t3)));
    if (__any(mx > m)) {
      const float mn = fmaxf(m, mx), al = __builtin_amdgcn_exp2f((m - mn) * L2E);
      l *= al; m = mn;
#pragma unroll
      for (int d = 0; d < 8; ++d) acc[d] *= al;
    }
    const float p0 = __builtin_amdgcn_exp2f((t0 - m) * L2E), p1 = __builtin_amdgcn_exp2f((t1 - m) * L2E),
                p2 = __builtin_amdgcn_exp2f((t2 - m) * L2E), p3 = __builtin_amdgcn_exp2f((t3 - m) * L2E);
    l += (p0 + p1) + (p2 + p3);
    const v2u pf = {pk(p0, p1), pk(p2, p3)};
    if (i == NCH - 1) {
      const float lt = xg_sum(l);
      if (lane < 4) *(LAS f2*)(L + L_MLS + w * 32 + c * 8) = f2{m, lt};
    }
    need(2 * i + 2);
    s4 vf[8];
#pragma unroll
    for (int d = 0; d < 8; ++d) vf[d] = __builtin_amdgcn_ds_read_tr16_b64_v4i16((LAS s4*)(size_t)((va ^ (d << 5)) + sl));
    asm volatile("s_waitcnt lgkmcnt(0)" ::: "memory");
    FLAG[4 + w] = i + 1;
#pragma unroll
    for (int d = 0; d < 8; ++d) acc[d] = mfma16(vf[d], pf, acc[d]);
  }
  STAMP(4);
  if (c < 4) {
#pragma unroll
    for (int d = 0; d < 8; ++d) *(LAS f4*)(L + L_OM + w * 2048 + c * 512 + (16 * d + 4 * g) * 4) = acc[d];
  }
  wg_barrier();
  {
    f2 ml[4]; f4 ov[4];
#pragma unroll
    for (int k = 0; k < 4; ++k) { ml[k] = *(const LAS f2*)(L + L_MLS + k * 32 + w * 8); ov[k] = *(const LAS f4*)(L + L_OM + k * 2048 + w * 512 + (lane & 31) * 16); }
    const float M = fmaxf(fmaxf(ml[0].x, ml[1].x), fmaxf(ml[2].x, ml[3].x));
    float Ls = 0.f; f4 O = {0.f, 0.f, 0.f, 0.f};
#pragma unroll
    for (int k = 0; k < 4; ++k) { const float e = __builtin_amdgcn_exp2f((ml[k].x - M) * L2E); Ls = fmaf(e, ml[k].y, Ls); O += ov[k] * e; }
    const u32 wgi = grp * 8 + s;
    const Tag T = mktag(tg);
    st4(rw, lane < 32 ? WS_O + wgi * 2048 + w * 512 + lane * 16 : (lane == 32 ? WS_ML + wgi * 64 + w * 16 : OOB), enc4(lane < 32 ? O : f4{M, Ls, M, Ls}, T));
  }
  STAMP(5);
  wg_barrier();
  wg_barrier();
  wg_barrier();
  FLUSH();
}
}

extern "C" void kernel_launch(void* const* d_in, const int* in_sizes, int n_in, void* d_out, int out_size, void* d_ws, size_t ws_size,
                              hipStream_t stream) {
  fk::Params p;
  p.x = (const fk::u16*)d_in[0]; p.k = (const fk::u16*)d_in[1]; p.v = (const fk::u16*)d_in[2];
  p.wq = (const fk::u16*)d_in[3]; p.wo = (const fk::u16*)d_in[4];
  p.wg = (const fk::u16*)d_in[6]; p.wu = (const fk::u16*)d_in[7]; p.wd = (const fk::u16*)d_in[8];
  p.out = (fk::u16*)d_out; p.ws = (unsigned char*)d_ws;
  hipLaunchKernelGGL(fk::fused_block, dim3(256), dim3(1024), 0, stream, p);
}
```

```cpp
#include <hip/hip_runtime.h>
#include <stdint.h>

#define FK_W 5
#define FK_WNT 1
#define FK_W0 120
#define FK_P1 18
#define FK_P2 22
#define FK_ORD 1
#define FK_G0 0
#define FK_WB 7
#define FK_WC 10
#define FK_LG 60
#define FK_T0 80
#define FK_TAU 1870
#define FK_DIAG 0
#define FK_DIAGB 0
#define FK_DIAGK 1
#define FK_D0 40
#define FK_D1 12
#define FK_D2 10
#define FK_D3 14
#define FK_STAMPS 0

namespace fk {
#define GAS __attribute__((address_space(1)))
#define LAS __attribute__((address_space(3)))
typedef unsigned u32;
typedef unsigned long long u64;
typedef unsigned short u16;
typedef u32 v2u __attribute__((ext_vector_type(2)));
typedef u32 v4u __attribute__((ext_vector_type(4)));
typedef int v4i __attribute__((ext_vector_type(4)));
typedef float f2 __attribute__((ext_vector_type(2)));
typedef float f4 __attribute__((ext_vector_type(4)));
typedef short s4 __attribute__((ext_vector_type(4)));
typedef __bf16 b2 __attribute__((ext_vector_type(2)));
typedef __bf16 b8 __attribute__((ext_vector_type(8)));

constexpr int DM = 512, SK = 16384, FF = 1792, NQ = 4096;
constexpr int NCH = 32;
constexpr float L2E = 1.4426950408889634f, SCALE = 0.08838834764831845f, EPS = 1e-5f;
constexpr u32 WS_E = 0, WS_C = 1024  , WS_QG = 4096, WS_O = WS_QG + 32 * 16384, WS_ML = WS_O + 256 * 2048, WS_Y = WS_ML + 256 * 64,
              WS_D = WS_Y + 8 * 65536, WS_END = WS_D + 4 * 56 * 2048, WS_DBG = WS_END;
constexpr u32 OOB = 0x80000000u;
constexpr u32 L_RING = 0, L_FLAG = 131072  , L_QRED = L_FLAG + 256  , L_QS = L_QRED + 4096,
              L_PRIV = L_QS + 1024  , L_OM = L_PRIV + 8192, L_MLS = L_OM + 8192, L_YRED = L_MLS + 128, L_HS = L_YRED + 1024,
              L_GU = L_HS + 1024, L_END = L_GU + 1024;
constexpr u32 SPIN_LDS = 1u << 19, SPIN_MEM = 1u << 16;

struct Params {
  const u16 *x, *k, *v, *wq, *wo, *wg, *wu, *wd;
  u16* out;
  unsigned char* ws;
};

#define CB() asm volatile("" ::: "memory")
__device__ __forceinline__ void wg_barrier() {
  asm volatile("s_waitcnt lgkmcnt(0)\n\ts_barrier" ::: "memory");
  __builtin_amdgcn_sched_barrier(0);
}
__device__ __forceinline__ float asf(u32 x) { return __builtin_bit_cast(float, x); }
__device__ __forceinline__ u32 asu(float x) { return __builtin_bit_cast(u32, x); }
__device__ __forceinline__ u32 pk(float lo, float hi) { f2 z = {lo, hi}; return __builtin_bit_cast(u32, __builtin_convertvector(z, b2)); }
__device__ __forceinline__ float blo(u32 p) { return asf(p << 16); }
__device__ __forceinline__ float bhi(u32 p) { return asf(p & 0xffff0000u); }
__device__ __forceinline__ float rb(float x) { return blo(pk(x, x)); }
template <int CTRL, int RM = 0xf> __device__ __forceinline__ float dpp0(float x) {
  return asf(__builtin_amdgcn_update_dpp(0, asu(x), CTRL, RM, 0xf, true));
}
template <int CTRL> __device__ __forceinline__ float dppm(float x) { return asf(__builtin_amdgcn_mov_dpp(asu(x), CTRL, 0xf, 0xf, true)); }
__device__ __forceinline__ float wsum63(float v) {
  v += dpp0<0x111>(v); v += dpp0<0x112>(v); v += dpp0<0x114>(v); v += dpp0<0x118>(v);
  v += asf(__builtin_amdgcn_update_dpp(0, asu(v), 0x142, 0xa, 0xf, false));
  v += asf(__builtin_amdgcn_update_dpp(0, asu(v), 0x143, 0xc, 0xf, false));
  return v;
}
__device__ __forceinline__ float wsum(float v) { return asf(__builtin_amdgcn_readlane(asu(wsum63(v)), 63)); }
__device__ __forceinline__ float xg_max(float x) {
  auto s = __builtin_amdgcn_permlane16_swap(asu(x), asu(x), false, false);
  x = fmaxf(asf(s[0]), asf(s[1]));
  auto t = __builtin_amdgcn_permlane32_swap(asu(x), asu(x), false, false);
  return fmaxf(asf(t[0]), asf(t[1]));
}
__device__ __forceinline__ float xg_sum(float x) {
  auto s = __builtin_amdgcn_permlane16_swap(asu(x), asu(x), false, false);
  x = asf(s[0]) + asf(s[1]);
  auto t = __builtin_amdgcn_permlane32_swap(asu(x), asu(x), false, false);
  return asf(t[0]) + asf(t[1]);
}
__device__ __forceinline__ f4 mfma32(v4u a, v4u b, f4 c) {
  return __builtin_amdgcn_mfma_f32_16x16x32_bf16(__builtin_bit_cast(b8, a), __builtin_bit_cast(b8, b), c, 0, 0, 0);
}
__device__ __forceinline__ f4 mfma16(s4 a, v2u b, f4 c) {
  return __builtin_amdgcn_mfma_f32_16x16x16bf16_1k(a, __builtin_bit_cast(s4, b), c, 0, 0, 0);
}
__device__ __forceinline__ u32 plo(u32 r1, u32 r0) { return __builtin_amdgcn_perm(r1, r0, 0x05040100u); }
__device__ __forceinline__ u32 phi(u32 r1, u32 r0) { return __builtin_amdgcn_perm(r1, r0, 0x07060302u); }
__device__ __forceinline__ void tr84(const v2u (&r)[8], v4u (&f)[4]) {
#pragma unroll
  for (int t = 0; t < 4; ++t) {
    f[0][t] = plo(r[2 * t + 1].x, r[2 * t].x); f[1][t] = phi(r[2 * t + 1].x, r[2 * t].x);
    f[2][t] = plo(r[2 * t + 1].y, r[2 * t].y); f[3][t] = phi(r[2 * t + 1].y, r[2 * t].y);
  }
}
typedef __amdgpu_buffer_rsrc_t rsrc_t;
__device__ __forceinline__ rsrc_t mkr(const void* p, u32 bytes) { return __builtin_amdgcn_make_buffer_rsrc((void*)p, 0, bytes, 0x00020000); }
__device__ __forceinline__ v4i mkr_s(const void* p, u32 bytes) {
  const u64 a = (u64)p; v4i r;
  r.x = __builtin_amdgcn_readfirstlane((u32)a); r.y = __builtin_amdgcn_readfirstlane((u32)(a >> 32)); r.z = (int)bytes; r.w = 0x00020000;
  return r;
}
constexpr int SC1 = 16, WAUX = FK_WNT ? 2 : 0;
__device__ __forceinline__ v2u ld2(rsrc_t r, u32 vo, u32 so = 0) { return __builtin_amdgcn_raw_buffer_load_b64(r, vo, so, SC1); }
__device__ __forceinline__ v4u ld4(rsrc_t r, u32 vo, u32 so = 0) { return __builtin_amdgcn_raw_buffer_load_b128(r, vo, so, SC1); }
__device__ __forceinline__ void st2(rsrc_t r, u32 vo, v2u v) { __builtin_amdgcn_raw_buffer_store_b64(v, r, vo, 0, SC1); }
__device__ __forceinline__ void st4(rsrc_t r, u32 vo, v4u v) { __builtin_amdgcn_raw_buffer_store_b128(v, r, vo, 0, SC1); }

#define STAMP(i) do {} while (0)
#define FLUSH() do {} while (0)

template <int N> __device__ __forceinline__ void pause() { if (N > 0) asm volatile("s_sleep %0" ::"i"(N) : "memory"); }
__device__ __forceinline__ float rsum16(float v) {
  v += dppm<0x128>(v); v += dppm<0x124>(v); v += dppm<0x122>(v); v += dppm<0x121>(v);
  return v;
}
struct Tag { u32 b[4]; };
__device__ __forceinline__ Tag mktag(u32 t) { return Tag{{t & 0xffu, (t >> 8) & 0xffu, (t >> 16) & 0xffu, t >> 24}}; }
__device__ __forceinline__ u32 enc(float x, u32 tb) { return ((asu(x) + 0x80u) & 0xffffff00u) | tb; }
__device__ __forceinline__ v4u enc4(f4 x, const Tag& t) { return v4u{enc(x[0], t.b[0]), enc(x[1], t.b[1]), enc(x[2], t.b[2]), enc(x[3], t.b[3])}; }
__device__ __forceinline__ float dec(u32 y) { return asf(y & 0xffffff00u); }
__device__ __forceinline__ bool bad2(v2u y, u32 t16) { return (__builtin_amdgcn_perm(y.y, y.x, 0x0c0c0400u)) != t16; }
__device__ __forceinline__ bool bad4(v4u y, u32 t32) {
  return __builtin_amdgcn_perm(__builtin_amdgcn_perm(y.w, y.z, 0x0c0c0400u), __builtin_amdgcn_perm(y.y, y.x, 0x0c0c0400u), 0x05040100u) != t32;
}
__device__ __forceinline__ f2 ln_stats(float piv, float s1, float s2) {
  const float md = s1 * (1.f / DM), e2 = s2 * (1.f / DM);
  const float var = rb(fmaxf(fmaf(-md, md, e2), 0.f));
  return f2{rb(piv + md), __builtin_amdgcn_rcpf(rb(__builtin_amdgcn_sqrtf(rb(var + EPS))))};
}
__device__ __forceinline__ v4u ln_apply(v4u xr, f2 st) {
  v4u o;
#pragma unroll
  for (int i = 0; i < 4; ++i) { const u32 d = pk(blo(xr[i]) - st.x, bhi(xr[i]) - st.x); o[i] = pk(blo(d) * st.y, bhi(d) * st.y); }
  return o;
}
__device__ __forceinline__ v4u layernorm8(v4u xr) {
  const float piv = asf(__builtin_amdgcn_readfirstlane(xr[0] << 16));
  float s1 = 0.f, s2 = 0.f;
#pragma unroll
  for (int i = 0; i < 4; ++i) { const float d0 = blo(xr[i]) - piv, d1 = bhi(xr[i]) - piv; s1 += d0 + d1; s2 = fmaf(d0, d0, fmaf(d1, d1, s2)); }
  s1 = wsum63(s1); s2 = wsum63(s2);
  return ln_apply(xr, ln_stats(piv, asf(__builtin_amdgcn_readlane(asu(s1), 63)), asf(__builtin_amdgcn_readlane(asu(s2), 63))));
}

#define FK_PIECE(T, VO) \
  "s_waitcnt vmcnt(%c[wm])\n\t" \
  ".if %c[pt] == " #T "\n\tds_write_b32 %[fa], %[fv]\n\t.endif\n\t" \
  "buffer_load_dwordx4 " VO ", %[rs], %[so] offen offset:" #T "*1024 nt lds\n\t"
template <int W>
__device__ __forceinline__ void dma_tile(v4i rs, u32 so, u32 la, u32 v0, u32 v1, u32 v2, u32 v3, u32 fa, int tn) {
  const int landed = tn - 1 - (W - 5) / 4;
  asm volatile("s_mov_b32 m0, %[la]\n\t" FK_PIECE(0, "%[v0]") FK_PIECE(1, "%[v1]") FK_PIECE(2, "%[v2]") FK_PIECE(3, "%[v3]")
               :
               : [rs] "s"(rs), [so] "s"(so), [la] "s"(la), [v0] "v"(v0), [v1] "v"(v1), [v2] "v"(v2), [v3] "v"(v3), [fa] "v"(fa),
                 [fv] "v"(landed > 0 ? landed : 0), [wm] "i"(W - 1), [pt] "i"((W - 5) % 4)
               : "memory");
}
__device__ __forceinline__ void dma_adapt(int lag, v4i rs, u32 so, u32 la, u32 v0, u32 v1, u32 v2, u32 v3, u32 fa, int tn) {
  if (lag <= 0) dma_tile<FK_W>(rs, so, la, v0, v1, v2, v3, fa, tn);
  else if (lag <= FK_LG) dma_tile<FK_WB>(rs, so, la, v0, v1, v2, v3, fa, tn);
  else dma_tile<FK_WC>(rs, so, la, v0, v1, v2, v3, fa, tn);
}
__device__ __forceinline__ bool lds_wait_ge(volatile LAS u32* w, u32 need, bool& dead) {
  for (u32 n = 0;; ++n) {
    if ((u32)__builtin_amdgcn_readfirstlane(*w) >= need) break;
    if (dead || n > SPIN_LDS) { dead = true; break; }
    __builtin_amdgcn_s_sleep(1);
  }
  CB();
  return true;
}

__global__ void __launch_bounds__(1024, 4) fused_block(Params p) {
  __shared__ __attribute__((aligned(1024))) unsigned char lds_raw[L_END];
  LAS unsigned char* L = (LAS unsigned char*)lds_raw;
  const int tid = threadIdx.x, lane = tid & 63, wave = __builtin_amdgcn_readfirstlane(tid >> 6), w = wave & 3, role = wave >> 2;
  const int g = lane >> 4, c = lane & 15;
  const int kvh = blockIdx.x & 7, b = blockIdx.x >> 6, s = (blockIdx.x >> 3) & 7, grp = b * 8 + kvh;
  const rsrc_t rw = mkr(p.ws, WS_END);
  volatile LAS u32* FLAG = (volatile LAS u32*)(L + L_FLAG);
  const bool ffn = s < 7;
  const int j = kvh * 7 + s;
  bool dead = false;
  STAMP(0);

  if (role == 1) {
    __builtin_amdgcn_s_setprio(3);
    const u32 tb = (u32)__builtin_amdgcn_s_memrealtime() + FK_T0;
#define DIAG(n) do {} while (0)
    const v4i rk = mkr_s(p.k + (size_t)grp * SK * 128, SK * 256), rv = mkr_s(p.v + (size_t)grp * SK * 128, SK * 256);
    const u32 k0 = g * 256 + ((c ^ g) << 4), k1 = g * 256 + ((c ^ (4 + g)) << 4), k2 = g * 256 + ((c ^ (8 + g)) << 4), k3 = g * 256 + ((c ^ (12 + g)) << 4);
    const u32 u0 = g * 256 + ((c ^ (g << 1)) << 4), u1 = g * 256 + ((c ^ ((4 + g) << 1)) << 4);
    const u32 ring = (u32)(size_t)(L + L_RING) + w * 32768, fa = (u32)(size_t)(FLAG + w);
    STAMP(5);
    if (lane < 4) FLAG[w + 4 * lane] = 0u;
    wg_barrier();
    STAMP(7);
    dma_tile<FK_W>(rk, (u32)(s * 4 + w) * 4096u, ring, k0, k1, k2, k3, fa, 0);
    STAMP(6);
    dma_tile<FK_W>(rv, (u32)(s * 4 + w) * 4096u, ring + 4096, u0, u1, u0, u1, fa, 1);
    STAMP(8);
    DIAG(1);
#pragma unroll 1
    for (int i = 1; i < NCH; ++i) {
      const u32 so = (u32)((i * 8 + s) * 4 + w) * 4096u, la = ring + (i & 3) * 8192;
      if (i >= 4) lds_wait_ge(FLAG + 4 + w, i - 3, dead);
      const int lag = (int)((u32)__builtin_amdgcn_s_memrealtime() - tb) - ((i * FK_TAU) >> 4);
      dma_adapt(lag, rk, so, la, k0, k1, k2, k3, fa, 2 * i);
      dma_adapt(lag, rv, so, la + 4096, u0, u1, u0, u1, fa, 2 * i + 1);
    }
    asm volatile("s_waitcnt vmcnt(8)\n\tds_write_b32 %0, %1\n\ts_waitcnt vmcnt(4)\n\tds_write_b32 %0, %2\n\ts_waitcnt vmcnt(0)\n\tds_write_b32 %0, %3" ::"v"(fa),
                 "v"(2 * NCH - 2), "v"(2 * NCH - 1), "v"(2 * NCH) : "memory");
    STAMP(1);
    DIAG(2);
    wg_barrier();
    wg_barrier();
    wg_barrier();
    wg_barrier();
    if (w == 0) {
      const int jj = kvh * 8 + s;
      const u32 off = lane < 56 ? WS_D + (u32)(b * 56 + lane) * 2048 + jj * 32 : OOB;
      const v4u hv = *(const LAS v4u*)(L + L_HS + jj * 16);
      const u32 tg = lane < 56 ? FLAG[12] : 0u;
      pause<FK_D3>();
      STAMP(2);
      v4u d0 = ld4(rw, off), d1 = ld4(rw, off + 16);
      for (u32 n = 0; __any(bad4(d0, tg) || bad4(d1, tg)); ++n) {
        if (dead || n > SPIN_MEM) { dead = true; break; }
        CB();
        if (bad4(d0, tg) || bad4(d1, tg)) { d0 = ld4(rw, off); d1 = ld4(rw, off + 16); }
      }
      STAMP(3);
      v4u ov;
#pragma unroll
      for (int i = 0; i < 2; ++i) {
        const u32 a = pk(wsum63(dec(d0[2 * i])), wsum63(dec(d0[2 * i + 1]))), bq = pk(wsum63(dec(d1[2 * i])), wsum63(dec(d1[2 * i + 1])));
        ov[i] = pk(blo(hv[i]) + blo(a), bhi(hv[i]) + bhi(a));
        ov[2 + i] = pk(blo(hv[2 + i]) + blo(bq), bhi(hv[2 + i]) + bhi(bq));
      }
      if (lane == 63) *(GAS v4u*)(p.out + b * DM + jj * 8) = ov;
      STAMP(4);
    }
    FLUSH();
    return;
  }

  if (role == 2) {
    LAS unsigned char* priv = L + L_PRIV + w * 1024;
    wg_barrier();
    pause<FK_W0>(); pause<FK_W0>();
    v2u wor[4][8]; v4u dr[8];
    const rsrc_t ro = mkr(p.wo, NQ * DM * 2), rd = mkr(p.wd, FF * DM * 2);
#pragma unroll
    for (int u = 0; u < 4; ++u)
#pragma unroll
      for (int r = 0; r < 8; ++r) {
        wor[u][r] = __builtin_amdgcn_raw_buffer_load_b64(ro, g * 8 * 1024 + 8 * c, (kvh * 512 + 128 * w + 32 * u + r) * 1024 + 128 * s, WAUX);
        pause<FK_P1>();
      }
#pragma unroll
    for (int r = 0; r < 8; ++r) {
      dr[r] = __builtin_amdgcn_raw_buffer_load_b128(rd, ffn ? (8 * g) * 1024 + 16 * c : OOB, (32 * j + r) * 1024 + 256 * w, WAUX);
      pause<FK_P1>();
    }
    v4u wf[4][4], df[8];
#pragma unroll
    for (int u = 0; u < 4; ++u) { tr84(wor[u], wf[u]); asm volatile("" : "+v"(wf[u][0]), "+v"(wf[u][1]), "+v"(wf[u][2]), "+v"(wf[u][3])); }
#pragma unroll
    for (int t = 0; t < 4; ++t)
#pragma unroll
      for (int q = 0; q < 4; ++q) { df[2 * q][t] = plo(dr[2 * t + 1][q], dr[2 * t][q]); df[2 * q + 1][t] = phi(dr[2 * t + 1][q], dr[2 * t][q]); }
    asm volatile("" : "+v"(df[0]), "+v"(df[1]), "+v"(df[2]), "+v"(df[3]), "+v"(df[4]), "+v"(df[5]), "+v"(df[6]), "+v"(df[7]));
    lds_wait_ge(FLAG + 9, 4, dead);
    const u32 tg = FLAG[12], t16 = (lane & 1) ? tg >> 16 : tg & 0xffffu;
    const Tag T = mktag(tg);
    STAMP(1);
    wg_barrier();
    {
      const u32 oo = WS_O + grp * 8 * 2048 + w * 512 + lane * 8, om = WS_ML + grp * 512 + (lane & 7) * 64 + w * 16;
      pause<FK_D1>();
      STAMP(2);
      v2u o[8]; v4u ml;
#pragma unroll
      for (int k = 0; k < 8; ++k) o[k] = ld2(rw, oo, k * 2048);
      ml = ld4(rw, om);
      for (u32 n = 0;; ++n) {
        bool bad = bad4(ml, tg);
#pragma unroll
        for (int k = 0; k < 8; ++k) bad = bad || bad2(o[k], t16);
        if (!__any(bad)) break;
        if (dead || n > SPIN_MEM) { dead = true; break; }
        CB();
#pragma unroll
        for (int k = 0; k < 8; ++k) if (__any(bad2(o[k], t16))) o[k] = ld2(rw, oo, k * 2048);
        if (__any(bad4(ml, tg))) ml = ld4(rw, om);
      }
      STAMP(3);
      float M = dec(ml.x);
      M = fmaxf(M, dppm<0xB1>(M)); M = fmaxf(M, dppm<0x4E>(M)); M = fmaxf(M, dppm<0x141>(M));
      const float e = __builtin_amdgcn_exp2f((dec(ml.x) - M) * L2E);
      float Ls = e * dec(ml.y);
      Ls += dppm<0xB1>(Ls); Ls += dppm<0x4E>(Ls); Ls += dppm<0x141>(Ls);
      float o0 = 0.f, o1 = 0.f;
#pragma unroll
      for (int k = 0; k < 8; ++k) { const float ek = asf(__builtin_amdgcn_readlane(asu(e), k)); o0 = fmaf(ek, dec(o[k].x), o0); o1 = fmaf(ek, dec(o[k].y), o1); }
      const float inv = __builtin_amdgcn_rcpf(Ls);
      ((LAS u32*)priv)[lane] = pk(o0 * inv, o1 * inv);
      f4 y[4];
#pragma unroll
      for (int i = 0; i < 4; ++i) y[i] = f4{0.f, 0.f, 0.f, 0.f};
#pragma unroll
      for (int u = 0; u < 4; ++u) {
        const v4u a = *(const LAS v4u*)(priv + (32 * u + 8 * g) * 2);
#pragma unroll
        for (int i = 0; i < 4; ++i) y[i] = mfma32(a, wf[u][i], y[i]);
      }
      if (lane < 16) *(LAS f4*)(L + L_YRED + w * 256 + c * 16) = f4{y[0][0], y[1][0], y[2][0], y[3][0]};
    }
    wg_barrier();
    {
      const LAS f4* yr = (const LAS f4*)(L + L_YRED) + c;
      const f4 y = (yr[0] + yr[16]) + (yr[32] + yr[48]);
      st4(rw, lane < 32 ? WS_Y + (u32)(2 * w + g) * 65536 + grp * 2048 + s * 256 + c * 16 : OOB, enc4(y, T));
    }
    STAMP(4);
    wg_barrier();
    wg_barrier();
    STAMP(5);
    if (ffn) {
      const LAS float* gu = (const LAS float*)(L + L_GU) + (lane & 31);
      const float gt = rb((gu[0] + gu[64]) + (gu[128] + gu[192])), up = rb((gu[32] + gu[96]) + (gu[160] + gu[224]));
      const float sg = rb(gt * __builtin_amdgcn_rcpf(1.f + __builtin_amdgcn_exp2f(-gt * L2E)));
      ((LAS u16*)priv)[lane & 31] = (u16)pk(sg * up, 0.f);
      const v4u a = *(const LAS v4u*)(priv + 16 * g);
      f4 o[8];
#pragma unroll
      for (int i = 0; i < 8; ++i) o[i] = mfma32(a, df[i], f4{0.f, 0.f, 0.f, 0.f});
      const bool hi = g & 1;
      const v4u ov = enc4(f4{hi ? o[4][0] : o[0][0], hi ? o[5][0] : o[1][0], hi ? o[6][0] : o[2][0], hi ? o[7][0] : o[3][0]}, T);
      st4(rw, lane < 32 ? WS_D + (u32)(b * 56 + j) * 2048 + (u32)(128 * w + 8 * c + 4 * g) * 4 : OOB, ov);
    }
    STAMP(6);
    FLUSH();
    return;
  }

  if (role == 3) {
    LAS unsigned char* priv = L + L_PRIV + 4096 + w * 1024;
    const int t = tid - 768;
    wg_barrier();
    pause<FK_W0>(); pause<FK_W0>();
    const u32 xp = *(const GAS u32*)(p.x + b * DM + 2 * t);
    v2u gr[4][8];
    v4u gf[4][4];
    if (ffn) {
      const GAS unsigned char* gp = (const GAS unsigned char*)(c < 8 ? p.wg : p.wu) + (size_t)(128 * w + 8 * g) * (FF * 2) + 64 * j + 8 * (c & 7);
#pragma unroll
      for (int u = 0; u < 4; ++u)
#pragma unroll
        for (int r = 0; r < 8; ++r) {
          const GAS v2u* a = (const GAS v2u*)(gp + (size_t)(32 * u + r) * (FF * 2));
          gr[u][r] = FK_WNT ? __builtin_nontemporal_load(a) : *a;
          pause<FK_P2>();
        }
#pragma unroll
      for (int u = 0; u < 4; ++u) { tr84(gr[u], gf[u]); asm volatile("" : "+v"(gf[u][0]), "+v"(gf[u][1]), "+v"(gf[u][2]), "+v"(gf[u][3])); }
    }
    lds_wait_ge(FLAG + 9, 4, dead);
    const u32 tg = FLAG[12];
    if (blockIdx.x == 0) {
      const u32 tlo = FLAG[10];
      for (u32 n = 0;; ++n) {
        const v2u gv = ld2(rw, WS_QG + (u32)(lane >> 3) * 16384 + w * 4096 + (lane & 7) * 512);
        if (__all(gv.y == tlo)) break;
        if (dead || n > SPIN_MEM) { dead = true; break; }
        pause<8>();
      }
    }
    STAMP(1);
    DIAG(2);
    wg_barrier();
    wg_barrier();
    {
      const u32 off = WS_Y + (u32)kvh * 65536 + (u32)b * 8 * 2048 + (u32)t * 8, t16 = (lane & 1) ? tg >> 16 : tg & 0xffffu;
      pause<FK_D2>();
      STAMP(2);
      v2u y[8];
#pragma unroll
      for (int k = 0; k < 8; ++k) y[k] = ld2(rw, off, k * 2048);
      for (u32 n = 0;; ++n) {
        bool bad = false;
#pragma unroll
        for (int k = 0; k < 8; ++k) bad = bad || bad2(y[k], t16);
        if (!__any(bad)) break;
        if (dead || n > SPIN_MEM) { dead = true; break; }
        CB();
#pragma unroll
        for (int k = 0; k < 8; ++k) if (__any(bad2(y[k], t16))) y[k] = ld2(rw, off, k * 2048);
      }
      STAMP(3);
      const u32 at = pk(((dec(y[0].x) + dec(y[1].x)) + (dec(y[2].x) + dec(y[3].x))) + ((dec(y[4].x) + dec(y[5].x)) + (dec(y[6].x) + dec(y[7].x))),
                        ((dec(y[0].y) + dec(y[1].y)) + (dec(y[2].y) + dec(y[3].y))) + ((dec(y[4].y) + dec(y[5].y)) + (dec(y[6].y) + dec(y[7].y))));
      ((LAS u32*)(L + L_HS))[t] = pk(blo(xp) + blo(at), bhi(xp) + bhi(at));
    }
    wg_barrier();
    STAMP(4);
    if (ffn) {
      const v4u hn = layernorm8(*(const LAS v4u*)(L + L_HS + lane * 16));
      *(LAS v4u*)(priv + lane * 16) = hn;
      f4 acc[4];
#pragma unroll
      for (int i = 0; i < 4; ++i) acc[i] = f4{0.f, 0.f, 0.f, 0.f};
#pragma unroll
      for (int u = 0; u < 4; ++u) {
        const v4u a = *(const LAS v4u*)(priv + (128 * w + 32 * u + 8 * g) * 2);
#pragma unroll
        for (int i = 0; i < 4; ++i) acc[i] = mfma32(a, gf[u][i], acc[i]);
      }
      if (lane < 16) *(LAS f4*)(L + L_GU + w * 256 + c * 16) = f4{acc[0][0], acc[1][0], acc[2][0], acc[3][0]};
    }
    STAMP(5);
    wg_barrier();
    if (blockIdx.x == 0 && w == 0) st2(rw, lane == 0 ? WS_E : OOB, v2u{FLAG[10], FLAG[11]});
    FLUSH();
    return;
  }

  v4u qf[4];
  u32 tg;
  {
    STAMP(6);
    v4u x4[4];
#pragma unroll
    for (int t = 0; t < 4; ++t) x4[t] = *(const GAS v4u*)(p.x + g * DM + 128 * t + 8 * c);
    const v4u xa = *(const GAS v4u*)(p.x + (c & 3) * DM + 128 * b + 32 * w + 8 * g);
    const rsrc_t rq = mkr(p.wq, DM * NQ * 2);
    v2u wr[8];
#pragma unroll
    for (int r = 0; r < 8; ++r)
      wr[r] = __builtin_amdgcn_raw_buffer_load_b64(rq, g * 8 * 8192 + 8 * c, (128 * b + 32 * w + r) * 8192 + (kvh * 512 + 64 * s) * 2, 2);
    const v2u e = ld2(rw, WS_E);
    STAMP(7);
    wg_barrier();
    STAMP(10);
    const float piv = dppm<0x150>(blo(x4[0][0]));
    float s1 = 0.f, s2 = 0.f;
#pragma unroll
    for (int t = 0; t < 4; ++t)
#pragma unroll
      for (int i = 0; i < 4; ++i) { const float d0 = blo(x4[t][i]) - piv, d1 = bhi(x4[t][i]) - piv; s1 += d0 + d1; s2 = fmaf(d0, d0, fmaf(d1, d1, s2)); }
    const f2 st = ln_stats(piv, rsum16(s1), rsum16(s2));
    const f2 sa = {asf(__builtin_amdgcn_ds_bpermute((c & 3) * 64, asu(st.x))), asf(__builtin_amdgcn_ds_bpermute((c & 3) * 64, asu(st.y)))};
    const v4u a = ln_apply(xa, sa);
    v4u f[4];
    tr84(wr, f);
    f4 acc[4];
#pragma unroll
    for (int i = 0; i < 4; ++i) acc[i] = mfma32(a, f[i], f4{0.f, 0.f, 0.f, 0.f});
    if (lane < 16) {
#pragma unroll
      for (int r = 0; r < 4; ++r) *(LAS f4*)(L + L_QRED + w * 1024 + r * 256 + c * 16) = f4{acc[0][r], acc[1][r], acc[2][r], acc[3][r]};
    }
    const u64 epoch = ((u64)__builtin_amdgcn_readfirstlane(e.y) << 32 | __builtin_amdgcn_readfirstlane(e.x)) + 1;
    const u32 tlo = (u32)epoch, thi = (u32)(epoch >> 32) + tlo * 0x9E3779B1u;
    const u32 dl = tlo - (u32)(epoch >> 32);
    tg = ((tlo + 1u) & 0xffu) | (((tlo >> 8) + 3u * dl + 1u) & 0xffu) << 8 | (((tlo >> 16) + 5u * dl + 1u) & 0xffu) << 16 | ((tlo >> 24) + 7u * dl + 1u) << 24;
    if (w == 0 && lane == 0) { FLAG[10] = tlo; FLAG[11] = (u32)(epoch >> 32); FLAG[12] = tg; }
    asm volatile("s_waitcnt lgkmcnt(0)" ::: "memory");
    STAMP(11);
    u32 old = 0;
    if (lane == 0) old = __hip_atomic_fetch_add((LAS u32*)(L + L_FLAG) + 8, 1u, __ATOMIC_RELAXED, __HIP_MEMORY_SCOPE_WORKGROUP);
    old = __builtin_amdgcn_readfirstlane(old);
    CB();
    if (old == 3) {
      const LAS f2* qr = (const LAS f2*)(L + L_QRED) + g * 32 + c;
      const f2 q0 = (qr[0] + qr[128]) + (qr[256] + qr[384]), q1 = (qr[16] + qr[144]) + (qr[272] + qr[400]);
      const u32 off = WS_QG + (u32)(g * 8 + kvh) * 16384 + b * 4096 + s * 512 + c * 16;
      st4(rw, off, v4u{asu(q0.x), tlo, asu(q0.y), thi});
      st4(rw, off + 256, v4u{asu(q1.x), tlo, asu(q1.y), thi});
    }
    STAMP(1);
    pause<FK_D0>();
    v4u gq[4];
    const u32 qo = WS_QG + grp * 16384 + w * 1024 + lane * 16;
#pragma unroll
    for (int t = 0; t < 4; ++t) gq[t] = ld4(rw, qo, t * 4096);
    for (u32 n = 0;; ++n) {
      bool bad = false;
#pragma unroll
      for (int t = 0; t < 4; ++t) bad = bad || gq[t].y != tlo || gq[t].w != thi;
      if (!__any(bad)) break;
      if (dead || n > SPIN_MEM) { dead = true; break; }
      pause<2>();
#pragma unroll
      for (int t = 0; t < 4; ++t) if (__any(gq[t].y != tlo || gq[t].w != thi)) gq[t] = ld4(rw, qo, t * 4096);
    }
    STAMP(12);
    ((LAS u32*)(L + L_QS))[64 * w + lane] = pk((asf(gq[0].x) + asf(gq[1].x)) + (asf(gq[2].x) + asf(gq[3].x)), (asf(gq[0].z) + asf(gq[1].z)) + (asf(gq[2].z) + asf(gq[3].z)));
    asm volatile("s_waitcnt lgkmcnt(0)" ::: "memory");
    if (lane == 0) __hip_atomic_fetch_add((LAS u32*)(L + L_FLAG) + 9, 1u, __ATOMIC_RELAXED, __HIP_MEMORY_SCOPE_WORKGROUP);
    lds_wait_ge(FLAG + 9, 4, dead);
#pragma unroll
    for (int u = 0; u < 4; ++u) qf[u] = *(const LAS v4u*)(L + L_QS + ((c & 3) * 128 + 32 * u + 8 * g) * 2);
    STAMP(2);
  }
  float m = -INFINITY, l = 0.f;
  f4 acc[8];
#pragma unroll
  for (int i = 0; i < 8; ++i) acc[i] = f4{0.f, 0.f, 0.f, 0.f};
  const LAS unsigned char* ringk = L + L_RING + w * 32768 + c * 256;
  const u32 vrow = 4 * g + (c >> 2);
  const u32 va = (u32)(size_t)(L + L_RING) + w * 32768 + 4096 + vrow * 256 + (vrow & 7) * 32 + 8 * (c & 3);
  u32 seen = 0;
  auto need = [&](u32 n) {
    for (u32 k = 0; seen < n; ++k) {
      seen = __builtin_amdgcn_readfirstlane(FLAG[w]);
      if (seen >= n) break;
      if (dead || k > SPIN_LDS) { dead = true; break; }
      __builtin_amdgcn_s_sleep(1);
    }
    CB();
  };
#pragma unroll 1
  for (int i = 0; i < NCH; ++i) {
    const u32 sl = (i & 3) * 8192;
    need(2 * i + 1);
    v4u kf[4];
#pragma unroll
    for (int u = 0; u < 4; ++u) kf[u] = *(const LAS v4u*)(ringk + sl + (((4 * u + g) ^ c) << 4));
    f4 s0 = mfma32(kf[0], qf[0], f4{0.f, 0.f, 0.f, 0.f}), s1 = mfma32(kf[2], qf[2], f4{0.f, 0.f, 0.f, 0.f});
    s0 = mfma32(kf[1], qf[1], s0); s1 = mfma32(kf[3], qf[3], s1);
    const u32 a01 = pk(s0[0] + s1[0], s0[1] + s1[1]), a23 = pk(s0[2] + s1[2], s0[3] + s1[3]);
    const u32 b01 = pk(blo(a01) * SCALE, bhi(a01) * SCALE), b23 = pk(blo(a23) * SCALE, bhi(a23) * SCALE);
    const float t0 = blo(b01), t1 = bhi(b01), t2 = blo(b23), t3 = bhi(b23);
    const float mx = xg_max(fmaxf(fmaxf(t0, t1), fmaxf(t2, t3)));
    if (__any(mx > m)) {
      const float mn = fmaxf(m, mx), al = __builtin_amdgcn_exp2f((m - mn) * L2E);
      l *= al; m = mn;
#pragma unroll
      for (int d = 0; d < 8; ++d) acc[d] *= al;
    }
    const float p0 = __builtin_amdgcn_exp2f((t0 - m) * L2E), p1 = __builtin_amdgcn_exp2f((t1 - m) * L2E),
                p2 = __builtin_amdgcn_exp2f((t2 - m) * L2E), p3 = __builtin_amdgcn_exp2f((t3 - m) * L2E);
    l += (p0 + p1) + (p2 + p3);
    const v2u pf = {pk(p0, p1), pk(p2, p3)};
    if (i == NCH - 1) {
      const float lt = xg_sum(l);
      if (lane < 4) *(LAS f2*)(L + L_MLS + w * 32 + c * 8) = f2{m, lt};
    }
    need(2 * i + 2);
    s4 vf[8];
#pragma unroll
    for (int d = 0; d < 8; ++d) vf[d] = __builtin_amdgcn_ds_read_tr16_b64_v4i16((LAS s4*)(size_t)((va ^ (d << 5)) + sl));
    asm volatile("s_waitcnt lgkmcnt(0)" ::: "memory");
    FLAG[4 + w] = i + 1;
#pragma unroll
    for (int d = 0; d < 8; ++d) acc[d] = mfma16(vf[d], pf, acc[d]);
  }
  STAMP(4);
  if (c < 4) {
#pragma unroll
    for (int d = 0; d < 8; ++d) *(LAS f4*)(L + L_OM + w * 2048 + c * 512 + (16 * d + 4 * g) * 4) = acc[d];
  }
  wg_barrier();
  {
    f2 ml[4]; f4 ov[4];
#pragma unroll
    for (int k = 0; k < 4; ++k) { ml[k] = *(const LAS f2*)(L + L_MLS + k * 32 + w * 8); ov[k] = *(const LAS f4*)(L + L_OM + k * 2048 + w * 512 + (lane & 31) * 16); }
    const float M = fmaxf(fmaxf(ml[0].x, ml[1].x), fmaxf(ml[2].x, ml[3].x));
    float Ls = 0.f; f4 O = {0.f, 0.f, 0.f, 0.f};
#pragma unroll
    for (int k = 0; k < 4; ++k) { const float e = __builtin_amdgcn_exp2f((ml[k].x - M) * L2E); Ls = fmaf(e, ml[k].y, Ls); O += ov[k] * e; }
    const u32 wgi = grp * 8 + s;
    const Tag T = mktag(tg);
    st4(rw, lane < 32 ? WS_O + wgi * 2048 + w * 512 + lane * 16 : (lane == 32 ? WS_ML + wgi * 64 + w * 16 : OOB), enc4(lane < 32 ? O : f4{M, Ls, M, Ls}, T));
  }
  STAMP(5);
  wg_barrier();
  wg_barrier();
  wg_barrier();
  FLUSH();
}
}

extern "C" void kernel_launch(void* const* d_in, const int* in_sizes, int n_in, void* d_out, int out_size, void* d_ws, size_t ws_size,
                              hipStream_t stream) {
  fk::Params p;
  p.x = (const fk::u16*)d_in[0]; p.k = (const fk::u16*)d_in[1]; p.v = (const fk::u16*)d_in[2];
  p.wq = (const fk::u16*)d_in[3]; p.wo = (const fk::u16*)d_in[4];
  p.wg = (const fk::u16*)d_in[6]; p.wu = (const fk::u16*)d_in[7]; p.wd = (const fk::u16*)d_in[8];
  p.out = (fk::u16*)d_out; p.ws = (unsigned char*)d_ws;
  hipLaunchKernelGGL(fk::fused_block, dim3(256), dim3(1024), 0, stream, p);
}
```

```cpp
#include <hip/hip_runtime.h>
#include <stdint.h>

#define FK_W 5
#define FK_WNT 1
#define FK_W0 120
#define FK_P1 18
#define FK_P2 22
#define FK_ORD 1
#define FK_G0 0
#define FK_WB 7
#define FK_WC 10
#define FK_LG 60
#define FK_T0 80
#define FK_TAU 1870
#define FK_DIAG 1
#define FK_DIAGB 0
#define FK_DIAGK 10
#define FK_D0 40
#define FK_D1 12
#define FK_D2 10
#define FK_D3 14
#define FK_STAMPS 0

namespace fk {
#define GAS __attribute__((address_space(1)))
#define LAS __attribute__((address_space(3)))
typedef unsigned u32;
typedef unsigned long long u64;
typedef unsigned short u16;
typedef u32 v2u __attribute__((ext_vector_type(2)));
typedef u32 v4u __attribute__((ext_vector_type(4)));
typedef int v4i __attribute__((ext_vector_type(4)));
typedef float f2 __attribute__((ext_vector_type(2)));
typedef float f4 __attribute__((ext_vector_type(4)));
typedef short s4 __attribute__((ext_vector_type(4)));
typedef __bf16 b2 __attribute__((ext_vector_type(2)));
typedef __bf16 b8 __attribute__((ext_vector_type(8)));

constexpr int DM = 512, SK = 16384, FF = 1792, NQ = 4096;
constexpr int NCH = 32;
constexpr float L2E = 1.4426950408889634f, SCALE = 0.08838834764831845f, EPS = 1e-5f;
constexpr u32 WS_E = 0, WS_C = 1024  , WS_QG = 4096, WS_O = WS_QG + 32 * 16384, WS_ML = WS_O + 256 * 2048, WS_Y = WS_ML + 256 * 64,
              WS_D = WS_Y + 8 * 65536, WS_END = WS_D + 4 * 56 * 2048, WS_DBG = WS_END;
constexpr u32 OOB = 0x80000000u;
constexpr u32 L_RING = 0, L_FLAG = 131072  , L_QRED = L_FLAG + 256  , L_QS = L_QRED + 4096,
              L_PRIV = L_QS + 1024  , L_OM = L_PRIV + 8192, L_MLS = L_OM + 8192, L_YRED = L_MLS + 128, L_HS = L_YRED + 1024,
              L_GU = L_HS + 1024, L_END = L_GU + 1024;
constexpr u32 SPIN_LDS = 1u << 19, SPIN_MEM = 1u << 16;

struct Params {
  const u16 *x, *k, *v, *wq, *wo, *wg, *wu, *wd;
  u16* out;
  unsigned char* ws;
};

#define CB() asm volatile("" ::: "memory")
__device__ __forceinline__ void wg_barrier() {
  asm volatile("s_waitcnt lgkmcnt(0)\n\ts_barrier" ::: "memory");
  __builtin_amdgcn_sched_barrier(0);
}
__device__ __forceinline__ float asf(u32 x) { return __builtin_bit_cast(float, x); }
__device__ __forceinline__ u32 asu(float x) { return __builtin_bit_cast(u32, x); }
__device__ __forceinline__ u32 pk(float lo, float hi) { f2 z = {lo, hi}; return __builtin_bit_cast(u32, __builtin_convertvector(z, b2)); }
__device__ __forceinline__ float blo(u32 p) { return asf(p << 16); }
__device__ __forceinline__ float bhi(u32 p) { return asf(p & 0xffff0000u); }
__device__ __forceinline__ float rb(float x) { return blo(pk(x, x)); }
template <int CTRL, int RM = 0xf> __device__ __forceinline__ float dpp0(float x) {
  return asf(__builtin_amdgcn_update_dpp(0, asu(x), CTRL, RM, 0xf, true));
}
template <int CTRL> __device__ __forceinline__ float dppm(float x) { return asf(__builtin_amdgcn_mov_dpp(asu(x), CTRL, 0xf, 0xf, true)); }
__device__ __forceinline__ float wsum63(float v) {
  v += dpp0<0x111>(v); v += dpp0<0x112>(v); v += dpp0<0x114>(v); v += dpp0<0x118>(v);
  v += asf(__builtin_amdgcn_update_dpp(0, asu(v), 0x142, 0xa, 0xf, false));
  v += asf(__builtin_amdgcn_update_dpp(0, asu(v), 0x143, 0xc, 0xf, false));
  return v;
}
__device__ __forceinline__ float wsum(float v) { return asf(__builtin_amdgcn_readlane(asu(wsum63(v)), 63)); }
__device__ __forceinline__ float xg_max(float x) {
  auto s = __builtin_amdgcn_permlane16_swap(asu(x), asu(x), false, false);
  x = fmaxf(asf(s[0]), asf(s[1]));
  auto t = __builtin_amdgcn_permlane32_swap(asu(x), asu(x), false, false);
  return fmaxf(asf(t[0]), asf(t[1]));
}
__device__ __forceinline__ float xg_sum(float x) {
  auto s = __builtin_amdgcn_permlane16_swap(asu(x), asu(x), false, false);
  x = asf(s[0]) + asf(s[1]);
  auto t = __builtin_amdgcn_permlane32_swap(asu(x), asu(x), false, false);
  return asf(t[0]) + asf(t[1]);
}
__device__ __forceinline__ f4 mfma32(v4u a, v4u b, f4 c) {
  return __builtin_amdgcn_mfma_f32_16x16x32_bf16(__builtin_bit_cast(b8, a), __builtin_bit_cast(b8, b), c, 0, 0, 0);
}
__device__ __forceinline__ f4 mfma16(s4 a, v2u b, f4 c) {
  return __builtin_amdgcn_mfma_f32_16x16x16bf16_1k(a, __builtin_bit_cast(s4, b), c, 0, 0, 0);
}
__device__ __forceinline__ u32 plo(u32 r1, u32 r0) { return __builtin_amdgcn_perm(r1, r0, 0x05040100u); }
__device__ __forceinline__ u32 phi(u32 r1, u32 r0) { return __builtin_amdgcn_perm(r1, r0, 0x07060302u); }
__device__ __forceinline__ void tr84(const v2u (&r)[8], v4u (&f)[4]) {
#pragma unroll
  for (int t = 0; t < 4; ++t) {
    f[0][t] = plo(r[2 * t + 1].x, r[2 * t].x); f[1][t] = phi(r[2 * t + 1].x, r[2 * t].x);
    f[2][t] = plo(r[2 * t + 1].y, r[2 * t].y); f[3][t] = phi(r[2 * t + 1].y, r[2 * t].y);
  }
}
typedef __amdgpu_buffer_rsrc_t rsrc_t;
__device__ __forceinline__ rsrc_t mkr(const void* p, u32 bytes) { return __builtin_amdgcn_make_buffer_rsrc((void*)p, 0, bytes, 0x00020000); }
__device__ __forceinline__ v4i mkr_s(const void* p, u32 bytes) {
  const u64 a = (u64)p; v4i r;
  r.x = __builtin_amdgcn_readfirstlane((u32)a); r.y = __builtin_amdgcn_readfirstlane((u32)(a >> 32)); r.z = (int)bytes; r.w = 0x00020000;
  return r;
}
constexpr int SC1 = 16, WAUX = FK_WNT ? 2 : 0;
__device__ __forceinline__ v2u ld2(rsrc_t r, u32 vo, u32 so = 0) { return __builtin_amdgcn_raw_buffer_load_b64(r, vo, so, SC1); }
__device__ __forceinline__ v4u ld4(rsrc_t r, u32 vo, u32 so = 0) { return __builtin_amdgcn_raw_buffer_load_b128(r, vo, so, SC1); }
__device__ __forceinline__ void st2(rsrc_t r, u32 vo, v2u v) { __builtin_amdgcn_raw_buffer_store_b64(v, r, vo, 0, SC1); }
__device__ __forceinline__ void st4(rsrc_t r, u32 vo, v4u v) { __builtin_amdgcn_raw_buffer_store_b128(v, r, vo, 0, SC1); }

#define STAMP(i) do {} while (0)
#define FLUSH() do {} while (0)

template <int N> __device__ __forceinline__ void pause() { if (N > 0) asm volatile("s_sleep %0" ::"i"(N) : "memory"); }
__device__ __forceinline__ float rsum16(float v) {
  v += dppm<0x128>(v); v += dppm<0x124>(v); v += dppm<0x122>(v); v += dppm<0x121>(v);
  return v;
}
struct Tag { u32 b[4]; };
__device__ __forceinline__ Tag mktag(u32 t) { return Tag{{t & 0xffu, (t >> 8) & 0xffu, (t >> 16) & 0xffu, t >> 24}}; }
__device__ __forceinline__ u32 enc(float x, u32 tb) { return ((asu(x) + 0x80u) & 0xffffff00u) | tb; }
__device__ __forceinline__ v4u enc4(f4 x, const Tag& t) { return v4u{enc(x[0], t.b[0]), enc(x[1], t.b[1]), enc(x[2], t.b[2]), enc(x[3], t.b[3])}; }
__device__ __forceinline__ float dec(u32 y) { return asf(y & 0xffffff00u); }
__device__ __forceinline__ bool bad2(v2u y, u32 t16) { return (__builtin_amdgcn_perm(y.y, y.x, 0x0c0c0400u)) != t16; }
__device__ __forceinline__ bool bad4(v4u y, u32 t32) {
  return __builtin_amdgcn_perm(__builtin_amdgcn_perm(y.w, y.z, 0x0c0c0400u), __builtin_amdgcn_perm(y.y, y.x, 0x0c0c0400u), 0x05040100u) != t32;
}
__device__ __forceinline__ f2 ln_stats(float piv, float s1, float s2) {
  const float md = s1 * (1.f / DM), e2 = s2 * (1.f / DM);
  const float var = rb(fmaxf(fmaf(-md, md, e2), 0.f));
  return f2{rb(piv + md), __builtin_amdgcn_rcpf(rb(__builtin_amdgcn_sqrtf(rb(var + EPS))))};
}
__device__ __forceinline__ v4u ln_apply(v4u xr, f2 st) {
  v4u o;
#pragma unroll
  for (int i = 0; i < 4; ++i) { const u32 d = pk(blo(xr[i]) - st.x, bhi(xr[i]) - st.x); o[i] = pk(blo(d) * st.y, bhi(d) * st.y); }
  return o;
}
__device__ __forceinline__ v4u layernorm8(v4u xr) {
  const float piv = asf(__builtin_amdgcn_readfirstlane(xr[0] << 16));
  float s1 = 0.f, s2 = 0.f;
#pragma unroll
  for (int i = 0; i < 4; ++i) { const float d0 = blo(xr[i]) - piv, d1 = bhi(xr[i]) - piv; s1 += d0 + d1; s2 = fmaf(d0, d0, fmaf(d1, d1, s2)); }
  s1 = wsum63(s1); s2 = wsum63(s2);
  return ln_apply(xr, ln_stats(piv, asf(__builtin_amdgcn_readlane(asu(s1), 63)), asf(__builtin_amdgcn_readlane(asu(s2), 63))));
}

#define FK_PIECE(T, VO) \
  "s_waitcnt vmcnt(%c[wm])\n\t" \
  ".if %c[pt] == " #T "\n\tds_write_b32 %[fa], %[fv]\n\t.endif\n\t" \
  "buffer_load_dwordx4 " VO ", %[rs], %[so] offen offset:" #T "*1024 nt lds\n\t"
template <int W>
__device__ __forceinline__ void dma_tile(v4i rs, u32 so, u32 la, u32 v0, u32 v1, u32 v2, u32 v3, u32 fa, int tn) {
  const int landed = tn - 1 - (W - 5) / 4;
  asm volatile("s_mov_b32 m0, %[la]\n\t" FK_PIECE(0, "%[v0]") FK_PIECE(1, "%[v1]") FK_PIECE(2, "%[v2]") FK_PIECE(3, "%[v3]")
               :
               : [rs] "s"(rs), [so] "s"(so), [la] "s"(la), [v0] "v"(v0), [v1] "v"(v1), [v2] "v"(v2), [v3] "v"(v3), [fa] "v"(fa),
                 [fv] "v"(landed > 0 ? landed : 0), [wm] "i"(W - 1), [pt] "i"((W - 5) % 4)
               : "memory");
}
__device__ __forceinline__ void dma_adapt(int lag, v4i rs, u32 so, u32 la, u32 v0, u32 v1, u32 v2, u32 v3, u32 fa, int tn) {
  if (lag <= 0) dma_tile<FK_W>(rs, so, la, v0, v1, v2, v3, fa, tn);
  else if (lag <= FK_LG) dma_tile<FK_WB>(rs, so, la, v0, v1, v2, v3, fa, tn);
  else dma_tile<FK_WC>(rs, so, la, v0, v1, v2, v3, fa, tn);
}
__device__ __forceinline__ bool lds_wait_ge(volatile LAS u32* w, u32 need, bool& dead) {
  for (u32 n = 0;; ++n) {
    if ((u32)__builtin_amdgcn_readfirstlane(*w) >= need) break;
    if (dead || n > SPIN_LDS) { dead = true; break; }
    __builtin_amdgcn_s_sleep(1);
  }
  CB();
  return true;
}

__global__ void __launch_bounds__(1024, 4) fused_block(Params p) {
  __shared__ __attribute__((aligned(1024))) unsigned char lds_raw[L_END];
  LAS unsigned char* L = (LAS unsigned char*)lds_raw;
  const int tid = threadIdx.x, lane = tid & 63, wave = __builtin_amdgcn_readfirstlane(tid >> 6), w = wave & 3, role = wave >> 2;
  const int g = lane >> 4, c = lane & 15;
  const int kvh = blockIdx.x & 7, b = blockIdx.x >> 6, s = (blockIdx.x >> 3) & 7, grp = b * 8 + kvh;
  const rsrc_t rw = mkr(p.ws, WS_END);
  volatile LAS u32* FLAG = (volatile LAS u32*)(L + L_FLAG);
  const bool ffn = s < 7;
  const int j = kvh * 7 + s;
  bool dead = false;
  STAMP(0);

  if (role == 1) {
    __builtin_amdgcn_s_setprio(3);
    const u32 tb = (u32)__builtin_amdgcn_s_memrealtime() + FK_T0;
    u32 dg = 0;
#define DIAG(n) do { if (FK_DIAG == n) dg = (u32)__builtin_amdgcn_s_memrealtime(); } while (0)
    const v4i rk = mkr_s(p.k + (size_t)grp * SK * 128, SK * 256), rv = mkr_s(p.v + (size_t)grp * SK * 128, SK * 256);
    const u32 k0 = g * 256 + ((c ^ g) << 4), k1 = g * 256 + ((c ^ (4 + g)) << 4), k2 = g * 256 + ((c ^ (8 + g)) << 4), k3 = g * 256 + ((c ^ (12 + g)) << 4);
    const u32 u0 = g * 256 + ((c ^ (g << 1)) << 4), u1 = g * 256 + ((c ^ ((4 + g) << 1)) << 4);
    const u32 ring = (u32)(size_t)(L + L_RING) + w * 32768, fa = (u32)(size_t)(FLAG + w);
    STAMP(5);
    if (lane < 4) FLAG[w + 4 * lane] = 0u;
    wg_barrier();
    STAMP(7);
    dma_tile<FK_W>(rk, (u32)(s * 4 + w) * 4096u, ring, k0, k1, k2, k3, fa, 0);
    STAMP(6);
    dma_tile<FK_W>(rv, (u32)(s * 4 + w) * 4096u, ring + 4096, u0, u1, u0, u1, fa, 1);
    STAMP(8);
    DIAG(1);
#pragma unroll 1
    for (int i = 1; i < NCH; ++i) {
      const u32 so = (u32)((i * 8 + s) * 4 + w) * 4096u, la = ring + (i & 3) * 8192;
      if (i >= 4) lds_wait_ge(FLAG + 4 + w, i - 3, dead);
      const int lag = (int)((u32)__builtin_amdgcn_s_memrealtime() - tb) - ((i * FK_TAU) >> 4);
      dma_adapt(lag, rk, so, la, k0, k1, k2, k3, fa, 2 * i);
      dma_adapt(lag, rv, so, la + 4096, u0, u1, u0, u1, fa, 2 * i + 1);
    }
    asm volatile("s_waitcnt vmcnt(8)\n\tds_write_b32 %0, %1\n\ts_waitcnt vmcnt(4)\n\tds_write_b32 %0, %2\n\ts_waitcnt vmcnt(0)\n\tds_write_b32 %0, %3" ::"v"(fa),
                 "v"(2 * NCH - 2), "v"(2 * NCH - 1), "v"(2 * NCH) : "memory");
    STAMP(1);
    DIAG(2);
    wg_barrier();
    wg_barrier();
    wg_barrier();
    wg_barrier();
    if (w == 0) {
      const int jj = kvh * 8 + s;
      const u32 off = lane < 56 ? WS_D + (u32)(b * 56 + lane) * 2048 + jj * 32 : OOB;
      const v4u hv = *(const LAS v4u*)(L + L_HS + jj * 16);
      const u32 tg = lane < 56 ? FLAG[12] : 0u;
      pause<FK_D3>();
      STAMP(2);
      v4u d0 = ld4(rw, off), d1 = ld4(rw, off + 16);
      for (u32 n = 0; __any(bad4(d0, tg) || bad4(d1, tg)); ++n) {
        if (dead || n > SPIN_MEM) { dead = true; break; }
        CB();
        if (bad4(d0, tg) || bad4(d1, tg)) { d0 = ld4(rw, off); d1 = ld4(rw, off + 16); }
      }
      STAMP(3);
      v4u ov;
#pragma unroll
      for (int i = 0; i < 2; ++i) {
        const u32 a = pk(wsum63(dec(d0[2 * i])), wsum63(dec(d0[2 * i + 1]))), bq = pk(wsum63(dec(d1[2 * i])), wsum63(dec(d1[2 * i + 1])));
        ov[i] = pk(blo(hv[i]) + blo(a), bhi(hv[i]) + bhi(a));
        ov[2 + i] = pk(blo(hv[2 + i]) + blo(bq), bhi(hv[2 + i]) + bhi(bq));
      }
      if (lane == 63) *(GAS v4u*)(p.out + b * DM + jj * 8) = ov;
      STAMP(4);
      DIAG(3);
      if (blockIdx.x == FK_DIAGB) { const u32 now = (u32)__builtin_amdgcn_s_memrealtime(), x = (dg - (tb - FK_T0)) * FK_DIAGK;
        while ((u32)__builtin_amdgcn_s_memrealtime() - now < x && x < 100000) __builtin_amdgcn_s_sleep(1); }
    }
    FLUSH();
    return;
  }

  if (role == 2) {
    LAS unsigned char* priv = L + L_PRIV + w * 1024;
    wg_barrier();
    pause<FK_W0>(); pause<FK_W0>();
    v2u wor[4][8]; v4u dr[8];
    const rsrc_t ro = mkr(p.wo, NQ * DM * 2), rd = mkr(p.wd, FF * DM * 2);
#pragma unroll
    for (int u = 0; u < 4; ++u)
#pragma unroll
      for (int r = 0; r < 8; ++r) {
        wor[u][r] = __builtin_amdgcn_raw_buffer_load_b64(ro, g * 8 * 1024 + 8 * c, (kvh * 512 + 128 * w + 32 * u + r) * 1024 + 128 * s, WAUX);
        pause<FK_P1>();
      }
#pragma unroll
    for (int r = 0; r < 8; ++r) {
      dr[r] = __builtin_amdgcn_raw_buffer_load_b128(rd, ffn ? (8 * g) * 1024 + 16 * c : OOB, (32 * j + r) * 1024 + 256 * w, WAUX);
      pause<FK_P1>();
    }
    v4u wf[4][4], df[8];
#pragma unroll
    for (int u = 0; u < 4; ++u) { tr84(wor[u], wf[u]); asm volatile("" : "+v"(wf[u][0]), "+v"(wf[u][1]), "+v"(wf[u][2]), "+v"(wf[u][3])); }
#pragma unroll
    for (int t = 0; t < 4; ++t)
#pragma unroll
      for (int q = 0; q < 4; ++q) { df[2 * q][t] = plo(dr[2 * t + 1][q], dr[2 * t][q]); df[2 * q + 1][t] = phi(dr[2 * t + 1][q], dr[2 * t][q]); }
    asm volatile("" : "+v"(df[0]), "+v"(df[1]), "+v"(df[2]), "+v"(df[3]), "+v"(df[4]), "+v"(df[5]), "+v"(df[6]), "+v"(df[7]));
    lds_wait_ge(FLAG + 9, 4, dead);
    const u32 tg = FLAG[12], t16 = (lane & 1) ? tg >> 16 : tg & 0xffffu;
    const Tag T = mktag(tg);
    STAMP(1);
    wg_barrier();
    {
      const u32 oo = WS_O + grp * 8 * 2048 + w * 512 + lane * 8, om = WS_ML + grp * 512 + (lane & 7) * 64 + w * 16;
      pause<FK_D1>();
      STAMP(2);
      v2u o[8]; v4u ml;
#pragma unroll
      for (int k = 0; k < 8; ++k) o[k] = ld2(rw, oo, k * 2048);
      ml = ld4(rw, om);
      for (u32 n = 0;; ++n) {
        bool bad = bad4(ml, tg);
#pragma unroll
        for (int k = 0; k < 8; ++k) bad = bad || bad2(o[k], t16);
        if (!__any(bad)) break;
        if (dead || n > SPIN_MEM) { dead = true; break; }
        CB();
#pragma unroll
        for (int k = 0; k < 8; ++k) if (__any(bad2(o[k], t16))) o[k] = ld2(rw, oo, k * 2048);
        if (__any(bad4(ml, tg))) ml = ld4(rw, om);
      }
      STAMP(3);
      float M = dec(ml.x);
      M = fmaxf(M, dppm<0xB1>(M)); M = fmaxf(M, dppm<0x4E>(M)); M = fmaxf(M, dppm<0x141>(M));
      const float e = __builtin_amdgcn_exp2f((dec(ml.x) - M) * L2E);
      float Ls = e * dec(ml.y);
      Ls += dppm<0xB1>(Ls); Ls += dppm<0x4E>(Ls); Ls += dppm<0x141>(Ls);
      float o0 = 0.f, o1 = 0.f;
#pragma unroll
      for (int k = 0; k < 8; ++k) { const float ek = asf(__builtin_amdgcn_readlane(asu(e), k)); o0 = fmaf(ek, dec(o[k].x), o0); o1 = fmaf(ek, dec(o[k].y), o1); }
      const float inv = __builtin_amdgcn_rcpf(Ls);
      ((LAS u32*)priv)[lane] = pk(o0 * inv, o1 * inv);
      f4 y[4];
#pragma unroll
      for (int i = 0; i < 4; ++i) y[i] = f4{0.f, 0.f, 0.f, 0.f};
#pragma unroll
      for (int u = 0; u < 4; ++u) {
        const v4u a = *(const LAS v4u*)(priv + (32 * u + 8 * g) * 2);
#pragma unroll
        for (int i = 0; i < 4; ++i) y[i] = mfma32(a, wf[u][i], y[i]);
      }
      if (lane < 16) *(LAS f4*)(L + L_YRED + w * 256 + c * 16) = f4{y[0][0], y[1][0], y[2][0], y[3][0]};
    }
    wg_barrier();
    {
      const LAS f4* yr = (const LAS f4*)(L + L_YRED) + c;
      const f4 y = (yr[0] + yr[16]) + (yr[32] + yr[48]);
      st4(rw, lane < 32 ? WS_Y + (u32)(2 * w + g) * 65536 + grp * 2048 + s * 256 + c * 16 : OOB, enc4(y, T));
    }
    STAMP(4);
    wg_barrier();
    wg_barrier();
    STAMP(5);
    if (ffn) {
      const LAS float* gu = (const LAS float*)(L + L_GU) + (lane & 31);
      const float gt = rb((gu[0] + gu[64]) + (gu[128] + gu[192])), up = rb((gu[32] + gu[96]) + (gu[160] + gu[224]));
      const float sg = rb(gt * __builtin_amdgcn_rcpf(1.f + __builtin_amdgcn_exp2f(-gt * L2E)));
      ((LAS u16*)priv)[lane & 31] = (u16)pk(sg * up, 0.f);
      const v4u a = *(const LAS v4u*)(priv + 16 * g);
      f4 o[8];
#pragma unroll
      for (int i = 0; i < 8; ++i) o[i] = mfma32(a, df[i], f4{0.f, 0.f, 0.f, 0.f});
      const bool hi = g & 1;
      const v4u ov = enc4(f4{hi ? o[4][0] : o[0][0], hi ? o[5][0] : o[1][0], hi ? o[6][0] : o[2][0], hi ? o[7][0] : o[3][0]}, T);
      st4(rw, lane < 32 ? WS_D + (u32)(b * 56 + j) * 2048 + (u32)(128 * w + 8 * c + 4 * g) * 4 : OOB, ov);
    }
    STAMP(6);
    FLUSH();
    return;
  }

  if (role == 3) {
    LAS unsigned char* priv = L + L_PRIV + 4096 + w * 1024;
    const int t = tid - 768;
    wg_barrier();
    pause<FK_W0>(); pause<FK_W0>();
    const u32 xp = *(const GAS u32*)(p.x + b * DM + 2 * t);
    v2u gr[4][8];
    v4u gf[4][4];
    if (ffn) {
      const GAS unsigned char* gp = (const GAS unsigned char*)(c < 8 ? p.wg : p.wu) + (size_t)(128 * w + 8 * g) * (FF * 2) + 64 * j + 8 * (c & 7);
#pragma unroll
      for (int u = 0; u < 4; ++u)
#pragma unroll
        for (int r = 0; r < 8; ++r) {
          const GAS v2u* a = (const GAS v2u*)(gp + (size_t)(32 * u + r) * (FF * 2));
          gr[u][r] = FK_WNT ? __builtin_nontemporal_load(a) : *a;
          pause<FK_P2>();
        }
#pragma unroll
      for (int u = 0; u < 4; ++u) { tr84(gr[u], gf[u]); asm volatile("" : "+v"(gf[u][0]), "+v"(gf[u][1]), "+v"(gf[u][2]), "+v"(gf[u][3])); }
    }
    lds_wait_ge(FLAG + 9, 4, dead);
    const u32 tg = FLAG[12];
    if (blockIdx.x == 0) {
      const u32 tlo = FLAG[10];
      for (u32 n = 0;; ++n) {
        const v2u gv = ld2(rw, WS_QG + (u32)(lane >> 3) * 16384 + w * 4096 + (lane & 7) * 512);
        if (__all(gv.y == tlo)) break;
        if (dead || n > SPIN_MEM) { dead = true; break; }
        pause<8>();
      }
    }
    STAMP(1);
    wg_barrier();
    wg_barrier();
    {
      const u32 off = WS_Y + (u32)kvh * 65536 + (u32)b * 8 * 2048 + (u32)t * 8, t16 = (lane & 1) ? tg >> 16 : tg & 0xffffu;
      pause<FK_D2>();
      STAMP(2);
      v2u y[8];
#pragma unroll
      for (int k = 0; k < 8; ++k) y[k] = ld2(rw, off, k * 2048);
      for (u32 n = 0;; ++n) {
        bool bad = false;
#pragma unroll
        for (int k = 0; k < 8; ++k) bad = bad || bad2(y[k], t16);
        if (!__any(bad)) break;
        if (dead || n > SPIN_MEM) { dead = true; break; }
        CB();
#pragma unroll
        for (int k = 0; k < 8; ++k) if (__any(bad2(y[k], t16))) y[k] = ld2(rw, off, k * 2048);
      }
      STAMP(3);
      const u32 at = pk(((dec(y[0].x) + dec(y[1].x)) + (dec(y[2].x) + dec(y[3].x))) + ((dec(y[4].x) + dec(y[5].x)) + (dec(y[6].x) + dec(y[7].x))),
                        ((dec(y[0].y) + dec(y[1].y)) + (dec(y[2].y) + dec(y[3].y))) + ((dec(y[4].y) + dec(y[5].y)) + (dec(y[6].y) + dec(y[7].y))));
      ((LAS u32*)(L + L_HS))[t] = pk(blo(xp) + blo(at), bhi(xp) + bhi(at));
    }
    wg_barrier();
    STAMP(4);
    if (ffn) {
      const v4u hn = layernorm8(*(const LAS v4u*)(L + L_HS + lane * 16));
      *(LAS v4u*)(priv + lane * 16) = hn;
      f4 acc[4];
#pragma unroll
      for (int i = 0; i < 4; ++i) acc[i] = f4{0.f, 0.f, 0.f, 0.f};
#pragma unroll
      for (int u = 0; u < 4; ++u) {
        const v4u a = *(const LAS v4u*)(priv + (128 * w + 32 * u + 8 * g) * 2);
#pragma unroll
        for (int i = 0; i < 4; ++i) acc[i] = mfma32(a, gf[u][i], acc[i]);
      }
      if (lane < 16) *(LAS f4*)(L + L_GU + w * 256 + c * 16) = f4{acc[0][0], acc[1][0], acc[2][0], acc[3][0]};
    }
    STAMP(5);
    wg_barrier();
    if (blockIdx.x == 0 && w == 0) st2(rw, lane == 0 ? WS_E : OOB, v2u{FLAG[10], FLAG[11]});
    FLUSH();
    return;
  }

  v4u qf[4];
  u32 tg;
  {
    STAMP(6);
    v4u x4[4];
#pragma unroll
    for (int t = 0; t < 4; ++t) x4[t] = *(const GAS v4u*)(p.x + g * DM + 128 * t + 8 * c);
    const v4u xa = *(const GAS v4u*)(p.x + (c & 3) * DM + 128 * b + 32 * w + 8 * g);
    const rsrc_t rq = mkr(p.wq, DM * NQ * 2);
    v2u wr[8];
#pragma unroll
    for (int r = 0; r < 8; ++r)
      wr[r] = __builtin_amdgcn_raw_buffer_load_b64(rq, g * 8 * 8192 + 8 * c, (128 * b + 32 * w + r) * 8192 + (kvh * 512 + 64 * s) * 2, 2);
    const v2u e = ld2(rw, WS_E);
    STAMP(7);
    wg_barrier();
    STAMP(10);
    const float piv = dppm<0x150>(blo(x4[0][0]));
    float s1 = 0.f, s2 = 0.f;
#pragma unroll
    for (int t = 0; t < 4; ++t)
#pragma unroll
      for (int i = 0; i < 4; ++i) { const float d0 = blo(x4[t][i]) - piv, d1 = bhi(x4[t][i]) - piv; s1 += d0 + d1; s2 = fmaf(d0, d0, fmaf(d1, d1, s2)); }
    const f2 st = ln_stats(piv, rsum16(s1), rsum16(s2));
    const f2 sa = {asf(__builtin_amdgcn_ds_bpermute((c & 3) * 64, asu(st.x))), asf(__builtin_amdgcn_ds_bpermute((c & 3) * 64, asu(st.y)))};
    const v4u a = ln_apply(xa, sa);
    v4u f[4];
    tr84(wr, f);
    f4 acc[4];
#pragma unroll
    for (int i = 0; i < 4; ++i) acc[i] = mfma32(a, f[i], f4{0.f, 0.f, 0.f, 0.f});
    if (lane < 16) {
#pragma unroll
      for (int r = 0; r < 4; ++r) *(LAS f4*)(L + L_QRED + w * 1024 + r * 256 + c * 16) = f4{acc[0][r], acc[1][r], acc[2][r], acc[3][r]};
    }
    const u64 epoch = ((u64)__builtin_amdgcn_readfirstlane(e.y) << 32 | __builtin_amdgcn_readfirstlane(e.x)) + 1;
    const u32 tlo = (u32)epoch, thi = (u32)(epoch >> 32) + tlo * 0x9E3779B1u;
    const u32 dl = tlo - (u32)(epoch >> 32);
    tg = ((tlo + 1u) & 0xffu) | (((tlo >> 8) + 3u * dl + 1u) & 0xffu) << 8 | (((tlo >> 16) + 5u * dl + 1u) & 0xffu) << 16 | ((tlo >> 24) + 7u * dl + 1u) << 24;
    if (w == 0 && lane == 0) { FLAG[10] = tlo; FLAG[11] = (u32)(epoch >> 32); FLAG[12] = tg; }
    asm volatile("s_waitcnt lgkmcnt(0)" ::: "memory");
    STAMP(11);
    u32 old = 0;
    if (lane == 0) old = __hip_atomic_fetch_add((LAS u32*)(L + L_FLAG) + 8, 1u, __ATOMIC_RELAXED, __HIP_MEMORY_SCOPE_WORKGROUP);
    old = __builtin_amdgcn_readfirstlane(old);
    CB();
    if (old == 3) {
      const LAS f2* qr = (const LAS f2*)(L + L_QRED) + g * 32 + c;
      const f2 q0 = (qr[0] + qr[128]) + (qr[256] + qr[384]), q1 = (qr[16] + qr[144]) + (qr[272] + qr[400]);
      const u32 off = WS_QG + (u32)(g * 8 + kvh) * 16384 + b * 4096 + s * 512 + c * 16;
      st4(rw, off, v4u{asu(q0.x), tlo, asu(q0.y), thi});
      st4(rw, off + 256, v4u{asu(q1.x), tlo, asu(q1.y), thi});
    }
    STAMP(1);
    pause<FK_D0>();
    v4u gq[4];
    const u32 qo = WS_QG + grp * 16384 + w * 1024 + lane * 16;
#pragma unroll
    for (int t = 0; t < 4; ++t) gq[t] = ld4(rw, qo, t * 4096);
    for (u32 n = 0;; ++n) {
      bool bad = false;
#pragma unroll
      for (int t = 0; t < 4; ++t) bad = bad || gq[t].y != tlo || gq[t].w != thi;
      if (!__any(bad)) break;
      if (dead || n > SPIN_MEM) { dead = true; break; }
      pause<2>();
#pragma unroll
      for (int t = 0; t < 4; ++t) if (__any(gq[t].y != tlo || gq[t].w != thi)) gq[t] = ld4(rw, qo, t * 4096);
    }
    STAMP(12);
    ((LAS u32*)(L + L_QS))[64 * w + lane] = pk((asf(gq[0].x) + asf(gq[1].x)) + (asf(gq[2].x) + asf(gq[3].x)), (asf(gq[0].z) + asf(gq[1].z)) + (asf(gq[2].z) + asf(gq[3].z)));
    asm volatile("s_waitcnt lgkmcnt(0)" ::: "memory");
    if (lane == 0) __hip_atomic_fetch_add((LAS u32*)(L + L_FLAG) + 9, 1u, __ATOMIC_RELAXED, __HIP_MEMORY_SCOPE_WORKGROUP);
    lds_wait_ge(FLAG + 9, 4, dead);
#pragma unroll
    for (int u = 0; u < 4; ++u) qf[u] = *(const LAS v4u*)(L + L_QS + ((c & 3) * 128 + 32 * u + 8 * g) * 2);
    STAMP(2);
  }
  float m = -INFINITY, l = 0.f;
  f4 acc[8];
#pragma unroll
  for (int i = 0; i < 8; ++i) acc[i] = f4{0.f, 0.f, 0.f, 0.f};
  const LAS unsigned char* ringk = L + L_RING + w * 32768 + c * 256;
  const u32 vrow = 4 * g + (c >> 2);
  const u32 va = (u32)(size_t)(L + L_RING) + w * 32768 + 4096 + vrow * 256 + (vrow & 7) * 32 + 8 * (c & 3);
  u32 seen = 0;
  auto need = [&](u32 n) {
    for (u32 k = 0; seen < n; ++k) {
      seen = __builtin_amdgcn_readfirstlane(FLAG[w]);
      if (seen >= n) break;
      if (dead || k > SPIN_LDS) { dead = true; break; }
      __builtin_amdgcn_s_sleep(1);
    }
    CB();
  };
#pragma unroll 1
  for (int i = 0; i < NCH; ++i) {
    const u32 sl = (i & 3) * 8192;
    need(2 * i + 1);
    v4u kf[4];
#pragma unroll
    for (int u = 0; u < 4; ++u) kf[u] = *(const LAS v4u*)(ringk + sl + (((4 * u + g) ^ c) << 4));
    f4 s0 = mfma32(kf[0], qf[0], f4{0.f, 0.f, 0.f, 0.f}), s1 = mfma32(kf[2], qf[2], f4{0.f, 0.f, 0.f, 0.f});
    s0 = mfma32(kf[1], qf[1], s0); s1 = mfma32(kf[3], qf[3], s1);
    const u32 a01 = pk(s0[0] + s1[0], s0[1] + s1[1]), a23 = pk(s0[2] + s1[2], s0[3] + s1[3]);
    const u32 b01 = pk(blo(a01) * SCALE, bhi(a01) * SCALE), b23 = pk(blo(a23) * SCALE, bhi(a23) * SCALE);
    const float t0 = blo(b01), t1 = bhi(b01), t2 = blo(b23), t3 = bhi(b23);
    const float mx = xg_max(fmaxf(fmaxf(t0, t1), fmaxf(t2, t3)));
    if (__any(mx > m)) {
      const float mn = fmaxf(m, mx), al = __builtin_amdgcn_exp2f((m - mn) * L2E);
      l *= al; m = mn;
#pragma unroll
      for (int d = 0; d < 8; ++d) acc[d] *= al;
    }
    const float p0 = __builtin_amdgcn_exp2f((t0 - m) * L2E), p1 = __builtin_amdgcn_exp2f((t1 - m) * L2E),
                p2 = __builtin_amdgcn_exp2f((t2 - m) * L2E), p3 = __builtin_amdgcn_exp2f((t3 - m) * L2E);
    l += (p0 + p1) + (p2 + p3);
    const v2u pf = {pk(p0, p1), pk(p2, p3)};
    if (i == NCH - 1) {
      const float lt = xg_sum(l);
      if (lane < 4) *(LAS f2*)(L + L_MLS + w * 32 + c * 8) = f2{m, lt};
    }
    need(2 * i + 2);
    s4 vf[8];
#pragma unroll
    for (int d = 0; d < 8; ++d) vf[d] = __builtin_amdgcn_ds_read_tr16_b64_v4i16((LAS s4*)(size_t)((va ^ (d << 5)) + sl));
    asm volatile("s_waitcnt lgkmcnt(0)" ::: "memory");
    FLAG[4 + w] = i + 1;
#pragma unroll
    for (int d = 0; d < 8; ++d) acc[d] = mfma16(vf[d], pf, acc[d]);
  }
  STAMP(4);
  if (c < 4) {
#pragma unroll
    for (int d = 0; d < 8; ++d) *(LAS f4*)(L + L_OM + w * 2048 + c * 512 + (16 * d + 4 * g) * 4) = acc[d];
  }
  wg_barrier();
  {
    f2 ml[4]; f4 ov[4];
#pragma unroll
    for (int k = 0; k < 4; ++k) { ml[k] = *(const LAS f2*)(L + L_MLS + k * 32 + w * 8); ov[k] = *(const LAS f4*)(L + L_OM + k * 2048 + w * 512 + (lane & 31) * 16); }
    const float M = fmaxf(fmaxf(ml[0].x, ml[1].x), fmaxf(ml[2].x, ml[3].x));
    float Ls = 0.f; f4 O = {0.f, 0.f, 0.f, 0.f};
#pragma unroll
    for (int k = 0; k < 4; ++k) { const float e = __builtin_amdgcn_exp2f((ml[k].x - M) * L2E); Ls = fmaf(e, ml[k].y, Ls); O += ov[k] * e; }
    const u32 wgi = grp * 8 + s;
    const Tag T = mktag(tg);
    st4(rw, lane < 32 ? WS_O + wgi * 2048 + w * 512 + lane * 16 : (lane == 32 ? WS_ML + wgi * 64 + w * 16 : OOB), enc4(lane < 32 ? O : f4{M, Ls, M, Ls}, T));
  }
  STAMP(5);
  wg_barrier();
  wg_barrier();
  wg_barrier();
  FLUSH();
}
}

extern "C" void kernel_launch(void* const* d_in, const int* in_sizes, int n_in, void* d_out, int out_size, void* d_ws, size_t ws_size,
                              hipStream_t stream) {
  fk::Params p;
  p.x = (const fk::u16*)d_in[0]; p.k = (const fk::u16*)d_in[1]; p.v = (const fk::u16*)d_in[2];
  p.wq = (const fk::u16*)d_in[3]; p.wo = (const fk::u16*)d_in[4];
  p.wg = (const fk::u16*)d_in[6]; p.wu = (const fk::u16*)d_in[7]; p.wd = (const fk::u16*)d_in[8];
  p.out = (fk::u16*)d_out; p.ws = (unsigned char*)d_ws;
  hipLaunchKernelGGL(fk::fused_block, dim3(256), dim3(1024), 0, stream, p);
}
```

```cpp
#include <hip/hip_runtime.h>
#include <stdint.h>

#define FK_W 5
#define FK_WNT 1
#define FK_W0 120
#define FK_P1 18
#define FK_P2 22
#define FK_ORD 1
#define FK_G0 0
#define FK_WB 7
#define FK_WC 10
#define FK_LG 60
#define FK_T0 80
#define FK_TAU 1900
#define FK_DIAG 0
#define FK_DIAGB 0
#define FK_DIAGK 1
#define FK_DB2 0
#define FK_BO 1
#define FK_D0 40
#define FK_D1 18
#define FK_D2 16
#define FK_D3 32
#define FK_STAMPS 0

namespace fk {
#define GAS __attribute__((address_space(1)))
#define LAS __attribute__((address_space(3)))
typedef unsigned u32;
typedef unsigned long long u64;
typedef unsigned short u16;
typedef u32 v2u __attribute__((ext_vector_type(2)));
typedef u32 v4u __attribute__((ext_vector_type(4)));
typedef int v4i __attribute__((ext_vector_type(4)));
typedef float f2 __attribute__((ext_vector_type(2)));
typedef float f4 __attribute__((ext_vector_type(4)));
typedef short s4 __attribute__((ext_vector_type(4)));
typedef __bf16 b2 __attribute__((ext_vector_type(2)));
typedef __bf16 b8 __attribute__((ext_vector_type(8)));

constexpr int DM = 512, SK = 16384, FF = 1792, NQ = 4096;
constexpr int NCH = 32;
constexpr float L2E = 1.4426950408889634f, SCALE = 0.08838834764831845f, EPS = 1e-5f;
constexpr u32 WS_E = 0, WS_QG = 4096, WS_O = WS_QG + 32 * 16384, OREC = 2048, WS_ML = WS_O + 256 * OREC  , WS_Y = WS_ML + 256 * 128,
              WS_D = WS_Y + 8 * 65536, WS_END = WS_D + 4 * 56 * 2048, WS_DBG = WS_END;
constexpr u32 OOB = 0x80000000u;
constexpr u32 L_RING = 0, L_FLAG = 131072  , L_QRED = L_FLAG + 256  , L_QS = L_QRED + 4096,
              L_PRIV = L_QS + 1024  , L_OM = L_PRIV + 8192, L_MLS = L_OM + 8192, L_YRED = L_MLS + 128, L_HS = L_YRED + 1024,
              L_GU = L_HS + 1024, L_END = L_GU + 1024;
constexpr u32 SPIN_LDS = 1u << 19, SPIN_MEM = 1u << 16;

struct Params {
  const u16 *x, *k, *v, *wq, *wo, *wg, *wu, *wd;
  u16* out;
  unsigned char* ws;
};

#define CB() asm volatile("" ::: "memory")
__device__ __forceinline__ void wg_barrier() {
  asm volatile("s_waitcnt lgkmcnt(0)\n\ts_barrier" ::: "memory");
  __builtin_amdgcn_sched_barrier(0);
}
__device__ __forceinline__ float asf(u32 x) { return __builtin_bit_cast(float, x); }
__device__ __forceinline__ u32 asu(float x) { return __builtin_bit_cast(u32, x); }
__device__ __forceinline__ u32 pk(float lo, float hi) { f2 z = {lo, hi}; return __builtin_bit_cast(u32, __builtin_convertvector(z, b2)); }
__device__ __forceinline__ float blo(u32 p) { return asf(p << 16); }
__device__ __forceinline__ float bhi(u32 p) { return asf(p & 0xffff0000u); }
__device__ __forceinline__ float rb(float x) { return blo(pk(x, x)); }
template <int CTRL, int RM = 0xf> __device__ __forceinline__ float dpp0(float x) {
  return asf(__builtin_amdgcn_update_dpp(0, asu(x), CTRL, RM, 0xf, true));
}
template <int CTRL> __device__ __forceinline__ float dppm(float x) { return asf(__builtin_amdgcn_mov_dpp(asu(x), CTRL, 0xf, 0xf, true)); }
__device__ __forceinline__ float wsum63(float v) {
  v += dpp0<0x111>(v); v += dpp0<0x112>(v); v += dpp0<0x114>(v); v += dpp0<0x118>(v);
  v += asf(__builtin_amdgcn_update_dpp(0, asu(v), 0x142, 0xa, 0xf, false));
  v += asf(__builtin_amdgcn_update_dpp(0, asu(v), 0x143, 0xc, 0xf, false));
  return v;
}
__device__ __forceinline__ float wsum(float v) { return asf(__builtin_amdgcn_readlane(asu(wsum63(v)), 63)); }
__device__ __forceinline__ float xg_max(float x) {
  auto s = __builtin_amdgcn_permlane16_swap(asu(x), asu(x), false, false);
  x = fmaxf(asf(s[0]), asf(s[1]));
  auto t = __builtin_amdgcn_permlane32_swap(asu(x), asu(x), false, false);
  return fmaxf(asf(t[0]), asf(t[1]));
}
__device__ __forceinline__ float xg_sum(float x) {
  auto s = __builtin_amdgcn_permlane16_swap(asu(x), asu(x), false, false);
  x = asf(s[0]) + asf(s[1]);
  auto t = __builtin_amdgcn_permlane32_swap(asu(x), asu(x), false, false);
  return asf(t[0]) + asf(t[1]);
}
__device__ __forceinline__ float x32_sum(float x) {
  auto t = __builtin_amdgcn_permlane32_swap(asu(x), asu(x), false, false);
  return asf(t[0]) + asf(t[1]);
}
__device__ __forceinline__ f4 mfma32(v4u a, v4u b, f4 c) {
  return __builtin_amdgcn_mfma_f32_16x16x32_bf16(__builtin_bit_cast(b8, a), __builtin_bit_cast(b8, b), c, 0, 0, 0);
}
__device__ __forceinline__ f4 mfma16(s4 a, v2u b, f4 c) {
  return __builtin_amdgcn_mfma_f32_16x16x16bf16_1k(a, __builtin_bit_cast(s4, b), c, 0, 0, 0);
}
__device__ __forceinline__ u32 plo(u32 r1, u32 r0) { return __builtin_amdgcn_perm(r1, r0, 0x05040100u); }
__device__ __forceinline__ u32 phi(u32 r1, u32 r0) { return __builtin_amdgcn_perm(r1, r0, 0x07060302u); }
__device__ __forceinline__ void tr84(const v2u (&r)[8], v4u (&f)[4]) {
#pragma unroll
  for (int t = 0; t < 4; ++t) {
    f[0][t] = plo(r[2 * t + 1].x, r[2 * t].x); f[1][t] = phi(r[2 * t + 1].x, r[2 * t].x);
    f[2][t] = plo(r[2 * t + 1].y, r[2 * t].y); f[3][t] = phi(r[2 * t + 1].y, r[2 * t].y);
  }
}
typedef __amdgpu_buffer_rsrc_t rsrc_t;
__device__ __forceinline__ rsrc_t mkr(const void* p, u32 bytes) { return __builtin_amdgcn_make_buffer_rsrc((void*)p, 0, bytes, 0x00020000); }
__device__ __forceinline__ v4i mkr_s(const void* p, u32 bytes) {
  const u64 a = (u64)p; v4i r;
  r.x = __builtin_amdgcn_readfirstlane((u32)a); r.y = __builtin_amdgcn_readfirstlane((u32)(a >> 32)); r.z = (int)bytes; r.w = 0x00020000;
  return r;
}
constexpr int SC1 = 16, WAUX = FK_WNT ? 2 : 0;
__device__ __forceinline__ v2u ld2(rsrc_t r, u32 vo, u32 so = 0) { return __builtin_amdgcn_raw_buffer_load_b64(r, vo, so, SC1); }
__device__ __forceinline__ v4u ld4(rsrc_t r, u32 vo, u32 so = 0) { return __builtin_amdgcn_raw_buffer_load_b128(r, vo, so, SC1); }
__device__ __forceinline__ void st2(rsrc_t r, u32 vo, v2u v) { __builtin_amdgcn_raw_buffer_store_b64(v, r, vo, 0, SC1); }
__device__ __forceinline__ void st4(rsrc_t r, u32 vo, v4u v) { __builtin_amdgcn_raw_buffer_store_b128(v, r, vo, 0, SC1); }

#define STAMP(i) do {} while (0)
#define FLUSH() do {} while (0)

template <int N> __device__ __forceinline__ void pause() { if (N > 0) asm volatile("s_sleep %0" ::"i"(N) : "memory"); }
__device__ __forceinline__ float rsum16(float v) {
  v += dppm<0x128>(v); v += dppm<0x124>(v); v += dppm<0x122>(v); v += dppm<0x121>(v);
  return v;
}
struct Tag { u32 b[4]; };
__device__ __forceinline__ Tag mktag(u32 t) { return Tag{{t & 0xffu, (t >> 8) & 0xffu, (t >> 16) & 0xffu, t >> 24}}; }
__device__ __forceinline__ u32 enc(float x, u32 tb) { return ((asu(x) + 0x80u) & 0xffffff00u) | tb; }
__device__ __forceinline__ v4u enc4(f4 x, const Tag& t) { return v4u{enc(x[0], t.b[0]), enc(x[1], t.b[1]), enc(x[2], t.b[2]), enc(x[3], t.b[3])}; }
__device__ __forceinline__ float dec(u32 y) { return asf(y & 0xffffff00u); }
__device__ __forceinline__ bool bad2(v2u y, u32 t16) { return (__builtin_amdgcn_perm(y.y, y.x, 0x0c0c0400u)) != t16; }
__device__ __forceinline__ bool bad4(v4u y, u32 t32) {
  return __builtin_amdgcn_perm(__builtin_amdgcn_perm(y.w, y.z, 0x0c0c0400u), __builtin_amdgcn_perm(y.y, y.x, 0x0c0c0400u), 0x05040100u) != t32;
}
__device__ __forceinline__ bool half_any(bool x, int lane) { const u64 m = __ballot(x); return (lane < 32 ? (u32)m : (u32)(m >> 32)) != 0u; }
__device__ __forceinline__ f2 ln_stats(float piv, float s1, float s2) {
  const float md = s1 * (1.f / DM), e2 = s2 * (1.f / DM);
  const float var = rb(fmaxf(fmaf(-md, md, e2), 0.f));
  return f2{rb(piv + md), __builtin_amdgcn_rcpf(rb(__builtin_amdgcn_sqrtf(rb(var + EPS))))};
}
__device__ __forceinline__ v4u ln_apply(v4u xr, f2 st) {
  v4u o;
#pragma unroll
  for (int i = 0; i < 4; ++i) { const u32 d = pk(blo(xr[i]) - st.x, bhi(xr[i]) - st.x); o[i] = pk(blo(d) * st.y, bhi(d) * st.y); }
  return o;
}
__device__ __forceinline__ v4u layernorm8(v4u xr) {
  const float piv = asf(__builtin_amdgcn_readfirstlane(xr[0] << 16));
  float s1 = 0.f, s2 = 0.f;
#pragma unroll
  for (int i = 0; i < 4; ++i) { const float d0 = blo(xr[i]) - piv, d1 = bhi(xr[i]) - piv; s1 += d0 + d1; s2 = fmaf(d0, d0, fmaf(d1, d1, s2)); }
  s1 = wsum63(s1); s2 = wsum63(s2);
  return ln_apply(xr, ln_stats(piv, asf(__builtin_amdgcn_readlane(asu(s1), 63)), asf(__builtin_amdgcn_readlane(asu(s2), 63))));
}

#define FK_PIECE(T, VO) \
  "s_waitcnt vmcnt(%c[wm])\n\t" \
  ".if %c[pt] == " #T "\n\tds_write_b32 %[fa], %[fv]\n\t.endif\n\t" \
  "buffer_load_dwordx4 " VO ", %[rs], %[so] offen offset:" #T "*1024 nt lds\n\t"
template <int W>
__device__ __forceinline__ void dma_tile(v4i rs, u32 so, u32 la, u32 v0, u32 v1, u32 v2, u32 v3, u32 fa, int tn) {
  const int landed = tn - 1 - (W - 5) / 4;
  asm volatile("s_mov_b32 m0, %[la]\n\t" FK_PIECE(0, "%[v0]") FK_PIECE(1, "%[v1]") FK_PIECE(2, "%[v2]") FK_PIECE(3, "%[v3]")
               :
               : [rs] "s"(rs), [so] "s"(so), [la] "s"(la), [v0] "v"(v0), [v1] "v"(v1), [v2] "v"(v2), [v3] "v"(v3), [fa] "v"(fa),
                 [fv] "v"(landed > 0 ? landed : 0), [wm] "i"(W - 1), [pt] "i"((W - 5) % 4)
               : "memory");
}
__device__ __forceinline__ void dma_adapt(int lag, v4i rs, u32 so, u32 la, u32 v0, u32 v1, u32 v2, u32 v3, u32 fa, int tn) {
  if (lag <= 0) dma_tile<FK_W>(rs, so, la, v0, v1, v2, v3, fa, tn);
  else if (lag <= FK_LG) dma_tile<FK_WB>(rs, so, la, v0, v1, v2, v3, fa, tn);
  else dma_tile<FK_WC>(rs, so, la, v0, v1, v2, v3, fa, tn);
}
__device__ __forceinline__ bool lds_wait_ge(volatile LAS u32* w, u32 need, bool& dead) {
  for (u32 n = 0;; ++n) {
    if ((u32)__builtin_amdgcn_readfirstlane(*w) >= need) break;
    if (dead || n > SPIN_LDS) { dead = true; break; }
    __builtin_amdgcn_s_sleep(1);
  }
  CB();
  return true;
}

__global__ void __launch_bounds__(1024, 4) fused_block(Params p) {
  __shared__ __attribute__((aligned(1024))) unsigned char lds_raw[L_END];
  LAS unsigned char* L = (LAS unsigned char*)lds_raw;
  const int tid = threadIdx.x, lane = tid & 63, wave = __builtin_amdgcn_readfirstlane(tid >> 6), w = wave & 3, role = wave >> 2;
  const int g = lane >> 4, c = lane & 15;
  const int kvh = blockIdx.x & 7, b = blockIdx.x >> 6, s = (blockIdx.x >> 3) & 7, grp = b * 8 + kvh;
  const rsrc_t rw = mkr(p.ws, WS_END);
  volatile LAS u32* FLAG = (volatile LAS u32*)(L + L_FLAG);
  const bool ffn = s < 7;
  const int j = kvh * 7 + s;
  bool dead = false;
  STAMP(0);

  if (role == 1) {
    __builtin_amdgcn_s_setprio(3);
    const u32 tb = (u32)__builtin_amdgcn_s_memrealtime() + FK_T0;
#define DIAG(n) do {} while (0)
    const v4i rk = mkr_s(p.k + (size_t)grp * SK * 128, SK * 256), rv = mkr_s(p.v + (size_t)grp * SK * 128, SK * 256);
    const u32 k0 = g * 256 + ((c ^ g) << 4), k1 = g * 256 + ((c ^ (4 + g)) << 4), k2 = g * 256 + ((c ^ (8 + g)) << 4), k3 = g * 256 + ((c ^ (12 + g)) << 4);
    const u32 u0 = g * 256 + ((c ^ (g << 1)) << 4), u1 = g * 256 + ((c ^ ((4 + g) << 1)) << 4);
    const u32 ring = (u32)(size_t)(L + L_RING) + w * 32768, fa = (u32)(size_t)(FLAG + w);
    STAMP(5);
    if (lane < 4) FLAG[w + 4 * lane] = 0u;
    wg_barrier();
    STAMP(7);
    dma_tile<FK_W>(rk, (u32)(s * 4 + w) * 4096u, ring, k0, k1, k2, k3, fa, 0);
    STAMP(6);
    dma_tile<FK_W>(rv, (u32)(s * 4 + w) * 4096u, ring + 4096, u0, u1, u0, u1, fa, 1);
    STAMP(8);
    DIAG(1);
#pragma unroll 1
    for (int i = 1; i < NCH; ++i) {
      const u32 so = (u32)((i * 8 + s) * 4 + w) * 4096u, la = ring + (i & 3) * 8192;
      if (i >= 4) lds_wait_ge(FLAG + 4 + w, i - 3, dead);
      const int lag = (int)((u32)__builtin_amdgcn_s_memrealtime() - tb) - ((i * FK_TAU) >> 4);
      dma_adapt(lag, rk, so, la, k0, k1, k2, k3, fa, 2 * i);
      dma_adapt(lag, rv, so, la + 4096, u0, u1, u0, u1, fa, 2 * i + 1);
    }
    asm volatile("s_waitcnt vmcnt(8)\n\tds_write_b32 %0, %1\n\ts_waitcnt vmcnt(4)\n\tds_write_b32 %0, %2\n\ts_waitcnt vmcnt(0)\n\tds_write_b32 %0, %3" ::"v"(fa),
                 "v"(2 * NCH - 2), "v"(2 * NCH - 1), "v"(2 * NCH) : "memory");
    STAMP(1);
    DIAG(2);
    __builtin_amdgcn_s_setprio(0);
    v4u dr[8], df[8];
    const rsrc_t rd = mkr(p.wd, FF * DM * 2);
#pragma unroll
    for (int r = 0; r < 8; ++r) dr[r] = __builtin_amdgcn_raw_buffer_load_b128(rd, ffn ? (8 * g) * 1024 + 16 * c : OOB, (32 * j + r) * 1024 + 256 * w, WAUX);
    wg_barrier();
    const Tag T = mktag(FLAG[12]);
    LAS unsigned char* priv = L + L_QRED + w * 1024;
#pragma unroll
    for (int t = 0; t < 4; ++t)
#pragma unroll
      for (int q = 0; q < 4; ++q) { df[2 * q][t] = plo(dr[2 * t + 1][q], dr[2 * t][q]); df[2 * q + 1][t] = phi(dr[2 * t + 1][q], dr[2 * t][q]); }
    asm volatile("" : "+v"(df[0]), "+v"(df[1]), "+v"(df[2]), "+v"(df[3]), "+v"(df[4]), "+v"(df[5]), "+v"(df[6]), "+v"(df[7]));
    STAMP(2);
    wg_barrier();
    wg_barrier();
    wg_barrier();
    STAMP(3);
    if (ffn) {
      const LAS float* gu = (const LAS float*)(L + L_GU) + (lane & 31);
      const float gt = rb((gu[0] + gu[64]) + (gu[128] + gu[192])), up = rb((gu[32] + gu[96]) + (gu[160] + gu[224]));
      const float sg = rb(gt * __builtin_amdgcn_rcpf(1.f + __builtin_amdgcn_exp2f(-gt * L2E)));
      ((LAS u16*)priv)[lane & 31] = (u16)pk(sg * up, 0.f);
      const v4u a = *(const LAS v4u*)(priv + 16 * g);
      f4 o[8];
#pragma unroll
      for (int i = 0; i < 8; ++i) o[i] = mfma32(a, df[i], f4{0.f, 0.f, 0.f, 0.f});
      const bool hi = g & 1;
      const v4u ov = enc4(f4{hi ? o[4][0] : o[0][0], hi ? o[5][0] : o[1][0], hi ? o[6][0] : o[2][0], hi ? o[7][0] : o[3][0]}, T);
      st4(rw, lane < 32 ? WS_D + (u32)(b * 56 + j) * 2048 + (u32)(128 * w + 8 * c + 4 * g) * 4 : OOB, ov);
    }
    STAMP(4);
    FLUSH();
    return;
  }

  if (role == 2) {
    LAS unsigned char* priv = L + L_PRIV + w * 1024;
    wg_barrier();
    pause<FK_W0>(); pause<FK_W0>();
    v2u wor[4][8];
    const rsrc_t ro = mkr(p.wo, NQ * DM * 2);
#pragma unroll
    for (int u = 0; u < 4; ++u)
#pragma unroll
      for (int r = 0; r < 8; ++r) {
        wor[u][r] = __builtin_amdgcn_raw_buffer_load_b64(ro, g * 8 * 1024 + 8 * c, (kvh * 512 + 128 * w + 32 * u + r) * 1024 + 128 * s, WAUX);
        pause<FK_P1>();
      }
    v4u wf[4][4];
#pragma unroll
    for (int u = 0; u < 4; ++u) { tr84(wor[u], wf[u]); asm volatile("" : "+v"(wf[u][0]), "+v"(wf[u][1]), "+v"(wf[u][2]), "+v"(wf[u][3])); }
    lds_wait_ge(FLAG + 9, 4, dead);
    const u32 tg = FLAG[12];
    const Tag T = mktag(tg);
    STAMP(1);
    wg_barrier();
    {
      const u32 ob = WS_O + grp * 8 * OREC, om = WS_ML + (grp * 8 + (lane & 7)) * 128 + w * 16, oo = ob + (lane >> 5) * OREC + w * 512 + (lane & 31) * 16;
      pause<FK_D1>();
      STAMP(2);
      v4u o[4], ml;
      ml = ld4(rw, om);
      for (u32 n = 0; __any(bad4(ml, tg)); ++n) {
        if (dead || n > SPIN_MEM) { dead = true; break; }
        pause<FK_BO>();
        ml = ld4(rw, om);
      }
      STAMP(7);
#pragma unroll
      for (int t = 0; t < 4; ++t) o[t] = ld4(rw, oo, ((t + s) & 3) * 2 * OREC);
      float M = dec(ml.x);
      M = fmaxf(M, dppm<0xB1>(M)); M = fmaxf(M, dppm<0x4E>(M)); M = fmaxf(M, dppm<0x141>(M));
      const float e = __builtin_amdgcn_exp2f((dec(ml.x) - M) * L2E);
      float Ls = e * dec(ml.y);
      Ls += dppm<0xB1>(Ls); Ls += dppm<0x4E>(Ls); Ls += dppm<0x141>(Ls);
      const float en = e * __builtin_amdgcn_rcpf(Ls);
      float et[4];
#pragma unroll
      for (int t = 0; t < 4; ++t) {
        const float e0 = asf(__builtin_amdgcn_readlane(asu(en), 2 * ((t + s) & 3))), e1 = asf(__builtin_amdgcn_readlane(asu(en), 2 * ((t + s) & 3) + 1));
        et[t] = lane < 32 ? e0 : e1;
      }
      asm volatile("" : "+v"(et[0]), "+v"(et[1]), "+v"(et[2]), "+v"(et[3]));
      for (u32 n = 0;; ++n) {
        bool bad = false;
#pragma unroll
        for (int t = 0; t < 4; ++t) bad = bad || bad4(o[t], tg);
        if (!__any(bad)) break;
        if (dead || n > SPIN_MEM) { dead = true; break; }
        pause<FK_BO>();
#pragma unroll
        for (int t = 0; t < 4; ++t) if (half_any(bad4(o[t], tg), lane)) o[t] = ld4(rw, oo, ((t + s) & 3) * 2 * OREC);
      }
      STAMP(3);
      f4 os = {0.f, 0.f, 0.f, 0.f};
#pragma unroll
      for (int t = 0; t < 4; ++t)
#pragma unroll
        for (int i = 0; i < 4; ++i) os[i] = fmaf(et[t], dec(o[t][i]), os[i]);
#pragma unroll
      for (int i = 0; i < 4; ++i) os[i] = x32_sum(os[i]);
      ((LAS v2u*)priv)[lane & 31] = v2u{pk(os[0], os[1]), pk(os[2], os[3])};
      f4 y[4];
#pragma unroll
      for (int i = 0; i < 4; ++i) y[i] = f4{0.f, 0.f, 0.f, 0.f};
#pragma unroll
      for (int u = 0; u < 4; ++u) {
        const v4u a = *(const LAS v4u*)(priv + (32 * u + 8 * g) * 2);
#pragma unroll
        for (int i = 0; i < 4; ++i) y[i] = mfma32(a, wf[u][i], y[i]);
      }
      if (lane < 16) *(LAS f4*)(L + L_YRED + w * 256 + c * 16) = f4{y[0][0], y[1][0], y[2][0], y[3][0]};
    }
    wg_barrier();
    {
      const LAS f4* yr = (const LAS f4*)(L + L_YRED) + c;
      const f4 y = (yr[0] + yr[16]) + (yr[32] + yr[48]);
      st4(rw, lane < 32 ? WS_Y + (u32)(2 * w + g) * 65536 + grp * 2048 + s * 256 + c * 16 : OOB, enc4(y, T));
    }
    STAMP(4);
    wg_barrier();
    wg_barrier();
    STAMP(6);
    FLUSH();
    return;
  }

  if (role == 3) {
    LAS unsigned char* priv = L + L_PRIV + 4096 + w * 1024;
    wg_barrier();
    pause<FK_W0>(); pause<FK_W0>();
    const v2u xp = *(const GAS v2u*)(p.x + b * DM + 128 * w + 4 * (lane & 31));
    v2u gr[4][8];
    v4u gf[4][4];
    if (ffn) {
      const GAS unsigned char* gp = (const GAS unsigned char*)(c < 8 ? p.wg : p.wu) + (size_t)(128 * w + 8 * g) * (FF * 2) + 64 * j + 8 * (c & 7);
#pragma unroll
      for (int u = 0; u < 4; ++u)
#pragma unroll
        for (int r = 0; r < 8; ++r) {
          const GAS v2u* a = (const GAS v2u*)(gp + (size_t)(32 * u + r) * (FF * 2));
          gr[u][r] = FK_WNT ? __builtin_nontemporal_load(a) : *a;
          pause<FK_P2>();
        }
#pragma unroll
      for (int u = 0; u < 4; ++u) { tr84(gr[u], gf[u]); asm volatile("" : "+v"(gf[u][0]), "+v"(gf[u][1]), "+v"(gf[u][2]), "+v"(gf[u][3])); }
    }
    lds_wait_ge(FLAG + 9, 4, dead);
    const u32 tg = FLAG[12];
    if (blockIdx.x == 0) {
      const u32 tlo = FLAG[10];
      for (u32 n = 0;; ++n) {
        const v2u gv = ld2(rw, WS_QG + (u32)(lane >> 3) * 16384 + w * 4096 + (lane & 7) * 512);
        if (__all(gv.y == tlo)) break;
        if (dead || n > SPIN_MEM) { dead = true; break; }
        pause<8>();
      }
    }
    STAMP(1);
    wg_barrier();
    wg_barrier();
    {
      const u32 yb = WS_Y + (u32)kvh * 65536 + (u32)b * 8 * 2048 + (lane >> 5) * 2048 + w * 512 + (lane & 31) * 16;
      pause<FK_D2>();
      STAMP(2);
      v4u y[4];
#pragma unroll
      for (int k = 0; k < 4; ++k) y[k] = ld4(rw, yb, ((k + s) & 3) * 4096);
      for (u32 n = 0;; ++n) {
        bool bad = false;
#pragma unroll
        for (int k = 0; k < 4; ++k) bad = bad || bad4(y[k], tg);
        if (!__any(bad)) break;
        if (dead || n > SPIN_MEM) { dead = true; break; }
        pause<FK_BO>();
#pragma unroll
        for (int k = 0; k < 4; ++k) if (half_any(bad4(y[k], tg), lane)) y[k] = ld4(rw, yb, ((k + s) & 3) * 4096);
      }
      STAMP(3);
      f4 a;
#pragma unroll
      for (int i = 0; i < 4; ++i) {
        const float p0 = dec(y[0][i]), p1 = dec(y[1][i]), p2 = dec(y[2][i]), p3 = dec(y[3][i]);
        a[i] = x32_sum((s & 1) ? (p3 + p0) + (p1 + p2) : (p0 + p1) + (p2 + p3));
      }
      const u32 a0 = pk(a[0], a[1]), a1 = pk(a[2], a[3]);
      ((LAS v2u*)(L + L_HS))[32 * w + (lane & 31)] = v2u{pk(blo(xp.x) + blo(a0), bhi(xp.x) + bhi(a0)), pk(blo(xp.y) + blo(a1), bhi(xp.y) + bhi(a1))};
    }
    wg_barrier();
    STAMP(4);
    if (ffn) {
      const v4u hn = layernorm8(*(const LAS v4u*)(L + L_HS + lane * 16));
      *(LAS v4u*)(priv + lane * 16) = hn;
      f4 acc[4];
#pragma unroll
      for (int i = 0; i < 4; ++i) acc[i] = f4{0.f, 0.f, 0.f, 0.f};
#pragma unroll
      for (int u = 0; u < 4; ++u) {
        const v4u a = *(const LAS v4u*)(priv + (128 * w + 32 * u + 8 * g) * 2);
#pragma unroll
        for (int i = 0; i < 4; ++i) acc[i] = mfma32(a, gf[u][i], acc[i]);
      }
      if (lane < 16) *(LAS f4*)(L + L_GU + w * 256 + c * 16) = f4{acc[0][0], acc[1][0], acc[2][0], acc[3][0]};
    }
    STAMP(5);
    wg_barrier();
    if (blockIdx.x == 0 && w == 0) st2(rw, lane == 0 ? WS_E : OOB, v2u{FLAG[10], FLAG[11]});
    FLUSH();
    return;
  }

  v4u qf[4];
  u32 tg;
  {
    STAMP(6);
    v4u x4[4];
#pragma unroll
    for (int t = 0; t < 4; ++t) x4[t] = *(const GAS v4u*)(p.x + g * DM + 128 * t + 8 * c);
    const v4u xa = *(const GAS v4u*)(p.x + (c & 3) * DM + 128 * b + 32 * w + 8 * g);
    const rsrc_t rq = mkr(p.wq, DM * NQ * 2);
    v2u wr[8];
#pragma unroll
    for (int r = 0; r < 8; ++r)
      wr[r] = __builtin_amdgcn_raw_buffer_load_b64(rq, g * 8 * 8192 + 8 * c, (128 * b + 32 * w + r) * 8192 + (kvh * 512 + 64 * s) * 2, 2);
    const v2u e = ld2(rw, WS_E);
    wg_barrier();
    STAMP(10);
    const float piv = dppm<0x150>(blo(x4[0][0]));
    float s1 = 0.f, s2 = 0.f;
#pragma unroll
    for (int t = 0; t < 4; ++t)
#pragma unroll
      for (int i = 0; i < 4; ++i) { const float d0 = blo(x4[t][i]) - piv, d1 = bhi(x4[t][i]) - piv; s1 += d0 + d1; s2 = fmaf(d0, d0, fmaf(d1, d1, s2)); }
    const f2 st = ln_stats(piv, rsum16(s1), rsum16(s2));
    const f2 sa = {asf(__builtin_amdgcn_ds_bpermute((c & 3) * 64, asu(st.x))), asf(__builtin_amdgcn_ds_bpermute((c & 3) * 64, asu(st.y)))};
    const v4u a = ln_apply(xa, sa);
    v4u f[4];
    tr84(wr, f);
    f4 acc[4];
#pragma unroll
    for (int i = 0; i < 4; ++i) acc[i] = mfma32(a, f[i], f4{0.f, 0.f, 0.f, 0.f});
    if (lane < 16) {
#pragma unroll
      for (int r = 0; r < 4; ++r) *(LAS f4*)(L + L_QRED + w * 1024 + r * 256 + c * 16) = f4{acc[0][r], acc[1][r], acc[2][r], acc[3][r]};
    }
    const u64 epoch = ((u64)__builtin_amdgcn_readfirstlane(e.y) << 32 | __builtin_amdgcn_readfirstlane(e.x)) + 1;
    const u32 tlo = (u32)epoch, thi = (u32)(epoch >> 32) + tlo * 0x9E3779B1u;
    const u32 dl = tlo - (u32)(epoch >> 32);
    tg = ((tlo + 1u) & 0xffu) | (((tlo >> 8) + 3u * dl + 1u) & 0xffu) << 8 | (((tlo >> 16) + 5u * dl + 1u) & 0xffu) << 16 | ((tlo >> 24) + 7u * dl + 1u) << 24;
    if (w == 0 && lane == 0) { FLAG[10] = tlo; FLAG[11] = (u32)(epoch >> 32); FLAG[12] = tg; }
    asm volatile("s_waitcnt lgkmcnt(0)" ::: "memory");
    STAMP(11);
    u32 old = 0;
    if (lane == 0) old = __hip_atomic_fetch_add((LAS u32*)(L + L_FLAG) + 8, 1u, __ATOMIC_RELAXED, __HIP_MEMORY_SCOPE_WORKGROUP);
    old = __builtin_amdgcn_readfirstlane(old);
    CB();
    if (old == 3) {
      const LAS f2* qr = (const LAS f2*)(L + L_QRED) + g * 32 + c;
      const f2 q0 = (qr[0] + qr[128]) + (qr[256] + qr[384]), q1 = (qr[16] + qr[144]) + (qr[272] + qr[400]);
      const u32 off = WS_QG + (u32)(g * 8 + kvh) * 16384 + b * 4096 + s * 512 + c * 16;
      st4(rw, off, v4u{asu(q0.x), tlo, asu(q0.y), thi});
      st4(rw, off + 256, v4u{asu(q1.x), tlo, asu(q1.y), thi});
    }
    STAMP(1);
    pause<FK_D0>();
    v4u gq[4];
    const u32 qo = WS_QG + grp * 16384 + w * 1024 + lane * 16;
#pragma unroll
    for (int t = 0; t < 4; ++t) gq[t] = ld4(rw, qo, t * 4096);
    for (u32 n = 0;; ++n) {
      bool bad = false;
#pragma unroll
      for (int t = 0; t < 4; ++t) bad = bad || gq[t].y != tlo || gq[t].w != thi;
      if (!__any(bad)) break;
      if (dead || n > SPIN_MEM) { dead = true; break; }
      pause<2>();
#pragma unroll
      for (int t = 0; t < 4; ++t) if (__any(gq[t].y != tlo || gq[t].w != thi)) gq[t] = ld4(rw, qo, t * 4096);
    }
    STAMP(12);
    ((LAS u32*)(L + L_QS))[64 * w + lane] = pk((asf(gq[0].x) + asf(gq[1].x)) + (asf(gq[2].x) + asf(gq[3].x)), (asf(gq[0].z) + asf(gq[1].z)) + (asf(gq[2].z) + asf(gq[3].z)));
    asm volatile("s_waitcnt lgkmcnt(0)" ::: "memory");
    if (lane == 0) __hip_atomic_fetch_add((LAS u32*)(L + L_FLAG) + 9, 1u, __ATOMIC_RELAXED, __HIP_MEMORY_SCOPE_WORKGROUP);
    lds_wait_ge(FLAG + 9, 4, dead);
#pragma unroll
    for (int u = 0; u < 4; ++u) qf[u] = *(const LAS v4u*)(L + L_QS + ((c & 3) * 128 + 32 * u + 8 * g) * 2);
    STAMP(2);
  }
  float m = -INFINITY, l = 0.f;
  f4 acc[8];
#pragma unroll
  for (int i = 0; i < 8; ++i) acc[i] = f4{0.f, 0.f, 0.f, 0.f};
  const LAS unsigned char* ringk = L + L_RING + w * 32768 + c * 256;
  const u32 vrow = 4 * g + (c >> 2);
  const u32 va = (u32)(size_t)(L + L_RING) + w * 32768 + 4096 + vrow * 256 + (vrow & 7) * 32 + 8 * (c & 3);
  u32 seen = 0;
  auto need = [&](u32 n) {
    for (u32 k = 0; seen < n; ++k) {
      seen = __builtin_amdgcn_readfirstlane(FLAG[w]);
      if (seen >= n) break;
      if (dead || k > SPIN_LDS) { dead = true; break; }
      __builtin_amdgcn_s_sleep(1);
    }
    CB();
  };
#pragma unroll 1
  for (int i = 0; i < NCH; ++i) {
    const u32 sl = (i & 3) * 8192;
    need(2 * i + 1);
    v4u kf[4];
#pragma unroll
    for (int u = 0; u < 4; ++u) kf[u] = *(const LAS v4u*)(ringk + sl + (((4 * u + g) ^ c) << 4));
    f4 s0 = mfma32(kf[0], qf[0], f4{0.f, 0.f, 0.f, 0.f}), s1 = mfma32(kf[2], qf[2], f4{0.f, 0.f, 0.f, 0.f});
    s0 = mfma32(kf[1], qf[1], s0); s1 = mfma32(kf[3], qf[3], s1);
    const u32 a01 = pk(s0[0] + s1[0], s0[1] + s1[1]), a23 = pk(s0[2] + s1[2], s0[3] + s1[3]);
    const u32 b01 = pk(blo(a01) * SCALE, bhi(a01) * SCALE), b23 = pk(blo(a23) * SCALE, bhi(a23) * SCALE);
    const float t0 = blo(b01), t1 = bhi(b01), t2 = blo(b23), t3 = bhi(b23);
    const float mx = xg_max(fmaxf(fmaxf(t0, t1), fmaxf(t2, t3)));
    if (__any(mx > m)) {
      const float mn = fmaxf(m, mx), al = __builtin_amdgcn_exp2f((m - mn) * L2E);
      l *= al; m = mn;
#pragma unroll
      for (int d = 0; d < 8; ++d) acc[d] *= al;
    }
    const float p0 = __builtin_amdgcn_exp2f((t0 - m) * L2E), p1 = __builtin_amdgcn_exp2f((t1 - m) * L2E),
                p2 = __builtin_amdgcn_exp2f((t2 - m) * L2E), p3 = __builtin_amdgcn_exp2f((t3 - m) * L2E);
    l += (p0 + p1) + (p2 + p3);
    const v2u pf = {pk(p0, p1), pk(p2, p3)};
    if (i == NCH - 1) {
      const float lt = xg_sum(l);
      if (lane < 4) *(LAS f2*)(L + L_MLS + w * 32 + c * 8) = f2{m, lt};
    }
    need(2 * i + 2);
    s4 vf[8];
#pragma unroll
    for (int d = 0; d < 8; ++d) vf[d] = __builtin_amdgcn_ds_read_tr16_b64_v4i16((LAS s4*)(size_t)((va ^ (d << 5)) + sl));
    asm volatile("s_waitcnt lgkmcnt(0)" ::: "memory");
    FLAG[4 + w] = i + 1;
#pragma unroll
    for (int d = 0; d < 8; ++d) acc[d] = mfma16(vf[d], pf, acc[d]);
  }
  STAMP(4);
  if (c < 4) {
#pragma unroll
    for (int d = 0; d < 8; ++d) *(LAS f4*)(L + L_OM + w * 2048 + c * 512 + (16 * d + 4 * g) * 4) = acc[d];
  }
  wg_barrier();
  {
    const int hd = lane < 32 ? w : (lane & 3);
    f2 ml[4]; f4 ov[4];
#pragma unroll
    for (int k = 0; k < 4; ++k) { ml[k] = *(const LAS f2*)(L + L_MLS + k * 32 + hd * 8); ov[k] = *(const LAS f4*)(L + L_OM + k * 2048 + w * 512 + (lane & 31) * 16); }
    const float M = fmaxf(fmaxf(ml[0].x, ml[1].x), fmaxf(ml[2].x, ml[3].x));
    float Ls = 0.f; f4 O = {0.f, 0.f, 0.f, 0.f};
#pragma unroll
    for (int k = 0; k < 4; ++k) { const float e = __builtin_amdgcn_exp2f((ml[k].x - M) * L2E); Ls = fmaf(e, ml[k].y, Ls); O += ov[k] * e; }
    const u32 wgi = grp * 8 + s;
    st4(rw, lane < 32 ? WS_O + wgi * OREC + w * 512 + lane * 16 : (lane < 40 && w == 0 ? WS_ML + wgi * 128 + (lane - 32) * 16 : OOB), enc4(lane < 32 ? O : f4{M, Ls, M, Ls}, mktag(tg)));
  }
  STAMP(5);
  wg_barrier();
  wg_barrier();
  wg_barrier();
    if (w < 2) {
      const int col = (kvh * 8 + s) * 8 + 4 * w;
      const u32 off = lane < 56 ? WS_D + (u32)(b * 56 + lane) * 2048 + col * 4 : OOB;
      const v2u hv = *(const LAS v2u*)(L + L_HS + col * 2);
      const u32 tgl = lane < 56 ? tg : 0u;
      pause<FK_D3>();
      STAMP(7);
      v4u d0 = ld4(rw, off);
      for (u32 n = 0; __any(bad4(d0, tgl)); ++n) {
        if (dead || n > SPIN_MEM) { dead = true; break; }
        pause<FK_BO>();
        d0 = ld4(rw, off);
      }
      STAMP(8);
      const u32 a0 = pk(wsum63(dec(d0[0])), wsum63(dec(d0[1]))), a1 = pk(wsum63(dec(d0[2])), wsum63(dec(d0[3])));
      if (lane == 63) *(GAS v2u*)(p.out + b * DM + col) = v2u{pk(blo(hv.x) + blo(a0), bhi(hv.x) + bhi(a0)), pk(blo(hv.y) + blo(a1), bhi(hv.y) + bhi(a1))};
      STAMP(9);
    }
  FLUSH();
}
}

extern "C" void kernel_launch(void* const* d_in, const int* in_sizes, int n_in, void* d_out, int out_size, void* d_ws, size_t ws_size,
                              hipStream_t stream) {
  fk::Params p;
  p.x = (const fk::u16*)d_in[0]; p.k = (const fk::u16*)d_in[1]; p.v = (const fk::u16*)d_in[2];
  p.wq = (const fk::u16*)d_in[3]; p.wo = (const fk::u16*)d_in[4];
  p.wg = (const fk::u16*)d_in[6]; p.wu = (const fk::u16*)d_in[7]; p.wd = (const fk::u16*)d_in[8];
  p.out = (fk::u16*)d_out; p.ws = (unsigned char*)d_ws;
  hipLaunchKernelGGL(fk::fused_block, dim3(256), dim3(1024), 0, stream, p);
}
```

```cpp
#include <hip/hip_runtime.h>
#include <stdint.h>

#define FK_W 5
#define FK_WNT 1
#define FK_W0 120
#define FK_P1 18
#define FK_P2 22
#define FK_ORD 1
#define FK_G0 0
#define FK_WB 7
#define FK_WC 10
#define FK_LG 60
#define FK_T0 80
#define FK_TAU 1830
#define FK_DIAG 0
#define FK_DIAGB 0
#define FK_DIAGK 1
#define FK_DB2 0
#define FK_KNT 1
#define FK_VNT 0
#define FK_BO 1
#define FK_D0 40
#define FK_D1 18
#define FK_D2 16
#define FK_D3 32
#define FK_STAMPS 0

namespace fk {
#define GAS __attribute__((address_space(1)))
#define LAS __attribute__((address_space(3)))
typedef unsigned u32;
typedef unsigned long long u64;
typedef unsigned short u16;
typedef u32 v2u __attribute__((ext_vector_type(2)));
typedef u32 v4u __attribute__((ext_vector_type(4)));
typedef int v4i __attribute__((ext_vector_type(4)));
typedef float f2 __attribute__((ext_vector_type(2)));
typedef float f4 __attribute__((ext_vector_type(4)));
typedef short s4 __attribute__((ext_vector_type(4)));
typedef __bf16 b2 __attribute__((ext_vector_type(2)));
typedef __bf16 b8 __attribute__((ext_vector_type(8)));

constexpr int DM = 512, SK = 16384, FF = 1792, NQ = 4096;
constexpr int NCH = 32;
constexpr float L2E = 1.4426950408889634f, SCALE = 0.08838834764831845f, EPS = 1e-5f;
constexpr u32 WS_E = 0, WS_QG = 4096, WS_O = WS_QG + 32 * 16384, OREC = 2048, WS_ML = WS_O + 256 * OREC  , WS_Y = WS_ML + 256 * 128,
              WS_D = WS_Y + 8 * 65536, WS_END = WS_D + 4 * 56 * 2048, WS_DBG = WS_END;
constexpr u32 OOB = 0x80000000u;
constexpr u32 L_RING = 0, L_FLAG = 131072  , L_QRED = L_FLAG + 256  , L_QS = L_QRED + 4096,
              L_PRIV = L_QS + 1024  , L_OM = L_PRIV + 8192, L_MLS = L_OM + 8192, L_YRED = L_MLS + 128, L_HS = L_YRED + 1024,
              L_GU = L_HS + 1024, L_END = L_GU + 1024;
constexpr u32 SPIN_LDS = 1u << 19, SPIN_MEM = 1u << 16;

struct Params {
  const u16 *x, *k, *v, *wq, *wo, *wg, *wu, *wd;
  u16* out;
  unsigned char* ws;
};

#define CB() asm volatile("" ::: "memory")
__device__ __forceinline__ void wg_barrier() {
  asm volatile("s_waitcnt lgkmcnt(0)\n\ts_barrier" ::: "memory");
  __builtin_amdgcn_sched_barrier(0);
}
__device__ __forceinline__ float asf(u32 x) { return __builtin_bit_cast(float, x); }
__device__ __forceinline__ u32 asu(float x) { return __builtin_bit_cast(u32, x); }
__device__ __forceinline__ u32 pk(float lo, float hi) { f2 z = {lo, hi}; return __builtin_bit_cast(u32, __builtin_convertvector(z, b2)); }
__device__ __forceinline__ float blo(u32 p) { return asf(p << 16); }
__device__ __forceinline__ float bhi(u32 p) { return asf(p & 0xffff0000u); }
__device__ __forceinline__ float rb(float x) { return blo(pk(x, x)); }
template <int CTRL, int RM = 0xf> __device__ __forceinline__ float dpp0(float x) {
  return asf(__builtin_amdgcn_update_dpp(0, asu(x), CTRL, RM, 0xf, true));
}
template <int CTRL> __device__ __forceinline__ float dppm(float x) { return asf(__builtin_amdgcn_mov_dpp(asu(x), CTRL, 0xf, 0xf, true)); }
__device__ __forceinline__ float wsum63(float v) {
  v += dpp0<0x111>(v); v += dpp0<0x112>(v); v += dpp0<0x114>(v); v += dpp0<0x118>(v);
  v += asf(__builtin_amdgcn_update_dpp(0, asu(v), 0x142, 0xa, 0xf, false));
  v += asf(__builtin_amdgcn_update_dpp(0, asu(v), 0x143, 0xc, 0xf, false));
  return v;
}
__device__ __forceinline__ float wsum(float v) { return asf(__builtin_amdgcn_readlane(asu(wsum63(v)), 63)); }
__device__ __forceinline__ float xg_max(float x) {
  auto s = __builtin_amdgcn_permlane16_swap(asu(x), asu(x), false, false);
  x = fmaxf(asf(s[0]), asf(s[1]));
  auto t = __builtin_amdgcn_permlane32_swap(asu(x), asu(x), false, false);
  return fmaxf(asf(t[0]), asf(t[1]));
}
__device__ __forceinline__ float xg_sum(float x) {
  auto s = __builtin_amdgcn_permlane16_swap(asu(x), asu(x), false, false);
  x = asf(s[0]) + asf(s[1]);
  auto t = __builtin_amdgcn_permlane32_swap(asu(x), asu(x), false, false);
  return asf(t[0]) + asf(t[1]);
}
__device__ __forceinline__ float x32_sum(float x) {
  auto t = __builtin_amdgcn_permlane32_swap(asu(x), asu(x), false, false);
  return asf(t[0]) + asf(t[1]);
}
__device__ __forceinline__ f4 mfma32(v4u a, v4u b, f4 c) {
  return __builtin_amdgcn_mfma_f32_16x16x32_bf16(__builtin_bit_cast(b8, a), __builtin_bit_cast(b8, b), c, 0, 0, 0);
}
__device__ __forceinline__ f4 mfma16(s4 a, v2u b, f4 c) {
  return __builtin_amdgcn_mfma_f32_16x16x16bf16_1k(a, __builtin_bit_cast(s4, b), c, 0, 0, 0);
}
__device__ __forceinline__ u32 plo(u32 r1, u32 r0) { return __builtin_amdgcn_perm(r1, r0, 0x05040100u); }
__device__ __forceinline__ u32 phi(u32 r1, u32 r0) { return __builtin_amdgcn_perm(r1, r0, 0x07060302u); }
__device__ __forceinline__ void tr84(const v2u (&r)[8], v4u (&f)[4]) {
#pragma unroll
  for (int t = 0; t < 4; ++t) {
    f[0][t] = plo(r[2 * t + 1].x, r[2 * t].x); f[1][t] = phi(r[2 * t + 1].x, r[2 * t].x);
    f[2][t] = plo(r[2 * t + 1].y, r[2 * t].y); f[3][t] = phi(r[2 * t + 1].y, r[2 * t].y);
  }
}
typedef __amdgpu_buffer_rsrc_t rsrc_t;
__device__ __forceinline__ rsrc_t mkr(const void* p, u32 bytes) { return __builtin_amdgcn_make_buffer_rsrc((void*)p, 0, bytes, 0x00020000); }
__device__ __forceinline__ v4i mkr_s(const void* p, u32 bytes) {
  const u64 a = (u64)p; v4i r;
  r.x = __builtin_amdgcn_readfirstlane((u32)a); r.y = __builtin_amdgcn_readfirstlane((u32)(a >> 32)); r.z = (int)bytes; r.w = 0x00020000;
  return r;
}
constexpr int SC1 = 16, WAUX = FK_WNT ? 2 : 0;
__device__ __forceinline__ v2u ld2(rsrc_t r, u32 vo, u32 so = 0) { return __builtin_amdgcn_raw_buffer_load_b64(r, vo, so, SC1); }
__device__ __forceinline__ v4u ld4(rsrc_t r, u32 vo, u32 so = 0) { return __builtin_amdgcn_raw_buffer_load_b128(r, vo, so, SC1); }
__device__ __forceinline__ void st2(rsrc_t r, u32 vo, v2u v) { __builtin_amdgcn_raw_buffer_store_b64(v, r, vo, 0, SC1); }
__device__ __forceinline__ void st4(rsrc_t r, u32 vo, v4u v) { __builtin_amdgcn_raw_buffer_store_b128(v, r, vo, 0, SC1); }

#define STAMP(i) do {} while (0)
#define FLUSH() do {} while (0)

template <int N> __device__ __forceinline__ void pause() { if (N > 0) asm volatile("s_sleep %0" ::"i"(N) : "memory"); }
__device__ __forceinline__ float rsum16(float v) {
  v += dppm<0x128>(v); v += dppm<0x124>(v); v += dppm<0x122>(v); v += dppm<0x121>(v);
  return v;
}
struct Tag { u32 b[4]; };
__device__ __forceinline__ Tag mktag(u32 t) { return Tag{{t & 0xffu, (t >> 8) & 0xffu, (t >> 16) & 0xffu, t >> 24}}; }
__device__ __forceinline__ u32 enc(float x, u32 tb) { return ((asu(x) + 0x80u) & 0xffffff00u) | tb; }
__device__ __forceinline__ v4u enc4(f4 x, const Tag& t) { return v4u{enc(x[0], t.b[0]), enc(x[1], t.b[1]), enc(x[2], t.b[2]), enc(x[3], t.b[3])}; }
__device__ __forceinline__ float dec(u32 y) { return asf(y & 0xffffff00u); }
__device__ __forceinline__ bool bad2(v2u y, u32 t16) { return (__builtin_amdgcn_perm(y.y, y.x, 0x0c0c0400u)) != t16; }
__device__ __forceinline__ bool bad4(v4u y, u32 t32) {
  return __builtin_amdgcn_perm(__builtin_amdgcn_perm(y.w, y.z, 0x0c0c0400u), __builtin_amdgcn_perm(y.y, y.x, 0x0c0c0400u), 0x05040100u) != t32;
}
__device__ __forceinline__ bool half_any(bool x, int lane) { const u64 m = __ballot(x); return (lane < 32 ? (u32)m : (u32)(m >> 32)) != 0u; }
__device__ __forceinline__ f2 ln_stats(float piv, float s1, float s2) {
  const float md = s1 * (1.f / DM), e2 = s2 * (1.f / DM);
  const float var = rb(fmaxf(fmaf(-md, md, e2), 0.f));
  return f2{rb(piv + md), __builtin_amdgcn_rcpf(rb(__builtin_amdgcn_sqrtf(rb(var + EPS))))};
}
__device__ __forceinline__ v4u ln_apply(v4u xr, f2 st) {
  v4u o;
#pragma unroll
  for (int i = 0; i < 4; ++i) { const u32 d = pk(blo(xr[i]) - st.x, bhi(xr[i]) - st.x); o[i] = pk(blo(d) * st.y, bhi(d) * st.y); }
  return o;
}
__device__ __forceinline__ v4u layernorm8(v4u xr) {
  const float piv = asf(__builtin_amdgcn_readfirstlane(xr[0] << 16));
  float s1 = 0.f, s2 = 0.f;
#pragma unroll
  for (int i = 0; i < 4; ++i) { const float d0 = blo(xr[i]) - piv, d1 = bhi(xr[i]) - piv; s1 += d0 + d1; s2 = fmaf(d0, d0, fmaf(d1, d1, s2)); }
  s1 = wsum63(s1); s2 = wsum63(s2);
  return ln_apply(xr, ln_stats(piv, asf(__builtin_amdgcn_readlane(asu(s1), 63)), asf(__builtin_amdgcn_readlane(asu(s2), 63))));
}

#define FK_PIECE(T, VO) \
  "s_waitcnt vmcnt(%c[wm])\n\t" \
  ".if %c[pt] == " #T "\n\tds_write_b32 %[fa], %[fv]\n\t.endif\n\t" \
  ".if %c[nt]\n\tbuffer_load_dwordx4 " VO ", %[rs], %[so] offen offset:" #T "*1024 nt lds\n\t.else\n\tbuffer_load_dwordx4 " VO ", %[rs], %[so] offen offset:" #T "*1024 lds\n\t.endif\n\t"
template <int W, int NT>
__device__ __forceinline__ void dma_tile(v4i rs, u32 so, u32 la, u32 v0, u32 v1, u32 v2, u32 v3, u32 fa, int tn) {
  const int landed = tn - 1 - (W - 5) / 4;
  asm volatile("s_mov_b32 m0, %[la]\n\t" FK_PIECE(0, "%[v0]") FK_PIECE(1, "%[v1]") FK_PIECE(2, "%[v2]") FK_PIECE(3, "%[v3]")
               :
               : [rs] "s"(rs), [so] "s"(so), [la] "s"(la), [v0] "v"(v0), [v1] "v"(v1), [v2] "v"(v2), [v3] "v"(v3), [fa] "v"(fa),
                 [fv] "v"(landed > 0 ? landed : 0), [wm] "i"(W - 1), [pt] "i"((W - 5) % 4), [nt] "i"(NT)
               : "memory");
}
template <int NT>
__device__ __forceinline__ void dma_adapt(int lag, v4i rs, u32 so, u32 la, u32 v0, u32 v1, u32 v2, u32 v3, u32 fa, int tn) {
  if (lag <= 0) dma_tile<FK_W, NT>(rs, so, la, v0, v1, v2, v3, fa, tn);
  else if (lag <= FK_LG) dma_tile<FK_WB, NT>(rs, so, la, v0, v1, v2, v3, fa, tn);
  else dma_tile<FK_WC, NT>(rs, so, la, v0, v1, v2, v3, fa, tn);
}
__device__ __forceinline__ bool lds_wait_ge(volatile LAS u32* w, u32 need, bool& dead) {
  for (u32 n = 0;; ++n) {
    if ((u32)__builtin_amdgcn_readfirstlane(*w) >= need) break;
    if (dead || n > SPIN_LDS) { dead = true; break; }
    __builtin_amdgcn_s_sleep(1);
  }
  CB();
  return true;
}

__global__ void __launch_bounds__(1024, 4) fused_block(Params p) {
  __shared__ __attribute__((aligned(1024))) unsigned char lds_raw[L_END];
  LAS unsigned char* L = (LAS unsigned char*)lds_raw;
  const int tid = threadIdx.x, lane = tid & 63, wave = __builtin_amdgcn_readfirstlane(tid >> 6), w = wave & 3, role = wave >> 2;
  const int g = lane >> 4, c = lane & 15;
  const int kvh = blockIdx.x & 7, b = blockIdx.x >> 6, s = (blockIdx.x >> 3) & 7, grp = b * 8 + kvh;
  const rsrc_t rw = mkr(p.ws, WS_END);
  volatile LAS u32* FLAG = (volatile LAS u32*)(L + L_FLAG);
  const bool ffn = s < 7;
  const int j = kvh * 7 + s;
  bool dead = false;
  STAMP(0);

  if (role == 1) {
    __builtin_amdgcn_s_setprio(3);
    const u32 tb = (u32)__builtin_amdgcn_s_memrealtime() + FK_T0;
#define DIAG(n) do {} while (0)
    const v4i rk = mkr_s(p.k + (size_t)grp * SK * 128, SK * 256), rv = mkr_s(p.v + (size_t)grp * SK * 128, SK * 256);
    const u32 k0 = g * 256 + ((c ^ g) << 4), k1 = g * 256 + ((c ^ (4 + g)) << 4), k2 = g * 256 + ((c ^ (8 + g)) << 4), k3 = g * 256 + ((c ^ (12 + g)) << 4);
    const u32 u0 = g * 256 + ((c ^ (g << 1)) << 4), u1 = g * 256 + ((c ^ ((4 + g) << 1)) << 4);
    const u32 ring = (u32)(size_t)(L + L_RING) + w * 32768, fa = (u32)(size_t)(FLAG + w);
    STAMP(5);
    if (lane < 4) FLAG[w + 4 * lane] = 0u;
    wg_barrier();
    STAMP(7);
    dma_tile<FK_W, FK_KNT>(rk, (u32)(s * 4 + w) * 4096u, ring, k0, k1, k2, k3, fa, 0);
    STAMP(6);
    dma_tile<FK_W, FK_VNT>(rv, (u32)(s * 4 + w) * 4096u, ring + 4096, u0, u1, u0, u1, fa, 1);
    STAMP(8);
    DIAG(1);
#pragma unroll 1
    for (int i = 1; i < NCH; ++i) {
      const u32 so = (u32)((i * 8 + s) * 4 + w) * 4096u, la = ring + (i & 3) * 8192;
      if (i >= 4) lds_wait_ge(FLAG + 4 + w, i - 3, dead);
      const int lag = (int)((u32)__builtin_amdgcn_s_memrealtime() - tb) - ((i * FK_TAU) >> 4);
      dma_adapt<FK_KNT>(lag, rk, so, la, k0, k1, k2, k3, fa, 2 * i);
      dma_adapt<FK_VNT>(lag, rv, so, la + 4096, u0, u1, u0, u1, fa, 2 * i + 1);
    }
    asm volatile("s_waitcnt vmcnt(8)\n\tds_write_b32 %0, %1\n\ts_waitcnt vmcnt(4)\n\tds_write_b32 %0, %2\n\ts_waitcnt vmcnt(0)\n\tds_write_b32 %0, %3" ::"v"(fa),
                 "v"(2 * NCH - 2), "v"(2 * NCH - 1), "v"(2 * NCH) : "memory");
    STAMP(1);
    DIAG(2);
    __builtin_amdgcn_s_setprio(0);
    v4u dr[8], df[8];
    const rsrc_t rd = mkr(p.wd, FF * DM * 2);
#pragma unroll
    for (int r = 0; r < 8; ++r) dr[r] = __builtin_amdgcn_raw_buffer_load_b128(rd, ffn ? (8 * g) * 1024 + 16 * c : OOB, (32 * j + r) * 1024 + 256 * w, WAUX);
    wg_barrier();
    const Tag T = mktag(FLAG[12]);
    LAS unsigned char* priv = L + L_QRED + w * 1024;
#pragma unroll
    for (int t = 0; t < 4; ++t)
#pragma unroll
      for (int q = 0; q < 4; ++q) { df[2 * q][t] = plo(dr[2 * t + 1][q], dr[2 * t][q]); df[2 * q + 1][t] = phi(dr[2 * t + 1][q], dr[2 * t][q]); }
    asm volatile("" : "+v"(df[0]), "+v"(df[1]), "+v"(df[2]), "+v"(df[3]), "+v"(df[4]), "+v"(df[5]), "+v"(df[6]), "+v"(df[7]));
    STAMP(2);
    wg_barrier();
    wg_barrier();
    wg_barrier();
    STAMP(3);
    if (ffn) {
      const LAS float* gu = (const LAS float*)(L + L_GU) + (lane & 31);
      const float gt = rb((gu[0] + gu[64]) + (gu[128] + gu[192])), up = rb((gu[32] + gu[96]) + (gu[160] + gu[224]));
      const float sg = rb(gt * __builtin_amdgcn_rcpf(1.f + __builtin_amdgcn_exp2f(-gt * L2E)));
      ((LAS u16*)priv)[lane & 31] = (u16)pk(sg * up, 0.f);
      const v4u a = *(const LAS v4u*)(priv + 16 * g);
      f4 o[8];
#pragma unroll
      for (int i = 0; i < 8; ++i) o[i] = mfma32(a, df[i], f4{0.f, 0.f, 0.f, 0.f});
      const bool hi = g & 1;
      const v4u ov = enc4(f4{hi ? o[4][0] : o[0][0], hi ? o[5][0] : o[1][0], hi ? o[6][0] : o[2][0], hi ? o[7][0] : o[3][0]}, T);
      st4(rw, lane < 32 ? WS_D + (u32)(b * 56 + j) * 2048 + (u32)(128 * w + 8 * c + 4 * g) * 4 : OOB, ov);
    }
    STAMP(4);
    FLUSH();
    return;
  }

  if (role == 2) {
    LAS unsigned char* priv = L + L_PRIV + w * 1024;
    wg_barrier();
    pause<FK_W0>(); pause<FK_W0>();
    v2u wor[4][8];
    const rsrc_t ro = mkr(p.wo, NQ * DM * 2);
#pragma unroll
    for (int u = 0; u < 4; ++u)
#pragma unroll
      for (int r = 0; r < 8; ++r) {
        wor[u][r] = __builtin_amdgcn_raw_buffer_load_b64(ro, g * 8 * 1024 + 8 * c, (kvh * 512 + 128 * w + 32 * u + r) * 1024 + 128 * s, WAUX);
        pause<FK_P1>();
      }
    v4u wf[4][4];
#pragma unroll
    for (int u = 0; u < 4; ++u) { tr84(wor[u], wf[u]); asm volatile("" : "+v"(wf[u][0]), "+v"(wf[u][1]), "+v"(wf[u][2]), "+v"(wf[u][3])); }
    lds_wait_ge(FLAG + 9, 4, dead);
    const u32 tg = FLAG[12];
    const Tag T = mktag(tg);
    STAMP(1);
    wg_barrier();
    {
      const u32 ob = WS_O + grp * 8 * OREC, om = WS_ML + (grp * 8 + (lane & 7)) * 128 + w * 16, oo = ob + (lane >> 5) * OREC + w * 512 + (lane & 31) * 16;
      pause<FK_D1>();
      STAMP(2);
      v4u o[4], ml;
      ml = ld4(rw, om);
      for (u32 n = 0; __any(bad4(ml, tg)); ++n) {
        if (dead || n > SPIN_MEM) { dead = true; break; }
        pause<FK_BO>();
        ml = ld4(rw, om);
      }
      STAMP(7);
#pragma unroll
      for (int t = 0; t < 4; ++t) o[t] = ld4(rw, oo, ((t + s) & 3) * 2 * OREC);
      float M = dec(ml.x);
      M = fmaxf(M, dppm<0xB1>(M)); M = fmaxf(M, dppm<0x4E>(M)); M = fmaxf(M, dppm<0x141>(M));
      const float e = __builtin_amdgcn_exp2f((dec(ml.x) - M) * L2E);
      float Ls = e * dec(ml.y);
      Ls += dppm<0xB1>(Ls); Ls += dppm<0x4E>(Ls); Ls += dppm<0x141>(Ls);
      const float en = e * __builtin_amdgcn_rcpf(Ls);
      float et[4];
#pragma unroll
      for (int t = 0; t < 4; ++t) {
        const float e0 = asf(__builtin_amdgcn_readlane(asu(en), 2 * ((t + s) & 3))), e1 = asf(__builtin_amdgcn_readlane(asu(en), 2 * ((t + s) & 3) + 1));
        et[t] = lane < 32 ? e0 : e1;
      }
      asm volatile("" : "+v"(et[0]), "+v"(et[1]), "+v"(et[2]), "+v"(et[3]));
      for (u32 n = 0;; ++n) {
        bool bad = false;
#pragma unroll
        for (int t = 0; t < 4; ++t) bad = bad || bad4(o[t], tg);
        if (!__any(bad)) break;
        if (dead || n > SPIN_MEM) { dead = true; break; }
        pause<FK_BO>();
#pragma unroll
        for (int t = 0; t < 4; ++t) if (half_any(bad4(o[t], tg), lane)) o[t] = ld4(rw, oo, ((t + s) & 3) * 2 * OREC);
      }
      STAMP(3);
      f4 os = {0.f, 0.f, 0.f, 0.f};
#pragma unroll
      for (int t = 0; t < 4; ++t)
#pragma unroll
        for (int i = 0; i < 4; ++i) os[i] = fmaf(et[t], dec(o[t][i]), os[i]);
#pragma unroll
      for (int i = 0; i < 4; ++i) os[i] = x32_sum(os[i]);
      ((LAS v2u*)priv)[lane & 31] = v2u{pk(os[0], os[1]), pk(os[2], os[3])};
      f4 y[4];
#pragma unroll
      for (int i = 0; i < 4; ++i) y[i] = f4{0.f, 0.f, 0.f, 0.f};
#pragma unroll
      for (int u = 0; u < 4; ++u) {
        const v4u a = *(const LAS v4u*)(priv + (32 * u + 8 * g) * 2);
#pragma unroll
        for (int i = 0; i < 4; ++i) y[i] = mfma32(a, wf[u][i], y[i]);
      }
      if (lane < 16) *(LAS f4*)(L + L_YRED + w * 256 + c * 16) = f4{y[0][0], y[1][0], y[2][0], y[3][0]};
    }
    wg_barrier();
    {
      const LAS f4* yr = (const LAS f4*)(L + L_YRED) + c;
      const f4 y = (yr[0] + yr[16]) + (yr[32] + yr[48]);
      st4(rw, lane < 32 ? WS_Y + (u32)(2 * w + g) * 65536 + grp * 2048 + s * 256 + c * 16 : OOB, enc4(y, T));
    }
    STAMP(4);
    wg_barrier();
    wg_barrier();
    STAMP(6);
    FLUSH();
    return;
  }

  if (role == 3) {
    LAS unsigned char* priv = L + L_PRIV + 4096 + w * 1024;
    wg_barrier();
    pause<FK_W0>(); pause<FK_W0>();
    const v2u xp = *(const GAS v2u*)(p.x + b * DM + 128 * w + 4 * (lane & 31));
    v2u gr[4][8];
    v4u gf[4][4];
    if (ffn) {
      const GAS unsigned char* gp = (const GAS unsigned char*)(c < 8 ? p.wg : p.wu) + (size_t)(128 * w + 8 * g) * (FF * 2) + 64 * j + 8 * (c & 7);
#pragma unroll
      for (int u = 0; u < 4; ++u)
#pragma unroll
        for (int r = 0; r < 8; ++r) {
          const GAS v2u* a = (const GAS v2u*)(gp + (size_t)(32 * u + r) * (FF * 2));
          gr[u][r] = FK_WNT ? __builtin_nontemporal_load(a) : *a;
          pause<FK_P2>();
        }
#pragma unroll
      for (int u = 0; u < 4; ++u) { tr84(gr[u], gf[u]); asm volatile("" : "+v"(gf[u][0]), "+v"(gf[u][1]), "+v"(gf[u][2]), "+v"(gf[u][3])); }
    }
    lds_wait_ge(FLAG + 9, 4, dead);
    const u32 tg = FLAG[12];
    if (blockIdx.x == 0) {
      const u32 tlo = FLAG[10];
      for (u32 n = 0;; ++n) {
        const v2u gv = ld2(rw, WS_QG + (u32)(lane >> 3) * 16384 + w * 4096 + (lane & 7) * 512);
        if (__all(gv.y == tlo)) break;
        if (dead || n > SPIN_MEM) { dead = true; break; }
        pause<8>();
      }
    }
    STAMP(1);
    wg_barrier();
    wg_barrier();
    {
      const u32 yb = WS_Y + (u32)kvh * 65536 + (u32)b * 8 * 2048 + (lane >> 5) * 2048 + w * 512 + (lane & 31) * 16;
      pause<FK_D2>();
      STAMP(2);
      v4u y[4];
#pragma unroll
      for (int k = 0; k < 4; ++k) y[k] = ld4(rw, yb, ((k + s) & 3) * 4096);
      for (u32 n = 0;; ++n) {
        bool bad = false;
#pragma unroll
        for (int k = 0; k < 4; ++k) bad = bad || bad4(y[k], tg);
        if (!__any(bad)) break;
        if (dead || n > SPIN_MEM) { dead = true; break; }
        pause<FK_BO>();
#pragma unroll
        for (int k = 0; k < 4; ++k) if (half_any(bad4(y[k], tg), lane)) y[k] = ld4(rw, yb, ((k + s) & 3) * 4096);
      }
      STAMP(3);
      f4 a;
#pragma unroll
      for (int i = 0; i < 4; ++i) {
        const float p0 = dec(y[0][i]), p1 = dec(y[1][i]), p2 = dec(y[2][i]), p3 = dec(y[3][i]);
        a[i] = x32_sum((s & 1) ? (p3 + p0) + (p1 + p2) : (p0 + p1) + (p2 + p3));
      }
      const u32 a0 = pk(a[0], a[1]), a1 = pk(a[2], a[3]);
      ((LAS v2u*)(L + L_HS))[32 * w + (lane & 31)] = v2u{pk(blo(xp.x) + blo(a0), bhi(xp.x) + bhi(a0)), pk(blo(xp.y) + blo(a1), bhi(xp.y) + bhi(a1))};
    }
    wg_barrier();
    STAMP(4);
    if (ffn) {
      const v4u hn = layernorm8(*(const LAS v4u*)(L + L_HS + lane * 16));
      *(LAS v4u*)(priv + lane * 16) = hn;
      f4 acc[4];
#pragma unroll
      for (int i = 0; i < 4; ++i) acc[i] = f4{0.f, 0.f, 0.f, 0.f};
#pragma unroll
      for (int u = 0; u < 4; ++u) {
        const v4u a = *(const LAS v4u*)(priv + (128 * w + 32 * u + 8 * g) * 2);
#pragma unroll
        for (int i = 0; i < 4; ++i) acc[i] = mfma32(a, gf[u][i], acc[i]);
      }
      if (lane < 16) *(LAS f4*)(L + L_GU + w * 256 + c * 16) = f4{acc[0][0], acc[1][0], acc[2][0], acc[3][0]};
    }
    STAMP(5);
    wg_barrier();
    if (blockIdx.x == 0 && w == 0) st2(rw, lane == 0 ? WS_E : OOB, v2u{FLAG[10], FLAG[11]});
    FLUSH();
    return;
  }

  v4u qf[4];
  u32 tg;
  {
    STAMP(6);
    v4u x4[4];
#pragma unroll
    for (int t = 0; t < 4; ++t) x4[t] = *(const GAS v4u*)(p.x + g * DM + 128 * t + 8 * c);
    const v4u xa = *(const GAS v4u*)(p.x + (c & 3) * DM + 128 * b + 32 * w + 8 * g);
    const rsrc_t rq = mkr(p.wq, DM * NQ * 2);
    v2u wr[8];
#pragma unroll
    for (int r = 0; r < 8; ++r)
      wr[r] = __builtin_amdgcn_raw_buffer_load_b64(rq, g * 8 * 8192 + 8 * c, (128 * b + 32 * w + r) * 8192 + (kvh * 512 + 64 * s) * 2, 2);
    const v2u e = ld2(rw, WS_E);
    wg_barrier();
    STAMP(10);
    const float piv = dppm<0x150>(blo(x4[0][0]));
    float s1 = 0.f, s2 = 0.f;
#pragma unroll
    for (int t = 0; t < 4; ++t)
#pragma unroll
      for (int i = 0; i < 4; ++i) { const float d0 = blo(x4[t][i]) - piv, d1 = bhi(x4[t][i]) - piv; s1 += d0 + d1; s2 = fmaf(d0, d0, fmaf(d1, d1, s2)); }
    const f2 st = ln_stats(piv, rsum16(s1), rsum16(s2));
    const f2 sa = {asf(__builtin_amdgcn_ds_bpermute((c & 3) * 64, asu(st.x))), asf(__builtin_amdgcn_ds_bpermute((c & 3) * 64, asu(st.y)))};
    const v4u a = ln_apply(xa, sa);
    v4u f[4];
    tr84(wr, f);
    f4 acc[4];
#pragma unroll
    for (int i = 0; i < 4; ++i) acc[i] = mfma32(a, f[i], f4{0.f, 0.f, 0.f, 0.f});
    if (lane < 16) {
#pragma unroll
      for (int r = 0; r < 4; ++r) *(LAS f4*)(L + L_QRED + w * 1024 + r * 256 + c * 16) = f4{acc[0][r], acc[1][r], acc[2][r], acc[3][r]};
    }
    const u64 epoch = ((u64)__builtin_amdgcn_readfirstlane(e.y) << 32 | __builtin_amdgcn_readfirstlane(e.x)) + 1;
    const u32 tlo = (u32)epoch, thi = (u32)(epoch >> 32) + tlo * 0x9E3779B1u;
    const u32 dl = tlo - (u32)(epoch >> 32);
    tg = ((tlo + 1u) & 0xffu) | (((tlo >> 8) + 3u * dl + 1u) & 0xffu) << 8 | (((tlo >> 16) + 5u * dl + 1u) & 0xffu) << 16 | ((tlo >> 24) + 7u * dl + 1u) << 24;
    if (w == 0 && lane == 0) { FLAG[10] = tlo; FLAG[11] = (u32)(epoch >> 32); FLAG[12] = tg; }
    asm volatile("s_waitcnt lgkmcnt(0)" ::: "memory");
    STAMP(11);
    u32 old = 0;
    if (lane == 0) old = __hip_atomic_fetch_add((LAS u32*)(L + L_FLAG) + 8, 1u, __ATOMIC_RELAXED, __HIP_MEMORY_SCOPE_WORKGROUP);
    old = __builtin_amdgcn_readfirstlane(old);
    CB();
    if (old == 3) {
      const LAS f2* qr = (const LAS f2*)(L + L_QRED) + g * 32 + c;
      const f2 q0 = (qr[0] + qr[128]) + (qr[256] + qr[384]), q1 = (qr[16] + qr[144]) + (qr[272] + qr[400]);
      const u32 off = WS_QG + (u32)(g * 8 + kvh) * 16384 + b * 4096 + s * 512 + c * 16;
      st4(rw, off, v4u{asu(q0.x), tlo, asu(q0.y), thi});
      st4(rw, off + 256, v4u{asu(q1.x), tlo, asu(q1.y), thi});
    }
    STAMP(1);
    pause<FK_D0>();
    v4u gq[4];
    const u32 qo = WS_QG + grp * 16384 + w * 1024 + lane * 16;
#pragma unroll
    for (int t = 0; t < 4; ++t) gq[t] = ld4(rw, qo, t * 4096);
    for (u32 n = 0;; ++n) {
      bool bad = false;
#pragma unroll
      for (int t = 0; t < 4; ++t) bad = bad || gq[t].y != tlo || gq[t].w != thi;
      if (!__any(bad)) break;
      if (dead || n > SPIN_MEM) { dead = true; break; }
      pause<2>();
#pragma unroll
      for (int t = 0; t < 4; ++t) if (__any(gq[t].y != tlo || gq[t].w != thi)) gq[t] = ld4(rw, qo, t * 4096);
    }
    STAMP(12);
    ((LAS u32*)(L + L_QS))[64 * w + lane] = pk((asf(gq[0].x) + asf(gq[1].x)) + (asf(gq[2].x) + asf(gq[3].x)), (asf(gq[0].z) + asf(gq[1].z)) + (asf(gq[2].z) + asf(gq[3].z)));
    asm volatile("s_waitcnt lgkmcnt(0)" ::: "memory");
    if (lane == 0) __hip_atomic_fetch_add((LAS u32*)(L + L_FLAG) + 9, 1u, __ATOMIC_RELAXED, __HIP_MEMORY_SCOPE_WORKGROUP);
    lds_wait_ge(FLAG + 9, 4, dead);
#pragma unroll
    for (int u = 0; u < 4; ++u) qf[u] = *(const LAS v4u*)(L + L_QS + ((c & 3) * 128 + 32 * u + 8 * g) * 2);
    STAMP(2);
  }
  float m = -INFINITY, l = 0.f;
  f4 acc[8];
#pragma unroll
  for (int i = 0; i < 8; ++i) acc[i] = f4{0.f, 0.f, 0.f, 0.f};
  const LAS unsigned char* ringk = L + L_RING + w * 32768 + c * 256;
  const u32 vrow = 4 * g + (c >> 2);
  const u32 va = (u32)(size_t)(L + L_RING) + w * 32768 + 4096 + vrow * 256 + (vrow & 7) * 32 + 8 * (c & 3);
  u32 seen = 0;
  auto need = [&](u32 n) {
    for (u32 k = 0; seen < n; ++k) {
      seen = __builtin_amdgcn_readfirstlane(FLAG[w]);
      if (seen >= n) break;
      if (dead || k > SPIN_LDS) { dead = true; break; }
      __builtin_amdgcn_s_sleep(1);
    }
    CB();
  };
#pragma unroll 1
  for (int i = 0; i < NCH; ++i) {
    const u32 sl = (i & 3) * 8192;
    need(2 * i + 1);
    v4u kf[4];
#pragma unroll
    for (int u = 0; u < 4; ++u) kf[u] = *(const LAS v4u*)(ringk + sl + (((4 * u + g) ^ c) << 4));
    f4 s0 = mfma32(kf[0], qf[0], f4{0.f, 0.f, 0.f, 0.f}), s1 = mfma32(kf[2], qf[2], f4{0.f, 0.f, 0.f, 0.f});
    s0 = mfma32(kf[1], qf[1], s0); s1 = mfma32(kf[3], qf[3], s1);
    const u32 a01 = pk(s0[0] + s1[0], s0[1] + s1[1]), a23 = pk(s0[2] + s1[2], s0[3] + s1[3]);
    const u32 b01 = pk(blo(a01) * SCALE, bhi(a01) * SCALE), b23 = pk(blo(a23) * SCALE, bhi(a23) * SCALE);
    const float t0 = blo(b01), t1 = bhi(b01), t2 = blo(b23), t3 = bhi(b23);
    const float mx = xg_max(fmaxf(fmaxf(t0, t1), fmaxf(t2, t3)));
    if (__any(mx > m)) {
      const float mn = fmaxf(m, mx), al = __builtin_amdgcn_exp2f((m - mn) * L2E);
      l *= al; m = mn;
#pragma unroll
      for (int d = 0; d < 8; ++d) acc[d] *= al;
    }
    const float p0 = __builtin_amdgcn_exp2f((t0 - m) * L2E), p1 = __builtin_amdgcn_exp2f((t1 - m) * L2E),
                p2 = __builtin_amdgcn_exp2f((t2 - m) * L2E), p3 = __builtin_amdgcn_exp2f((t3 - m) * L2E);
    l += (p0 + p1) + (p2 + p3);
    const v2u pf = {pk(p0, p1), pk(p2, p3)};
    if (i == NCH - 1) {
      const float lt = xg_sum(l);
      if (lane < 4) *(LAS f2*)(L + L_MLS + w * 32 + c * 8) = f2{m, lt};
    }
    need(2 * i + 2);
    s4 vf[8];
#pragma unroll
    for (int d = 0; d < 8; ++d) vf[d] = __builtin_amdgcn_ds_read_tr16_b64_v4i16((LAS s4*)(size_t)((va ^ (d << 5)) + sl));
    asm volatile("s_waitcnt lgkmcnt(0)" ::: "memory");
    FLAG[4 + w] = i + 1;
#pragma unroll
    for (int d = 0; d < 8; ++d) acc[d] = mfma16(vf[d], pf, acc[d]);
  }
  STAMP(4);
  if (c < 4) {
#pragma unroll
    for (int d = 0; d < 8; ++d) *(LAS f4*)(L + L_OM + w * 2048 + c * 512 + (16 * d + 4 * g) * 4) = acc[d];
  }
  wg_barrier();
  {
    const int hd = lane < 32 ? w : (lane & 3);
    f2 ml[4]; f4 ov[4];
#pragma unroll
    for (int k = 0; k < 4; ++k) { ml[k] = *(const LAS f2*)(L + L_MLS + k * 32 + hd * 8); ov[k] = *(const LAS f4*)(L + L_OM + k * 2048 + w * 512 + (lane & 31) * 16); }
    const float M = fmaxf(fmaxf(ml[0].x, ml[1].x), fmaxf(ml[2].x, ml[3].x));
    float Ls = 0.f; f4 O = {0.f, 0.f, 0.f, 0.f};
#pragma unroll
    for (int k = 0; k < 4; ++k) { const float e = __builtin_amdgcn_exp2f((ml[k].x - M) * L2E); Ls = fmaf(e, ml[k].y, Ls); O += ov[k] * e; }
    const u32 wgi = grp * 8 + s;
    st4(rw, lane < 32 ? WS_O + wgi * OREC + w * 512 + lane * 16 : (lane < 40 && w == 0 ? WS_ML + wgi * 128 + (lane - 32) * 16 : OOB), enc4(lane < 32 ? O : f4{M, Ls, M, Ls}, mktag(tg)));
  }
  STAMP(5);
  wg_barrier();
  wg_barrier();
  wg_barrier();
    if (w < 2) {
      const int col = (kvh * 8 + s) * 8 + 4 * w;
      const u32 off = lane < 56 ? WS_D + (u32)(b * 56 + lane) * 2048 + col * 4 : OOB;
      const v2u hv = *(const LAS v2u*)(L + L_HS + col * 2);
      const u32 tgl = lane < 56 ? tg : 0u;
      pause<FK_D3>();
      STAMP(7);
      v4u d0 = ld4(rw, off);
      for (u32 n = 0; __any(bad4(d0, tgl)); ++n) {
        if (dead || n > SPIN_MEM) { dead = true; break; }
        pause<FK_BO>();
        d0 = ld4(rw, off);
      }
      STAMP(8);
      const u32 a0 = pk(wsum63(dec(d0[0])), wsum63(dec(d0[1]))), a1 = pk(wsum63(dec(d0[2])), wsum63(dec(d0[3])));
      if (lane == 63) *(GAS v2u*)(p.out + b * DM + col) = v2u{pk(blo(hv.x) + blo(a0), bhi(hv.x) + bhi(a0)), pk(blo(hv.y) + blo(a1), bhi(hv.y) + bhi(a1))};
      STAMP(9);
    }
  FLUSH();
}
}

extern "C" void kernel_launch(void* const* d_in, const int* in_sizes, int n_in, void* d_out, int out_size, void* d_ws, size_t ws_size,
                              hipStream_t stream) {
  fk::Params p;
  p.x = (const fk::u16*)d_in[0]; p.k = (const fk::u16*)d_in[1]; p.v = (const fk::u16*)d_in[2];
  p.wq = (const fk::u16*)d_in[3]; p.wo = (const fk::u16*)d_in[4];
  p.wg = (const fk::u16*)d_in[6]; p.wu = (const fk::u16*)d_in[7]; p.wd = (const fk::u16*)d_in[8];
  p.out = (fk::u16*)d_out; p.ws = (unsigned char*)d_ws;
  hipLaunchKernelGGL(fk::fused_block, dim3(256), dim3(1024), 0, stream, p);
}
```

```cpp
#include <hip/hip_runtime.h>
#include <stdint.h>

#define FK_W 5
#define FK_WNT 1
#define FK_W0 120
#define FK_P1 18
#define FK_P2 22
#define FK_ORD 1
#define FK_G0 0
#define FK_WB 7
#define FK_WC 10
#define FK_LG 60
#define FK_T0 80
#define FK_TAU 1830
#define FK_DIAG 4
#define FK_DIAGB 0
#define FK_DIAGK 10
#define FK_DB2 0
#define FK_KNT 1
#define FK_VNT 1
#define FK_BO 1
#define FK_D0 40
#define FK_D1 18
#define FK_D2 16
#define FK_D3 32
#define FK_STAMPS 0

namespace fk {
#define GAS __attribute__((address_space(1)))
#define LAS __attribute__((address_space(3)))
typedef unsigned u32;
typedef unsigned long long u64;
typedef unsigned short u16;
typedef u32 v2u __attribute__((ext_vector_type(2)));
typedef u32 v4u __attribute__((ext_vector_type(4)));
typedef int v4i __attribute__((ext_vector_type(4)));
typedef float f2 __attribute__((ext_vector_type(2)));
typedef float f4 __attribute__((ext_vector_type(4)));
typedef short s4 __attribute__((ext_vector_type(4)));
typedef __bf16 b2 __attribute__((ext_vector_type(2)));
typedef __bf16 b8 __attribute__((ext_vector_type(8)));

constexpr int DM = 512, SK = 16384, FF = 1792, NQ = 4096;
constexpr int NCH = 32;
constexpr float L2E = 1.4426950408889634f, SCALE = 0.08838834764831845f, EPS = 1e-5f;
constexpr u32 WS_E = 0, WS_QG = 4096, WS_O = WS_QG + 32 * 16384, OREC = 2048, WS_ML = WS_O + 256 * OREC  , WS_Y = WS_ML + 256 * 128,
              WS_D = WS_Y + 8 * 65536, WS_END = WS_D + 4 * 56 * 2048, WS_DBG = WS_END;
constexpr u32 OOB = 0x80000000u;
constexpr u32 L_RING = 0, L_FLAG = 131072  , L_QRED = L_FLAG + 256  , L_QS = L_QRED + 4096,
              L_PRIV = L_QS + 1024  , L_OM = L_PRIV + 8192, L_MLS = L_OM + 8192, L_YRED = L_MLS + 128, L_HS = L_YRED + 1024,
              L_GU = L_HS + 1024, L_END = L_GU + 1024;
constexpr u32 SPIN_LDS = 1u << 19, SPIN_MEM = 1u << 16;

struct Params {
  const u16 *x, *k, *v, *wq, *wo, *wg, *wu, *wd;
  u16* out;
  unsigned char* ws;
};

#define CB() asm volatile("" ::: "memory")
__device__ __forceinline__ void wg_barrier() {
  asm volatile("s_waitcnt lgkmcnt(0)\n\ts_barrier" ::: "memory");
  __builtin_amdgcn_sched_barrier(0);
}
__device__ __forceinline__ float asf(u32 x) { return __builtin_bit_cast(float, x); }
__device__ __forceinline__ u32 asu(float x) { return __builtin_bit_cast(u32, x); }
__device__ __forceinline__ u32 pk(float lo, float hi) { f2 z = {lo, hi}; return __builtin_bit_cast(u32, __builtin_convertvector(z, b2)); }
__device__ __forceinline__ float blo(u32 p) { return asf(p << 16); }
__device__ __forceinline__ float bhi(u32 p) { return asf(p & 0xffff0000u); }
__device__ __forceinline__ float rb(float x) { return blo(pk(x, x)); }
template <int CTRL, int RM = 0xf> __device__ __forceinline__ float dpp0(float x) {
  return asf(__builtin_amdgcn_update_dpp(0, asu(x), CTRL, RM, 0xf, true));
}
template <int CTRL> __device__ __forceinline__ float dppm(float x) { return asf(__builtin_amdgcn_mov_dpp(asu(x), CTRL, 0xf, 0xf, true)); }
__device__ __forceinline__ float wsum63(float v) {
  v += dpp0<0x111>(v); v += dpp0<0x112>(v); v += dpp0<0x114>(v); v += dpp0<0x118>(v);
  v += asf(__builtin_amdgcn_update_dpp(0, asu(v), 0x142, 0xa, 0xf, false));
  v += asf(__builtin_amdgcn_update_dpp(0, asu(v), 0x143, 0xc, 0xf, false));
  return v;
}
__device__ __forceinline__ float wsum(float v) { return asf(__builtin_amdgcn_readlane(asu(wsum63(v)), 63)); }
__device__ __forceinline__ float xg_max(float x) {
  auto s = __builtin_amdgcn_permlane16_swap(asu(x), asu(x), false, false);
  x = fmaxf(asf(s[0]), asf(s[1]));
  auto t = __builtin_amdgcn_permlane32_swap(asu(x), asu(x), false, false);
  return fmaxf(asf(t[0]), asf(t[1]));
}
__device__ __forceinline__ float xg_sum(float x) {
  auto s = __builtin_amdgcn_permlane16_swap(asu(x), asu(x), false, false);
  x = asf(s[0]) + asf(s[1]);
  auto t = __builtin_amdgcn_permlane32_swap(asu(x), asu(x), false, false);
  return asf(t[0]) + asf(t[1]);
}
__device__ __forceinline__ float x32_sum(float x) {
  auto t = __builtin_amdgcn_permlane32_swap(asu(x), asu(x), false, false);
  return asf(t[0]) + asf(t[1]);
}
__device__ __forceinline__ f4 mfma32(v4u a, v4u b, f4 c) {
  return __builtin_amdgcn_mfma_f32_16x16x32_bf16(__builtin_bit_cast(b8, a), __builtin_bit_cast(b8, b), c, 0, 0, 0);
}
__device__ __forceinline__ f4 mfma16(s4 a, v2u b, f4 c) {
  return __builtin_amdgcn_mfma_f32_16x16x16bf16_1k(a, __builtin_bit_cast(s4, b), c, 0, 0, 0);
}
__device__ __forceinline__ u32 plo(u32 r1, u32 r0) { return __builtin_amdgcn_perm(r1, r0, 0x05040100u); }
__device__ __forceinline__ u32 phi(u32 r1, u32 r0) { return __builtin_amdgcn_perm(r1, r0, 0x07060302u); }
__device__ __forceinline__ void tr84(const v2u (&r)[8], v4u (&f)[4]) {
#pragma unroll
  for (int t = 0; t < 4; ++t) {
    f[0][t] = plo(r[2 * t + 1].x, r[2 * t].x); f[1][t] = phi(r[2 * t + 1].x, r[2 * t].x);
    f[2][t] = plo(r[2 * t + 1].y, r[2 * t].y); f[3][t] = phi(r[2 * t + 1].y, r[2 * t].y);
  }
}
typedef __amdgpu_buffer_rsrc_t rsrc_t;
__device__ __forceinline__ rsrc_t mkr(const void* p, u32 bytes) { return __builtin_amdgcn_make_buffer_rsrc((void*)p, 0, bytes, 0x00020000); }
__device__ __forceinline__ v4i mkr_s(const void* p, u32 bytes) {
  const u64 a = (u64)p; v4i r;
  r.x = __builtin_amdgcn_readfirstlane((u32)a); r.y = __builtin_amdgcn_readfirstlane((u32)(a >> 32)); r.z = (int)bytes; r.w = 0x00020000;
  return r;
}
constexpr int SC1 = 16, WAUX = FK_WNT ? 2 : 0;
__device__ __forceinline__ v2u ld2(rsrc_t r, u32 vo, u32 so = 0) { return __builtin_amdgcn_raw_buffer_load_b64(r, vo, so, SC1); }
__device__ __forceinline__ v4u ld4(rsrc_t r, u32 vo, u32 so = 0) { return __builtin_amdgcn_raw_buffer_load_b128(r, vo, so, SC1); }
__device__ __forceinline__ void st2(rsrc_t r, u32 vo, v2u v) { __builtin_amdgcn_raw_buffer_store_b64(v, r, vo, 0, SC1); }
__device__ __forceinline__ void st4(rsrc_t r, u32 vo, v4u v) { __builtin_amdgcn_raw_buffer_store_b128(v, r, vo, 0, SC1); }

#define STAMP(i) do {} while (0)
#define FLUSH() do {} while (0)

template <int N> __device__ __forceinline__ void pause() { if (N > 0) asm volatile("s_sleep %0" ::"i"(N) : "memory"); }
__device__ __forceinline__ float rsum16(float v) {
  v += dppm<0x128>(v); v += dppm<0x124>(v); v += dppm<0x122>(v); v += dppm<0x121>(v);
  return v;
}
struct Tag { u32 b[4]; };
__device__ __forceinline__ Tag mktag(u32 t) { return Tag{{t & 0xffu, (t >> 8) & 0xffu, (t >> 16) & 0xffu, t >> 24}}; }
__device__ __forceinline__ u32 enc(float x, u32 tb) { return ((asu(x) + 0x80u) & 0xffffff00u) | tb; }
__device__ __forceinline__ v4u enc4(f4 x, const Tag& t) { return v4u{enc(x[0], t.b[0]), enc(x[1], t.b[1]), enc(x[2], t.b[2]), enc(x[3], t.b[3])}; }
__device__ __forceinline__ float dec(u32 y) { return asf(y & 0xffffff00u); }
__device__ __forceinline__ bool bad2(v2u y, u32 t16) { return (__builtin_amdgcn_perm(y.y, y.x, 0x0c0c0400u)) != t16; }
__device__ __forceinline__ bool bad4(v4u y, u32 t32) {
  return __builtin_amdgcn_perm(__builtin_amdgcn_perm(y.w, y.z, 0x0c0c0400u), __builtin_amdgcn_perm(y.y, y.x, 0x0c0c0400u), 0x05040100u) != t32;
}
__device__ __forceinline__ bool half_any(bool x, int lane) { const u64 m = __ballot(x); return (lane < 32 ? (u32)m : (u32)(m >> 32)) != 0u; }
__device__ __forceinline__ f2 ln_stats(float piv, float s1, float s2) {
  const float md = s1 * (1.f / DM), e2 = s2 * (1.f / DM);
  const float var = rb(fmaxf(fmaf(-md, md, e2), 0.f));
  return f2{rb(piv + md), __builtin_amdgcn_rcpf(rb(__builtin_amdgcn_sqrtf(rb(var + EPS))))};
}
__device__ __forceinline__ v4u ln_apply(v4u xr, f2 st) {
  v4u o;
#pragma unroll
  for (int i = 0; i < 4; ++i) { const u32 d = pk(blo(xr[i]) - st.x, bhi(xr[i]) - st.x); o[i] = pk(blo(d) * st.y, bhi(d) * st.y); }
  return o;
}
__device__ __forceinline__ v4u layernorm8(v4u xr) {
  const float piv = asf(__builtin_amdgcn_readfirstlane(xr[0] << 16));
  float s1 = 0.f, s2 = 0.f;
#pragma unroll
  for (int i = 0; i < 4; ++i) { const float d0 = blo(xr[i]) - piv, d1 = bhi(xr[i]) - piv; s1 += d0 + d1; s2 = fmaf(d0, d0, fmaf(d1, d1, s2)); }
  s1 = wsum63(s1); s2 = wsum63(s2);
  return ln_apply(xr, ln_stats(piv, asf(__builtin_amdgcn_readlane(asu(s1), 63)), asf(__builtin_amdgcn_readlane(asu(s2), 63))));
}

#define FK_PIECE(T, VO) \
  "s_waitcnt vmcnt(%c[wm])\n\t" \
  ".if %c[pt] == " #T "\n\tds_write_b32 %[fa], %[fv]\n\t.endif\n\t" \
  ".if %c[nt]\n\tbuffer_load_dwordx4 " VO ", %[rs], %[so] offen offset:" #T "*1024 nt lds\n\t.else\n\tbuffer_load_dwordx4 " VO ", %[rs], %[so] offen offset:" #T "*1024 lds\n\t.endif\n\t"
template <int W, int NT>
__device__ __forceinline__ void dma_tile(v4i rs, u32 so, u32 la, u32 v0, u32 v1, u32 v2, u32 v3, u32 fa, int tn) {
  const int landed = tn - 1 - (W - 5) / 4;
  asm volatile("s_mov_b32 m0, %[la]\n\t" FK_PIECE(0, "%[v0]") FK_PIECE(1, "%[v1]") FK_PIECE(2, "%[v2]") FK_PIECE(3, "%[v3]")
               :
               : [rs] "s"(rs), [so] "s"(so), [la] "s"(la), [v0] "v"(v0), [v1] "v"(v1), [v2] "v"(v2), [v3] "v"(v3), [fa] "v"(fa),
                 [fv] "v"(landed > 0 ? landed : 0), [wm] "i"(W - 1), [pt] "i"((W - 5) % 4), [nt] "i"(NT)
               : "memory");
}
template <int NT>
__device__ __forceinline__ void dma_adapt(int lag, v4i rs, u32 so, u32 la, u32 v0, u32 v1, u32 v2, u32 v3, u32 fa, int tn) {
  if (lag <= 0) dma_tile<FK_W, NT>(rs, so, la, v0, v1, v2, v3, fa, tn);
  else if (lag <= FK_LG) dma_tile<FK_WB, NT>(rs, so, la, v0, v1, v2, v3, fa, tn);
  else dma_tile<FK_WC, NT>(rs, so, la, v0, v1, v2, v3, fa, tn);
}
__device__ __forceinline__ bool lds_wait_ge(volatile LAS u32* w, u32 need, bool& dead) {
  for (u32 n = 0;; ++n) {
    if ((u32)__builtin_amdgcn_readfirstlane(*w) >= need) break;
    if (dead || n > SPIN_LDS) { dead = true; break; }
    __builtin_amdgcn_s_sleep(1);
  }
  CB();
  return true;
}

__global__ void __launch_bounds__(1024, 4) fused_block(Params p) {
  __shared__ __attribute__((aligned(1024))) unsigned char lds_raw[L_END];
  LAS unsigned char* L = (LAS unsigned char*)lds_raw;
  const int tid = threadIdx.x, lane = tid & 63, wave = __builtin_amdgcn_readfirstlane(tid >> 6), w = wave & 3, role = wave >> 2;
  const int g = lane >> 4, c = lane & 15;
  const int kvh = blockIdx.x & 7, b = blockIdx.x >> 6, s = (blockIdx.x >> 3) & 7, grp = b * 8 + kvh;
  const rsrc_t rw = mkr(p.ws, WS_END);
  volatile LAS u32* FLAG = (volatile LAS u32*)(L + L_FLAG);
  const bool ffn = s < 7;
  const int j = kvh * 7 + s;
  bool dead = false;
  STAMP(0);
  const u32 dg0 = (u32)__builtin_amdgcn_s_memrealtime();
#define DIAG(n) do { if (FK_DIAG == n && w == 0) { FLAG[14] = (u32)__builtin_amdgcn_s_memrealtime(); } } while (0)

  if (role == 1) {
    __builtin_amdgcn_s_setprio(3);
    const u32 tb = (u32)__builtin_amdgcn_s_memrealtime() + FK_T0;
    const v4i rk = mkr_s(p.k + (size_t)grp * SK * 128, SK * 256), rv = mkr_s(p.v + (size_t)grp * SK * 128, SK * 256);
    const u32 k0 = g * 256 + ((c ^ g) << 4), k1 = g * 256 + ((c ^ (4 + g)) << 4), k2 = g * 256 + ((c ^ (8 + g)) << 4), k3 = g * 256 + ((c ^ (12 + g)) << 4);
    const u32 u0 = g * 256 + ((c ^ (g << 1)) << 4), u1 = g * 256 + ((c ^ ((4 + g) << 1)) << 4);
    const u32 ring = (u32)(size_t)(L + L_RING) + w * 32768, fa = (u32)(size_t)(FLAG + w);
    STAMP(5);
    if (lane < 4) FLAG[w + 4 * lane] = 0u;
    wg_barrier();
    STAMP(7);
    dma_tile<FK_W, FK_KNT>(rk, (u32)(s * 4 + w) * 4096u, ring, k0, k1, k2, k3, fa, 0);
    STAMP(6);
    dma_tile<FK_W, FK_VNT>(rv, (u32)(s * 4 + w) * 4096u, ring + 4096, u0, u1, u0, u1, fa, 1);
    STAMP(8);
    DIAG(1);
#pragma unroll 1
    for (int i = 1; i < NCH; ++i) {
      const u32 so = (u32)((i * 8 + s) * 4 + w) * 4096u, la = ring + (i & 3) * 8192;
      if (i >= 4) lds_wait_ge(FLAG + 4 + w, i - 3, dead);
      const int lag = (int)((u32)__builtin_amdgcn_s_memrealtime() - tb) - ((i * FK_TAU) >> 4);
      dma_adapt<FK_KNT>(lag, rk, so, la, k0, k1, k2, k3, fa, 2 * i);
      dma_adapt<FK_VNT>(lag, rv, so, la + 4096, u0, u1, u0, u1, fa, 2 * i + 1);
    }
    asm volatile("s_waitcnt vmcnt(8)\n\tds_write_b32 %0, %1\n\ts_waitcnt vmcnt(4)\n\tds_write_b32 %0, %2\n\ts_waitcnt vmcnt(0)\n\tds_write_b32 %0, %3" ::"v"(fa),
                 "v"(2 * NCH - 2), "v"(2 * NCH - 1), "v"(2 * NCH) : "memory");
    STAMP(1);
    DIAG(2);
    __builtin_amdgcn_s_setprio(0);
    v4u dr[8], df[8];
    const rsrc_t rd = mkr(p.wd, FF * DM * 2);
#pragma unroll
    for (int r = 0; r < 8; ++r) dr[r] = __builtin_amdgcn_raw_buffer_load_b128(rd, ffn ? (8 * g) * 1024 + 16 * c : OOB, (32 * j + r) * 1024 + 256 * w, WAUX);
    wg_barrier();
    const Tag T = mktag(FLAG[12]);
    LAS unsigned char* priv = L + L_QRED + w * 1024;
#pragma unroll
    for (int t = 0; t < 4; ++t)
#pragma unroll
      for (int q = 0; q < 4; ++q) { df[2 * q][t] = plo(dr[2 * t + 1][q], dr[2 * t][q]); df[2 * q + 1][t] = phi(dr[2 * t + 1][q], dr[2 * t][q]); }
    asm volatile("" : "+v"(df[0]), "+v"(df[1]), "+v"(df[2]), "+v"(df[3]), "+v"(df[4]), "+v"(df[5]), "+v"(df[6]), "+v"(df[7]));
    STAMP(2);
    wg_barrier();
    wg_barrier();
    wg_barrier();
    STAMP(3);
    if (ffn) {
      const LAS float* gu = (const LAS float*)(L + L_GU) + (lane & 31);
      const float gt = rb((gu[0] + gu[64]) + (gu[128] + gu[192])), up = rb((gu[32] + gu[96]) + (gu[160] + gu[224]));
      const float sg = rb(gt * __builtin_amdgcn_rcpf(1.f + __builtin_amdgcn_exp2f(-gt * L2E)));
      ((LAS u16*)priv)[lane & 31] = (u16)pk(sg * up, 0.f);
      const v4u a = *(const LAS v4u*)(priv + 16 * g);
      f4 o[8];
#pragma unroll
      for (int i = 0; i < 8; ++i) o[i] = mfma32(a, df[i], f4{0.f, 0.f, 0.f, 0.f});
      const bool hi = g & 1;
      const v4u ov = enc4(f4{hi ? o[4][0] : o[0][0], hi ? o[5][0] : o[1][0], hi ? o[6][0] : o[2][0], hi ? o[7][0] : o[3][0]}, T);
      st4(rw, lane < 32 ? WS_D + (u32)(b * 56 + j) * 2048 + (u32)(128 * w + 8 * c + 4 * g) * 4 : OOB, ov);
    }
    STAMP(4);
    FLUSH();
    return;
  }

  if (role == 2) {
    LAS unsigned char* priv = L + L_PRIV + w * 1024;
    wg_barrier();
    pause<FK_W0>(); pause<FK_W0>();
    v2u wor[4][8];
    const rsrc_t ro = mkr(p.wo, NQ * DM * 2);
#pragma unroll
    for (int u = 0; u < 4; ++u)
#pragma unroll
      for (int r = 0; r < 8; ++r) {
        wor[u][r] = __builtin_amdgcn_raw_buffer_load_b64(ro, g * 8 * 1024 + 8 * c, (kvh * 512 + 128 * w + 32 * u + r) * 1024 + 128 * s, WAUX);
        pause<FK_P1>();
      }
    v4u wf[4][4];
#pragma unroll
    for (int u = 0; u < 4; ++u) { tr84(wor[u], wf[u]); asm volatile("" : "+v"(wf[u][0]), "+v"(wf[u][1]), "+v"(wf[u][2]), "+v"(wf[u][3])); }
    lds_wait_ge(FLAG + 9, 4, dead);
    const u32 tg = FLAG[12];
    const Tag T = mktag(tg);
    STAMP(1);
    wg_barrier();
    {
      const u32 ob = WS_O + grp * 8 * OREC, om = WS_ML + (grp * 8 + (lane & 7)) * 128 + w * 16, oo = ob + (lane >> 5) * OREC + w * 512 + (lane & 31) * 16;
      pause<FK_D1>();
      STAMP(2);
      v4u o[4], ml;
      ml = ld4(rw, om);
      for (u32 n = 0; __any(bad4(ml, tg)); ++n) {
        if (dead || n > SPIN_MEM) { dead = true; break; }
        pause<FK_BO>();
        ml = ld4(rw, om);
      }
      STAMP(7);
#pragma unroll
      for (int t = 0; t < 4; ++t) o[t] = ld4(rw, oo, ((t + s) & 3) * 2 * OREC);
      float M = dec(ml.x);
      M = fmaxf(M, dppm<0xB1>(M)); M = fmaxf(M, dppm<0x4E>(M)); M = fmaxf(M, dppm<0x141>(M));
      const float e = __builtin_amdgcn_exp2f((dec(ml.x) - M) * L2E);
      float Ls = e * dec(ml.y);
      Ls += dppm<0xB1>(Ls); Ls += dppm<0x4E>(Ls); Ls += dppm<0x141>(Ls);
      const float en = e * __builtin_amdgcn_rcpf(Ls);
      float et[4];
#pragma unroll
      for (int t = 0; t < 4; ++t) {
        const float e0 = asf(__builtin_amdgcn_readlane(asu(en), 2 * ((t + s) & 3))), e1 = asf(__builtin_amdgcn_readlane(asu(en), 2 * ((t + s) & 3) + 1));
        et[t] = lane < 32 ? e0 : e1;
      }
      asm volatile("" : "+v"(et[0]), "+v"(et[1]), "+v"(et[2]), "+v"(et[3]));
      for (u32 n = 0;; ++n) {
        bool bad = false;
#pragma unroll
        for (int t = 0; t < 4; ++t) bad = bad || bad4(o[t], tg);
        if (!__any(bad)) break;
        if (dead || n > SPIN_MEM) { dead = true; break; }
        pause<FK_BO>();
#pragma unroll
        for (int t = 0; t < 4; ++t) if (half_any(bad4(o[t], tg), lane)) o[t] = ld4(rw, oo, ((t + s) & 3) * 2 * OREC);
      }
      STAMP(3);
      DIAG(6);
      f4 os = {0.f, 0.f, 0.f, 0.f};
#pragma unroll
      for (int t = 0; t < 4; ++t)
#pragma unroll
        for (int i = 0; i < 4; ++i) os[i] = fmaf(et[t], dec(o[t][i]), os[i]);
#pragma unroll
      for (int i = 0; i < 4; ++i) os[i] = x32_sum(os[i]);
      ((LAS v2u*)priv)[lane & 31] = v2u{pk(os[0], os[1]), pk(os[2], os[3])};
      f4 y[4];
#pragma unroll
      for (int i = 0; i < 4; ++i) y[i] = f4{0.f, 0.f, 0.f, 0.f};
#pragma unroll
      for (int u = 0; u < 4; ++u) {
        const v4u a = *(const LAS v4u*)(priv + (32 * u + 8 * g) * 2);
#pragma unroll
        for (int i = 0; i < 4; ++i) y[i] = mfma32(a, wf[u][i], y[i]);
      }
      if (lane < 16) *(LAS f4*)(L + L_YRED + w * 256 + c * 16) = f4{y[0][0], y[1][0], y[2][0], y[3][0]};
    }
    wg_barrier();
    {
      const LAS f4* yr = (const LAS f4*)(L + L_YRED) + c;
      const f4 y = (yr[0] + yr[16]) + (yr[32] + yr[48]);
      st4(rw, lane < 32 ? WS_Y + (u32)(2 * w + g) * 65536 + grp * 2048 + s * 256 + c * 16 : OOB, enc4(y, T));
    }
    STAMP(4);
    wg_barrier();
    wg_barrier();
    STAMP(6);
    FLUSH();
    return;
  }

  if (role == 3) {
    LAS unsigned char* priv = L + L_PRIV + 4096 + w * 1024;
    wg_barrier();
    pause<FK_W0>(); pause<FK_W0>();
    const v2u xp = *(const GAS v2u*)(p.x + b * DM + 128 * w + 4 * (lane & 31));
    v2u gr[4][8];
    v4u gf[4][4];
    if (ffn) {
      const GAS unsigned char* gp = (const GAS unsigned char*)(c < 8 ? p.wg : p.wu) + (size_t)(128 * w + 8 * g) * (FF * 2) + 64 * j + 8 * (c & 7);
#pragma unroll
      for (int u = 0; u < 4; ++u)
#pragma unroll
        for (int r = 0; r < 8; ++r) {
          const GAS v2u* a = (const GAS v2u*)(gp + (size_t)(32 * u + r) * (FF * 2));
          gr[u][r] = FK_WNT ? __builtin_nontemporal_load(a) : *a;
          pause<FK_P2>();
        }
#pragma unroll
      for (int u = 0; u < 4; ++u) { tr84(gr[u], gf[u]); asm volatile("" : "+v"(gf[u][0]), "+v"(gf[u][1]), "+v"(gf[u][2]), "+v"(gf[u][3])); }
    }
    lds_wait_ge(FLAG + 9, 4, dead);
    const u32 tg = FLAG[12];
    if (blockIdx.x == 0) {
      const u32 tlo = FLAG[10];
      for (u32 n = 0;; ++n) {
        const v2u gv = ld2(rw, WS_QG + (u32)(lane >> 3) * 16384 + w * 4096 + (lane & 7) * 512);
        if (__all(gv.y == tlo)) break;
        if (dead || n > SPIN_MEM) { dead = true; break; }
        pause<8>();
      }
    }
    STAMP(1);
    wg_barrier();
    wg_barrier();
    {
      const u32 yb = WS_Y + (u32)kvh * 65536 + (u32)b * 8 * 2048 + (lane >> 5) * 2048 + w * 512 + (lane & 31) * 16;
      pause<FK_D2>();
      STAMP(2);
      v4u y[4];
#pragma unroll
      for (int k = 0; k < 4; ++k) y[k] = ld4(rw, yb, ((k + s) & 3) * 4096);
      for (u32 n = 0;; ++n) {
        bool bad = false;
#pragma unroll
        for (int k = 0; k < 4; ++k) bad = bad || bad4(y[k], tg);
        if (!__any(bad)) break;
        if (dead || n > SPIN_MEM) { dead = true; break; }
        pause<FK_BO>();
#pragma unroll
        for (int k = 0; k < 4; ++k) if (half_any(bad4(y[k], tg), lane)) y[k] = ld4(rw, yb, ((k + s) & 3) * 4096);
      }
      STAMP(3);
      DIAG(7);
      f4 a;
#pragma unroll
      for (int i = 0; i < 4; ++i) {
        const float p0 = dec(y[0][i]), p1 = dec(y[1][i]), p2 = dec(y[2][i]), p3 = dec(y[3][i]);
        a[i] = x32_sum((s & 1) ? (p3 + p0) + (p1 + p2) : (p0 + p1) + (p2 + p3));
      }
      const u32 a0 = pk(a[0], a[1]), a1 = pk(a[2], a[3]);
      ((LAS v2u*)(L + L_HS))[32 * w + (lane & 31)] = v2u{pk(blo(xp.x) + blo(a0), bhi(xp.x) + bhi(a0)), pk(blo(xp.y) + blo(a1), bhi(xp.y) + bhi(a1))};
    }
    wg_barrier();
    STAMP(4);
    if (ffn) {
      const v4u hn = layernorm8(*(const LAS v4u*)(L + L_HS + lane * 16));
      *(LAS v4u*)(priv + lane * 16) = hn;
      f4 acc[4];
#pragma unroll
      for (int i = 0; i < 4; ++i) acc[i] = f4{0.f, 0.f, 0.f, 0.f};
#pragma unroll
      for (int u = 0; u < 4; ++u) {
        const v4u a = *(const LAS v4u*)(priv + (128 * w + 32 * u + 8 * g) * 2);
#pragma unroll
        for (int i = 0; i < 4; ++i) acc[i] = mfma32(a, gf[u][i], acc[i]);
      }
      if (lane < 16) *(LAS f4*)(L + L_GU + w * 256 + c * 16) = f4{acc[0][0], acc[1][0], acc[2][0], acc[3][0]};
    }
    STAMP(5);
    DIAG(8);
    wg_barrier();
    if (blockIdx.x == 0 && w == 0) st2(rw, lane == 0 ? WS_E : OOB, v2u{FLAG[10], FLAG[11]});
    FLUSH();
    return;
  }

  v4u qf[4];
  u32 tg;
  {
    STAMP(6);
    v4u x4[4];
#pragma unroll
    for (int t = 0; t < 4; ++t) x4[t] = *(const GAS v4u*)(p.x + g * DM + 128 * t + 8 * c);
    const v4u xa = *(const GAS v4u*)(p.x + (c & 3) * DM + 128 * b + 32 * w + 8 * g);
    const rsrc_t rq = mkr(p.wq, DM * NQ * 2);
    v2u wr[8];
#pragma unroll
    for (int r = 0; r < 8; ++r)
      wr[r] = __builtin_amdgcn_raw_buffer_load_b64(rq, g * 8 * 8192 + 8 * c, (128 * b + 32 * w + r) * 8192 + (kvh * 512 + 64 * s) * 2, 2);
    const v2u e = ld2(rw, WS_E);
    wg_barrier();
    STAMP(10);
    const float piv = dppm<0x150>(blo(x4[0][0]));
    float s1 = 0.f, s2 = 0.f;
#pragma unroll
    for (int t = 0; t < 4; ++t)
#pragma unroll
      for (int i = 0; i < 4; ++i) { const float d0 = blo(x4[t][i]) - piv, d1 = bhi(x4[t][i]) - piv; s1 += d0 + d1; s2 = fmaf(d0, d0, fmaf(d1, d1, s2)); }
    const f2 st = ln_stats(piv, rsum16(s1), rsum16(s2));
    const f2 sa = {asf(__builtin_amdgcn_ds_bpermute((c & 3) * 64, asu(st.x))), asf(__builtin_amdgcn_ds_bpermute((c & 3) * 64, asu(st.y)))};
    const v4u a = ln_apply(xa, sa);
    v4u f[4];
    tr84(wr, f);
    f4 acc[4];
#pragma unroll
    for (int i = 0; i < 4; ++i) acc[i] = mfma32(a, f[i], f4{0.f, 0.f, 0.f, 0.f});
    if (lane < 16) {
#pragma unroll
      for (int r = 0; r < 4; ++r) *(LAS f4*)(L + L_QRED + w * 1024 + r * 256 + c * 16) = f4{acc[0][r], acc[1][r], acc[2][r], acc[3][r]};
    }
    const u64 epoch = ((u64)__builtin_amdgcn_readfirstlane(e.y) << 32 | __builtin_amdgcn_readfirstlane(e.x)) + 1;
    const u32 tlo = (u32)epoch, thi = (u32)(epoch >> 32) + tlo * 0x9E3779B1u;
    const u32 dl = tlo - (u32)(epoch >> 32);
    tg = ((tlo + 1u) & 0xffu) | (((tlo >> 8) + 3u * dl + 1u) & 0xffu) << 8 | (((tlo >> 16) + 5u * dl + 1u) & 0xffu) << 16 | ((tlo >> 24) + 7u * dl + 1u) << 24;
    if (w == 0 && lane == 0) { FLAG[10] = tlo; FLAG[11] = (u32)(epoch >> 32); FLAG[12] = tg; }
    asm volatile("s_waitcnt lgkmcnt(0)" ::: "memory");
    STAMP(11);
    u32 old = 0;
    if (lane == 0) old = __hip_atomic_fetch_add((LAS u32*)(L + L_FLAG) + 8, 1u, __ATOMIC_RELAXED, __HIP_MEMORY_SCOPE_WORKGROUP);
    old = __builtin_amdgcn_readfirstlane(old);
    CB();
    if (old == 3) {
      const LAS f2* qr = (const LAS f2*)(L + L_QRED) + g * 32 + c;
      const f2 q0 = (qr[0] + qr[128]) + (qr[256] + qr[384]), q1 = (qr[16] + qr[144]) + (qr[272] + qr[400]);
      const u32 off = WS_QG + (u32)(g * 8 + kvh) * 16384 + b * 4096 + s * 512 + c * 16;
      st4(rw, off, v4u{asu(q0.x), tlo, asu(q0.y), thi});
      st4(rw, off + 256, v4u{asu(q1.x), tlo, asu(q1.y), thi});
    }
    STAMP(1);
    pause<FK_D0>();
    v4u gq[4];
    const u32 qo = WS_QG + grp * 16384 + w * 1024 + lane * 16;
#pragma unroll
    for (int t = 0; t < 4; ++t) gq[t] = ld4(rw, qo, t * 4096);
    for (u32 n = 0;; ++n) {
      bool bad = false;
#pragma unroll
      for (int t = 0; t < 4; ++t) bad = bad || gq[t].y != tlo || gq[t].w != thi;
      if (!__any(bad)) break;
      if (dead || n > SPIN_MEM) { dead = true; break; }
      pause<2>();
#pragma unroll
      for (int t = 0; t < 4; ++t) if (__any(gq[t].y != tlo || gq[t].w != thi)) gq[t] = ld4(rw, qo, t * 4096);
    }
    STAMP(12);
    ((LAS u32*)(L + L_QS))[64 * w + lane] = pk((asf(gq[0].x) + asf(gq[1].x)) + (asf(gq[2].x) + asf(gq[3].x)), (asf(gq[0].z) + asf(gq[1].z)) + (asf(gq[2].z) + asf(gq[3].z)));
    asm volatile("s_waitcnt lgkmcnt(0)" ::: "memory");
    if (lane == 0) __hip_atomic_fetch_add((LAS u32*)(L + L_FLAG) + 9, 1u, __ATOMIC_RELAXED, __HIP_MEMORY_SCOPE_WORKGROUP);
    lds_wait_ge(FLAG + 9, 4, dead);
#pragma unroll
    for (int u = 0; u < 4; ++u) qf[u] = *(const LAS v4u*)(L + L_QS + ((c & 3) * 128 + 32 * u + 8 * g) * 2);
    STAMP(2);
    DIAG(4);
  }
  float m = -INFINITY, l = 0.f;
  f4 acc[8];
#pragma unroll
  for (int i = 0; i < 8; ++i) acc[i] = f4{0.f, 0.f, 0.f, 0.f};
  const LAS unsigned char* ringk = L + L_RING + w * 32768 + c * 256;
  const u32 vrow = 4 * g + (c >> 2);
  const u32 va = (u32)(size_t)(L + L_RING) + w * 32768 + 4096 + vrow * 256 + (vrow & 7) * 32 + 8 * (c & 3);
  u32 seen = 0;
  auto need = [&](u32 n) {
    for (u32 k = 0; seen < n; ++k) {
      seen = __builtin_amdgcn_readfirstlane(FLAG[w]);
      if (seen >= n) break;
      if (dead || k > SPIN_LDS) { dead = true; break; }
      __builtin_amdgcn_s_sleep(1);
    }
    CB();
  };
#pragma unroll 1
  for (int i = 0; i < NCH; ++i) {
    const u32 sl = (i & 3) * 8192;
    need(2 * i + 1);
    v4u kf[4];
#pragma unroll
    for (int u = 0; u < 4; ++u) kf[u] = *(const LAS v4u*)(ringk + sl + (((4 * u + g) ^ c) << 4));
    f4 s0 = mfma32(kf[0], qf[0], f4{0.f, 0.f, 0.f, 0.f}), s1 = mfma32(kf[2], qf[2], f4{0.f, 0.f, 0.f, 0.f});
    s0 = mfma32(kf[1], qf[1], s0); s1 = mfma32(kf[3], qf[3], s1);
    const u32 a01 = pk(s0[0] + s1[0], s0[1] + s1[1]), a23 = pk(s0[2] + s1[2], s0[3] + s1[3]);
    const u32 b01 = pk(blo(a01) * SCALE, bhi(a01) * SCALE), b23 = pk(blo(a23) * SCALE, bhi(a23) * SCALE);
    const float t0 = blo(b01), t1 = bhi(b01), t2 = blo(b23), t3 = bhi(b23);
    const float mx = xg_max(fmaxf(fmaxf(t0, t1), fmaxf(t2, t3)));
    if (__any(mx > m)) {
      const float mn = fmaxf(m, mx), al = __builtin_amdgcn_exp2f((m - mn) * L2E);
      l *= al; m = mn;
#pragma unroll
      for (int d = 0; d < 8; ++d) acc[d] *= al;
    }
    const float p0 = __builtin_amdgcn_exp2f((t0 - m) * L2E), p1 = __builtin_amdgcn_exp2f((t1 - m) * L2E),
                p2 = __builtin_amdgcn_exp2f((t2 - m) * L2E), p3 = __builtin_amdgcn_exp2f((t3 - m) * L2E);
    l += (p0 + p1) + (p2 + p3);
    const v2u pf = {pk(p0, p1), pk(p2, p3)};
    if (i == NCH - 1) {
      const float lt = xg_sum(l);
      if (lane < 4) *(LAS f2*)(L + L_MLS + w * 32 + c * 8) = f2{m, lt};
    }
    need(2 * i + 2);
    s4 vf[8];
#pragma unroll
    for (int d = 0; d < 8; ++d) vf[d] = __builtin_amdgcn_ds_read_tr16_b64_v4i16((LAS s4*)(size_t)((va ^ (d << 5)) + sl));
    asm volatile("s_waitcnt lgkmcnt(0)" ::: "memory");
    FLAG[4 + w] = i + 1;
#pragma unroll
    for (int d = 0; d < 8; ++d) acc[d] = mfma16(vf[d], pf, acc[d]);
  }
  STAMP(4);
  if (c < 4) {
#pragma unroll
    for (int d = 0; d < 8; ++d) *(LAS f4*)(L + L_OM + w * 2048 + c * 512 + (16 * d + 4 * g) * 4) = acc[d];
  }
  wg_barrier();
  {
    const int hd = lane < 32 ? w : (lane & 3);
    f2 ml[4]; f4 ov[4];
#pragma unroll
    for (int k = 0; k < 4; ++k) { ml[k] = *(const LAS f2*)(L + L_MLS + k * 32 + hd * 8); ov[k] = *(const LAS f4*)(L + L_OM + k * 2048 + w * 512 + (lane & 31) * 16); }
    const float M = fmaxf(fmaxf(ml[0].x, ml[1].x), fmaxf(ml[2].x, ml[3].x));
    float Ls = 0.f; f4 O = {0.f, 0.f, 0.f, 0.f};
#pragma unroll
    for (int k = 0; k < 4; ++k) { const float e = __builtin_amdgcn_exp2f((ml[k].x - M) * L2E); Ls = fmaf(e, ml[k].y, Ls); O += ov[k] * e; }
    const u32 wgi = grp * 8 + s;
    st4(rw, lane < 32 ? WS_O + wgi * OREC + w * 512 + lane * 16 : (lane < 40 && w == 0 ? WS_ML + wgi * 128 + (lane - 32) * 16 : OOB), enc4(lane < 32 ? O : f4{M, Ls, M, Ls}, mktag(tg)));
  }
  STAMP(5);
  DIAG(5);
  wg_barrier();
  wg_barrier();
  wg_barrier();
    if (w < 2) {
      const int col = (kvh * 8 + s) * 8 + 4 * w;
      const u32 off = lane < 56 ? WS_D + (u32)(b * 56 + lane) * 2048 + col * 4 : OOB;
      const v2u hv = *(const LAS v2u*)(L + L_HS + col * 2);
      const u32 tgl = lane < 56 ? tg : 0u;
      pause<FK_D3>();
      STAMP(7);
      v4u d0 = ld4(rw, off);
      for (u32 n = 0; __any(bad4(d0, tgl)); ++n) {
        if (dead || n > SPIN_MEM) { dead = true; break; }
        pause<FK_BO>();
        d0 = ld4(rw, off);
      }
      STAMP(8);
      const u32 a0 = pk(wsum63(dec(d0[0])), wsum63(dec(d0[1]))), a1 = pk(wsum63(dec(d0[2])), wsum63(dec(d0[3])));
      if (lane == 63) *(GAS v2u*)(p.out + b * DM + col) = v2u{pk(blo(hv.x) + blo(a0), bhi(hv.x) + bhi(a0)), pk(blo(hv.y) + blo(a1), bhi(hv.y) + bhi(a1))};
      STAMP(9);
      if (blockIdx.x == FK_DIAGB && w == 0) { const u32 now = (u32)__builtin_amdgcn_s_memrealtime(), x = ((FK_DIAG == 3 ? now : FLAG[14]) - dg0) * FK_DIAGK;
        while ((u32)__builtin_amdgcn_s_memrealtime() - now < x && x < 100000) __builtin_amdgcn_s_sleep(1); }
    }
  FLUSH();
}
}

extern "C" void kernel_launch(void* const* d_in, const int* in_sizes, int n_in, void* d_out, int out_size, void* d_ws, size_t ws_size,
                              hipStream_t stream) {
  fk::Params p;
  p.x = (const fk::u16*)d_in[0]; p.k = (const fk::u16*)d_in[1]; p.v = (const fk::u16*)d_in[2];
  p.wq = (const fk::u16*)d_in[3]; p.wo = (const fk::u16*)d_in[4];
  p.wg = (const fk::u16*)d_in[6]; p.wu = (const fk::u16*)d_in[7]; p.wd = (const fk::u16*)d_in[8];
  p.out = (fk::u16*)d_out; p.ws = (unsigned char*)d_ws;
  hipLaunchKernelGGL(fk::fused_block, dim3(256), dim3(1024), 0, stream, p);
}
```

```cpp
#include <hip/hip_runtime.h>
#include <stdint.h>

#define FK_W 5
#define FK_WNT 1
#define FK_W0 120
#define FK_P1 18
#define FK_P2 22
#define FK_ORD 1
#define FK_G0 0
#define FK_WB 7
#define FK_WC 10
#define FK_LG 60
#define FK_T0 80
#define FK_TAU 1830
#define FK_DIAG 0
#define FK_DIAGB 0
#define FK_DIAGK 1
#define FK_DB2 0
#define FK_KNT 1
#define FK_VNT 1
#define FK_WB2 1
#define FK_WB2D 0
#define FK_BO 1
#define FK_D0 40
#define FK_D1 18
#define FK_D2 16
#define FK_D3 32
#define FK_STAMPS 0

namespace fk {
#define GAS __attribute__((address_space(1)))
#define LAS __attribute__((address_space(3)))
typedef unsigned u32;
typedef unsigned long long u64;
typedef unsigned short u16;
typedef u32 v2u __attribute__((ext_vector_type(2)));
typedef u32 v4u __attribute__((ext_vector_type(4)));
typedef int v4i __attribute__((ext_vector_type(4)));
typedef float f2 __attribute__((ext_vector_type(2)));
typedef float f4 __attribute__((ext_vector_type(4)));
typedef short s4 __attribute__((ext_vector_type(4)));
typedef __bf16 b2 __attribute__((ext_vector_type(2)));
typedef __bf16 b8 __attribute__((ext_vector_type(8)));

constexpr int DM = 512, SK = 16384, FF = 1792, NQ = 4096;
constexpr int NCH = 32;
constexpr float L2E = 1.4426950408889634f, SCALE = 0.08838834764831845f, EPS = 1e-5f;
constexpr u32 WS_E = 0, WS_QG = 4096, WS_O = WS_QG + 32 * 16384, OREC = 2048, WS_ML = WS_O + 256 * OREC  , WS_Y = WS_ML + 256 * 128,
              WS_D = WS_Y + 8 * 65536, WS_END = WS_D + 4 * 56 * 2048, WS_DBG = WS_END;
constexpr u32 OOB = 0x80000000u;
constexpr u32 L_RING = 0, L_FLAG = 131072  , L_QRED = L_FLAG + 256  , L_QS = L_QRED + 4096,
              L_PRIV = L_QS + 1024  , L_OM = L_PRIV + 8192, L_MLS = L_OM + 8192, L_YRED = L_MLS + 128, L_HS = L_YRED + 1024,
              L_GU = L_HS + 1024, L_END = L_GU + 1024;
constexpr u32 SPIN_LDS = 1u << 19, SPIN_MEM = 1u << 16;

struct Params {
  const u16 *x, *k, *v, *wq, *wo, *wg, *wu, *wd;
  u16* out;
  unsigned char* ws;
};

#define CB() asm volatile("" ::: "memory")
__device__ __forceinline__ void wg_barrier() {
  asm volatile("s_waitcnt lgkmcnt(0)\n\ts_barrier" ::: "memory");
  __builtin_amdgcn_sched_barrier(0);
}
__device__ __forceinline__ float asf(u32 x) { return __builtin_bit_cast(float, x); }
__device__ __forceinline__ u32 asu(float x) { return __builtin_bit_cast(u32, x); }
__device__ __forceinline__ u32 pk(float lo, float hi) { f2 z = {lo, hi}; return __builtin_bit_cast(u32, __builtin_convertvector(z, b2)); }
__device__ __forceinline__ float blo(u32 p) { return asf(p << 16); }
__device__ __forceinline__ float bhi(u32 p) { return asf(p & 0xffff0000u); }
__device__ __forceinline__ float rb(float x) { return blo(pk(x, x)); }
template <int CTRL, int RM = 0xf> __device__ __forceinline__ float dpp0(float x) {
  return asf(__builtin_amdgcn_update_dpp(0, asu(x), CTRL, RM, 0xf, true));
}
template <int CTRL> __device__ __forceinline__ float dppm(float x) { return asf(__builtin_amdgcn_mov_dpp(asu(x), CTRL, 0xf, 0xf, true)); }
__device__ __forceinline__ float wsum63(float v) {
  v += dpp0<0x111>(v); v += dpp0<0x112>(v); v += dpp0<0x114>(v); v += dpp0<0x118>(v);
  v += asf(__builtin_amdgcn_update_dpp(0, asu(v), 0x142, 0xa, 0xf, false));
  v += asf(__builtin_amdgcn_update_dpp(0, asu(v), 0x143, 0xc, 0xf, false));
  return v;
}
__device__ __forceinline__ float wsum(float v) { return asf(__builtin_amdgcn_readlane(asu(wsum63(v)), 63)); }
__device__ __forceinline__ float xg_max(float x) {
  auto s = __builtin_amdgcn_permlane16_swap(asu(x), asu(x), false, false);
  x = fmaxf(asf(s[0]), asf(s[1]));
  auto t = __builtin_amdgcn_permlane32_swap(asu(x), asu(x), false, false);
  return fmaxf(asf(t[0]), asf(t[1]));
}
__device__ __forceinline__ float xg_sum(float x) {
  auto s = __builtin_amdgcn_permlane16_swap(asu(x), asu(x), false, false);
  x = asf(s[0]) + asf(s[1]);
  auto t = __builtin_amdgcn_permlane32_swap(asu(x), asu(x), false, false);
  return asf(t[0]) + asf(t[1]);
}
__device__ __forceinline__ float x32_sum(float x) {
  auto t = __builtin_amdgcn_permlane32_swap(asu(x), asu(x), false, false);
  return asf(t[0]) + asf(t[1]);
}
__device__ __forceinline__ f4 mfma32(v4u a, v4u b, f4 c) {
  return __builtin_amdgcn_mfma_f32_16x16x32_bf16(__builtin_bit_cast(b8, a), __builtin_bit_cast(b8, b), c, 0, 0, 0);
}
__device__ __forceinline__ f4 mfma16(s4 a, v2u b, f4 c) {
  return __builtin_amdgcn_mfma_f32_16x16x16bf16_1k(a, __builtin_bit_cast(s4, b), c, 0, 0, 0);
}
__device__ __forceinline__ u32 plo(u32 r1, u32 r0) { return __builtin_amdgcn_perm(r1, r0, 0x05040100u); }
__device__ __forceinline__ u32 phi(u32 r1, u32 r0) { return __builtin_amdgcn_perm(r1, r0, 0x07060302u); }
__device__ __forceinline__ void tr84(const v2u (&r)[8], v4u (&f)[4]) {
#pragma unroll
  for (int t = 0; t < 4; ++t) {
    f[0][t] = plo(r[2 * t + 1].x, r[2 * t].x); f[1][t] = phi(r[2 * t + 1].x, r[2 * t].x);
    f[2][t] = plo(r[2 * t + 1].y, r[2 * t].y); f[3][t] = phi(r[2 * t + 1].y, r[2 * t].y);
  }
}
typedef __amdgpu_buffer_rsrc_t rsrc_t;
__device__ __forceinline__ rsrc_t mkr(const void* p, u32 bytes) { return __builtin_amdgcn_make_buffer_rsrc((void*)p, 0, bytes, 0x00020000); }
__device__ __forceinline__ v4i mkr_s(const void* p, u32 bytes) {
  const u64 a = (u64)p; v4i r;
  r.x = __builtin_amdgcn_readfirstlane((u32)a); r.y = __builtin_amdgcn_readfirstlane((u32)(a >> 32)); r.z = (int)bytes; r.w = 0x00020000;
  return r;
}
constexpr int SC1 = 16, WAUX = FK_WNT ? 2 : 0;
__device__ __forceinline__ v2u ld2(rsrc_t r, u32 vo, u32 so = 0) { return __builtin_amdgcn_raw_buffer_load_b64(r, vo, so, SC1); }
__device__ __forceinline__ v4u ld4(rsrc_t r, u32 vo, u32 so = 0) { return __builtin_amdgcn_raw_buffer_load_b128(r, vo, so, SC1); }
__device__ __forceinline__ void st2(rsrc_t r, u32 vo, v2u v) { __builtin_amdgcn_raw_buffer_store_b64(v, r, vo, 0, SC1); }
__device__ __forceinline__ void st4(rsrc_t r, u32 vo, v4u v) { __builtin_amdgcn_raw_buffer_store_b128(v, r, vo, 0, SC1); }

#define STAMP(i) do {} while (0)
#define FLUSH() do {} while (0)

template <int N> __device__ __forceinline__ void pause() { if (N > 0) asm volatile("s_sleep %0" ::"i"(N) : "memory"); }
__device__ __forceinline__ float rsum16(float v) {
  v += dppm<0x128>(v); v += dppm<0x124>(v); v += dppm<0x122>(v); v += dppm<0x121>(v);
  return v;
}
struct Tag { u32 b[4]; };
__device__ __forceinline__ Tag mktag(u32 t) { return Tag{{t & 0xffu, (t >> 8) & 0xffu, (t >> 16) & 0xffu, t >> 24}}; }
__device__ __forceinline__ u32 enc(float x, u32 tb) { return ((asu(x) + 0x80u) & 0xffffff00u) | tb; }
__device__ __forceinline__ v4u enc4(f4 x, const Tag& t) { return v4u{enc(x[0], t.b[0]), enc(x[1], t.b[1]), enc(x[2], t.b[2]), enc(x[3], t.b[3])}; }
__device__ __forceinline__ float dec(u32 y) { return asf(y & 0xffffff00u); }
__device__ __forceinline__ bool bad2(v2u y, u32 t16) { return (__builtin_amdgcn_perm(y.y, y.x, 0x0c0c0400u)) != t16; }
__device__ __forceinline__ bool bad4(v4u y, u32 t32) {
  return __builtin_amdgcn_perm(__builtin_amdgcn_perm(y.w, y.z, 0x0c0c0400u), __builtin_amdgcn_perm(y.y, y.x, 0x0c0c0400u), 0x05040100u) != t32;
}
__device__ __forceinline__ bool half_any(bool x, int lane) { const u64 m = __ballot(x); return (lane < 32 ? (u32)m : (u32)(m >> 32)) != 0u; }
__device__ __forceinline__ f2 ln_stats(float piv, float s1, float s2) {
  const float md = s1 * (1.f / DM), e2 = s2 * (1.f / DM);
  const float var = rb(fmaxf(fmaf(-md, md, e2), 0.f));
  return f2{rb(piv + md), __builtin_amdgcn_rcpf(rb(__builtin_amdgcn_sqrtf(rb(var + EPS))))};
}
__device__ __forceinline__ v4u ln_apply(v4u xr, f2 st) {
  v4u o;
#pragma unroll
  for (int i = 0; i < 4; ++i) { const u32 d = pk(blo(xr[i]) - st.x, bhi(xr[i]) - st.x); o[i] = pk(blo(d) * st.y, bhi(d) * st.y); }
  return o;
}
__device__ __forceinline__ v4u layernorm8(v4u xr) {
  const float piv = asf(__builtin_amdgcn_readfirstlane(xr[0] << 16));
  float s1 = 0.f, s2 = 0.f;
#pragma unroll
  for (int i = 0; i < 4; ++i) { const float d0 = blo(xr[i]) - piv, d1 = bhi(xr[i]) - piv; s1 += d0 + d1; s2 = fmaf(d0, d0, fmaf(d1, d1, s2)); }
  s1 = wsum63(s1); s2 = wsum63(s2);
  return ln_apply(xr, ln_stats(piv, asf(__builtin_amdgcn_readlane(asu(s1), 63)), asf(__builtin_amdgcn_readlane(asu(s2), 63))));
}

#define FK_PIECE(T, VO) \
  "s_waitcnt vmcnt(%c[wm])\n\t" \
  ".if %c[pt] == " #T "\n\tds_write_b32 %[fa], %[fv]\n\t.endif\n\t" \
  ".if %c[nt]\n\tbuffer_load_dwordx4 " VO ", %[rs], %[so] offen offset:" #T "*1024 nt lds\n\t.else\n\tbuffer_load_dwordx4 " VO ", %[rs], %[so] offen offset:" #T "*1024 lds\n\t.endif\n\t"
template <int W, int NT>
__device__ __forceinline__ void dma_tile(v4i rs, u32 so, u32 la, u32 v0, u32 v1, u32 v2, u32 v3, u32 fa, int tn) {
  const int landed = tn - 1 - (W - 5) / 4;
  asm volatile("s_mov_b32 m0, %[la]\n\t" FK_PIECE(0, "%[v0]") FK_PIECE(1, "%[v1]") FK_PIECE(2, "%[v2]") FK_PIECE(3, "%[v3]")
               :
               : [rs] "s"(rs), [so] "s"(so), [la] "s"(la), [v0] "v"(v0), [v1] "v"(v1), [v2] "v"(v2), [v3] "v"(v3), [fa] "v"(fa),
                 [fv] "v"(landed > 0 ? landed : 0), [wm] "i"(W - 1), [pt] "i"((W - 5) % 4), [nt] "i"(NT)
               : "memory");
}
template <int NT>
__device__ __forceinline__ void dma_adapt(int lag, v4i rs, u32 so, u32 la, u32 v0, u32 v1, u32 v2, u32 v3, u32 fa, int tn) {
  if (lag <= 0) dma_tile<FK_W, NT>(rs, so, la, v0, v1, v2, v3, fa, tn);
  else if (lag <= FK_LG) dma_tile<FK_WB, NT>(rs, so, la, v0, v1, v2, v3, fa, tn);
  else dma_tile<FK_WC, NT>(rs, so, la, v0, v1, v2, v3, fa, tn);
}
__device__ __forceinline__ bool lds_wait_ge(volatile LAS u32* w, u32 need, bool& dead) {
  for (u32 n = 0;; ++n) {
    if ((u32)__builtin_amdgcn_readfirstlane(*w) >= need) break;
    if (dead || n > SPIN_LDS) { dead = true; break; }
    __builtin_amdgcn_s_sleep(1);
  }
  CB();
  return true;
}

__global__ void __launch_bounds__(1024, 4) fused_block(Params p) {
  __shared__ __attribute__((aligned(1024))) unsigned char lds_raw[L_END];
  LAS unsigned char* L = (LAS unsigned char*)lds_raw;
  const int tid = threadIdx.x, lane = tid & 63, wave = __builtin_amdgcn_readfirstlane(tid >> 6), w = wave & 3, role = wave >> 2;
  const int g = lane >> 4, c = lane & 15;
  const int kvh = blockIdx.x & 7, b = blockIdx.x >> 6, s = (blockIdx.x >> 3) & 7, grp = b * 8 + kvh;
  const rsrc_t rw = mkr(p.ws, WS_END);
  volatile LAS u32* FLAG = (volatile LAS u32*)(L + L_FLAG);
  const bool ffn = s < 7;
  const int j = kvh * 7 + s;
  bool dead = false;
  STAMP(0);
#define DIAG(n) do {} while (0)

  if (role == 1) {
    __builtin_amdgcn_s_setprio(3);
    const u32 tb = (u32)__builtin_amdgcn_s_memrealtime() + FK_T0;
    const v4i rk = mkr_s(p.k + (size_t)grp * SK * 128, SK * 256), rv = mkr_s(p.v + (size_t)grp * SK * 128, SK * 256);
    const u32 k0 = g * 256 + ((c ^ g) << 4), k1 = g * 256 + ((c ^ (4 + g)) << 4), k2 = g * 256 + ((c ^ (8 + g)) << 4), k3 = g * 256 + ((c ^ (12 + g)) << 4);
    const u32 u0 = g * 256 + ((c ^ (g << 1)) << 4), u1 = g * 256 + ((c ^ ((4 + g) << 1)) << 4);
    const u32 ring = (u32)(size_t)(L + L_RING) + w * 32768, fa = (u32)(size_t)(FLAG + w);
    STAMP(5);
    if (lane < 4) FLAG[w + 4 * lane] = 0u;
    wg_barrier();
    STAMP(7);
    dma_tile<FK_W, FK_KNT>(rk, (u32)(s * 4 + w) * 4096u, ring, k0, k1, k2, k3, fa, 0);
    STAMP(6);
    dma_tile<FK_W, FK_VNT>(rv, (u32)(s * 4 + w) * 4096u, ring + 4096, u0, u1, u0, u1, fa, 1);
    STAMP(8);
    DIAG(1);
#pragma unroll 1
    for (int i = 1; i < NCH; ++i) {
      const u32 so = (u32)((i * 8 + s) * 4 + w) * 4096u, la = ring + (i & 3) * 8192;
      if (i >= 4) lds_wait_ge(FLAG + 4 + w, i - 3, dead);
      const int lag = (int)((u32)__builtin_amdgcn_s_memrealtime() - tb) - ((i * FK_TAU) >> 4);
      dma_adapt<FK_KNT>(lag, rk, so, la, k0, k1, k2, k3, fa, 2 * i);
      dma_adapt<FK_VNT>(lag, rv, so, la + 4096, u0, u1, u0, u1, fa, 2 * i + 1);
    }
    asm volatile("s_waitcnt vmcnt(8)\n\tds_write_b32 %0, %1\n\ts_waitcnt vmcnt(4)\n\tds_write_b32 %0, %2\n\ts_waitcnt vmcnt(0)\n\tds_write_b32 %0, %3" ::"v"(fa),
                 "v"(2 * NCH - 2), "v"(2 * NCH - 1), "v"(2 * NCH) : "memory");
    STAMP(1);
    DIAG(2);
    __builtin_amdgcn_s_setprio(0);
    v4u dr[8], df[8];
    const rsrc_t rd = mkr(p.wd, FF * DM * 2);
#pragma unroll
    for (int r = 0; r < 8; ++r) dr[r] = __builtin_amdgcn_raw_buffer_load_b128(rd, ffn ? (8 * g) * 1024 + 16 * c : OOB, (32 * j + r) * 1024 + 256 * w, WAUX);
    wg_barrier();
    const Tag T = mktag(FLAG[12]);
    LAS unsigned char* priv = L + L_QRED + w * 1024;
#pragma unroll
    for (int t = 0; t < 4; ++t)
#pragma unroll
      for (int q = 0; q < 4; ++q) { df[2 * q][t] = plo(dr[2 * t + 1][q], dr[2 * t][q]); df[2 * q + 1][t] = phi(dr[2 * t + 1][q], dr[2 * t][q]); }
    asm volatile("" : "+v"(df[0]), "+v"(df[1]), "+v"(df[2]), "+v"(df[3]), "+v"(df[4]), "+v"(df[5]), "+v"(df[6]), "+v"(df[7]));
    STAMP(2);
    wg_barrier();
    wg_barrier();
    wg_barrier();
    STAMP(3);
    if (ffn) {
      const LAS float* gu = (const LAS float*)(L + L_GU) + (lane & 31);
      const float gt = rb((gu[0] + gu[64]) + (gu[128] + gu[192])), up = rb((gu[32] + gu[96]) + (gu[160] + gu[224]));
      const float sg = rb(gt * __builtin_amdgcn_rcpf(1.f + __builtin_amdgcn_exp2f(-gt * L2E)));
      ((LAS u16*)priv)[lane & 31] = (u16)pk(sg * up, 0.f);
      const v4u a = *(const LAS v4u*)(priv + 16 * g);
      f4 o[8];
#pragma unroll
      for (int i = 0; i < 8; ++i) o[i] = mfma32(a, df[i], f4{0.f, 0.f, 0.f, 0.f});
      const bool hi = g & 1;
      const v4u ov = enc4(f4{hi ? o[4][0] : o[0][0], hi ? o[5][0] : o[1][0], hi ? o[6][0] : o[2][0], hi ? o[7][0] : o[3][0]}, T);
      st4(rw, lane < 32 ? WS_D + (u32)(b * 56 + j) * 2048 + (u32)(128 * w + 8 * c + 4 * g) * 4 : OOB, ov);
    }
    STAMP(4);
    FLUSH();
    return;
  }

  if (role == 2) {
    LAS unsigned char* priv = L + L_PRIV + w * 1024;
    wg_barrier();
    pause<FK_W0>(); pause<FK_W0>();
    v2u wor[4][8];
    const rsrc_t ro = mkr(p.wo, NQ * DM * 2);
#pragma unroll
    for (int u = 0; u < 4; ++u)
#pragma unroll
      for (int r = 0; r < 8; ++r) {
        wor[u][r] = __builtin_amdgcn_raw_buffer_load_b64(ro, g * 8 * 1024 + 8 * c, (kvh * 512 + 128 * w + 32 * u + r) * 1024 + 128 * s, WAUX);
        pause<FK_P1>();
      }
    v4u wf[4][4];
#pragma unroll
    for (int u = 0; u < 4; ++u) { tr84(wor[u], wf[u]); asm volatile("" : "+v"(wf[u][0]), "+v"(wf[u][1]), "+v"(wf[u][2]), "+v"(wf[u][3])); }
    lds_wait_ge(FLAG + 9, 4, dead);
    const u32 tg = FLAG[12];
    const Tag T = mktag(tg);
    STAMP(1);
    wg_barrier();
    {
      const u32 ob = WS_O + grp * 8 * OREC, om = WS_ML + (grp * 8 + (lane & 7)) * 128 + w * 16, oo = ob + (lane >> 5) * OREC + w * 512 + (lane & 31) * 16;
      pause<FK_D1>();
      STAMP(2);
      v4u o[4], ml;
      ml = ld4(rw, om);
      for (u32 n = 0; __any(bad4(ml, tg)); ++n) {
        if (dead || n > SPIN_MEM) { dead = true; break; }
        pause<FK_BO>();
        ml = ld4(rw, om);
      }
      STAMP(7);
#pragma unroll
      for (int t = 0; t < 4; ++t) o[t] = ld4(rw, oo, ((t + s) & 3) * 2 * OREC);
      float M = dec(ml.x);
      M = fmaxf(M, dppm<0xB1>(M)); M = fmaxf(M, dppm<0x4E>(M)); M = fmaxf(M, dppm<0x141>(M));
      const float e = __builtin_amdgcn_exp2f((dec(ml.x) - M) * L2E);
      float Ls = e * dec(ml.y);
      Ls += dppm<0xB1>(Ls); Ls += dppm<0x4E>(Ls); Ls += dppm<0x141>(Ls);
      const float en = e * __builtin_amdgcn_rcpf(Ls);
      float et[4];
#pragma unroll
      for (int t = 0; t < 4; ++t) {
        const float e0 = asf(__builtin_amdgcn_readlane(asu(en), 2 * ((t + s) & 3))), e1 = asf(__builtin_amdgcn_readlane(asu(en), 2 * ((t + s) & 3) + 1));
        et[t] = lane < 32 ? e0 : e1;
      }
      asm volatile("" : "+v"(et[0]), "+v"(et[1]), "+v"(et[2]), "+v"(et[3]));
      for (u32 n = 0;; ++n) {
        bool bad = false;
#pragma unroll
        for (int t = 0; t < 4; ++t) bad = bad || bad4(o[t], tg);
        if (!__any(bad)) break;
        if (dead || n > SPIN_MEM) { dead = true; break; }
        pause<FK_BO>();
#pragma unroll
        for (int t = 0; t < 4; ++t) if (half_any(bad4(o[t], tg), lane)) o[t] = ld4(rw, oo, ((t + s) & 3) * 2 * OREC);
      }
      STAMP(3);
      DIAG(6);
      f4 os = {0.f, 0.f, 0.f, 0.f};
#pragma unroll
      for (int t = 0; t < 4; ++t)
#pragma unroll
        for (int i = 0; i < 4; ++i) os[i] = fmaf(et[t], dec(o[t][i]), os[i]);
#pragma unroll
      for (int i = 0; i < 4; ++i) os[i] = x32_sum(os[i]);
      ((LAS v2u*)priv)[lane & 31] = v2u{pk(os[0], os[1]), pk(os[2], os[3])};
      f4 y[4];
#pragma unroll
      for (int i = 0; i < 4; ++i) y[i] = f4{0.f, 0.f, 0.f, 0.f};
#pragma unroll
      for (int u = 0; u < 4; ++u) {
        const v4u a = *(const LAS v4u*)(priv + (32 * u + 8 * g) * 2);
#pragma unroll
        for (int i = 0; i < 4; ++i) y[i] = mfma32(a, wf[u][i], y[i]);
      }
      if (lane < 16) *(LAS f4*)(L + L_YRED + w * 256 + c * 16) = f4{y[0][0], y[1][0], y[2][0], y[3][0]};
    }
    wg_barrier();
    {
      const LAS f4* yr = (const LAS f4*)(L + L_YRED) + c;
      const f4 y = (yr[0] + yr[16]) + (yr[32] + yr[48]);
      st4(rw, lane < 32 ? WS_Y + (u32)(2 * w + g) * 65536 + grp * 2048 + s * 256 + c * 16 : OOB, enc4(y, T));
    }
    STAMP(4);
    wg_barrier();
    wg_barrier();
    STAMP(6);
    FLUSH();
    return;
  }

  if (role == 3) {
    LAS unsigned char* priv = L + L_PRIV + 4096 + w * 1024;
    wg_barrier();
    if (w == 3 && (FK_WB2 == 2 || blockIdx.x < 8)) { pause<FK_WB2D>(); __builtin_amdgcn_fence(__ATOMIC_RELEASE, "agent"); asm volatile("s_waitcnt vmcnt(0)" ::: "memory"); }
    pause<FK_W0>(); pause<FK_W0>();
    const v2u xp = *(const GAS v2u*)(p.x + b * DM + 128 * w + 4 * (lane & 31));
    v2u gr[4][8];
    v4u gf[4][4];
    if (ffn) {
      const GAS unsigned char* gp = (const GAS unsigned char*)(c < 8 ? p.wg : p.wu) + (size_t)(128 * w + 8 * g) * (FF * 2) + 64 * j + 8 * (c & 7);
#pragma unroll
      for (int u = 0; u < 4; ++u)
#pragma unroll
        for (int r = 0; r < 8; ++r) {
          const GAS v2u* a = (const GAS v2u*)(gp + (size_t)(32 * u + r) * (FF * 2));
          gr[u][r] = FK_WNT ? __builtin_nontemporal_load(a) : *a;
          pause<FK_P2>();
        }
#pragma unroll
      for (int u = 0; u < 4; ++u) { tr84(gr[u], gf[u]); asm volatile("" : "+v"(gf[u][0]), "+v"(gf[u][1]), "+v"(gf[u][2]), "+v"(gf[u][3])); }
    }
    lds_wait_ge(FLAG + 9, 4, dead);
    const u32 tg = FLAG[12];
    if (blockIdx.x == 0) {
      const u32 tlo = FLAG[10];
      for (u32 n = 0;; ++n) {
        const v2u gv = ld2(rw, WS_QG + (u32)(lane >> 3) * 16384 + w * 4096 + (lane & 7) * 512);
        if (__all(gv.y == tlo)) break;
        if (dead || n > SPIN_MEM) { dead = true; break; }
        pause<8>();
      }
    }
    STAMP(1);
    wg_barrier();
    wg_barrier();
    {
      const u32 yb = WS_Y + (u32)kvh * 65536 + (u32)b * 8 * 2048 + (lane >> 5) * 2048 + w * 512 + (lane & 31) * 16;
      pause<FK_D2>();
      STAMP(2);
      v4u y[4];
#pragma unroll
      for (int k = 0; k < 4; ++k) y[k] = ld4(rw, yb, ((k + s) & 3) * 4096);
      for (u32 n = 0;; ++n) {
        bool bad = false;
#pragma unroll
        for (int k = 0; k < 4; ++k) bad = bad || bad4(y[k], tg);
        if (!__any(bad)) break;
        if (dead || n > SPIN_MEM) { dead = true; break; }
        pause<FK_BO>();
#pragma unroll
        for (int k = 0; k < 4; ++k) if (half_any(bad4(y[k], tg), lane)) y[k] = ld4(rw, yb, ((k + s) & 3) * 4096);
      }
      STAMP(3);
      DIAG(7);
      f4 a;
#pragma unroll
      for (int i = 0; i < 4; ++i) {
        const float p0 = dec(y[0][i]), p1 = dec(y[1][i]), p2 = dec(y[2][i]), p3 = dec(y[3][i]);
        a[i] = x32_sum((s & 1) ? (p3 + p0) + (p1 + p2) : (p0 + p1) + (p2 + p3));
      }
      const u32 a0 = pk(a[0], a[1]), a1 = pk(a[2], a[3]);
      ((LAS v2u*)(L + L_HS))[32 * w + (lane & 31)] = v2u{pk(blo(xp.x) + blo(a0), bhi(xp.x) + bhi(a0)), pk(blo(xp.y) + blo(a1), bhi(xp.y) + bhi(a1))};
    }
    wg_barrier();
    STAMP(4);
    if (ffn) {
      const v4u hn = layernorm8(*(const LAS v4u*)(L + L_HS + lane * 16));
      *(LAS v4u*)(priv + lane * 16) = hn;
      f4 acc[4];
#pragma unroll
      for (int i = 0; i < 4; ++i) acc[i] = f4{0.f, 0.f, 0.f, 0.f};
#pragma unroll
      for (int u = 0; u < 4; ++u) {
        const v4u a = *(const LAS v4u*)(priv + (128 * w + 32 * u + 8 * g) * 2);
#pragma unroll
        for (int i = 0; i < 4; ++i) acc[i] = mfma32(a, gf[u][i], acc[i]);
      }
      if (lane < 16) *(LAS f4*)(L + L_GU + w * 256 + c * 16) = f4{acc[0][0], acc[1][0], acc[2][0], acc[3][0]};
    }
    STAMP(5);
    DIAG(8);
    wg_barrier();
    if (blockIdx.x == 0 && w == 0) st2(rw, lane == 0 ? WS_E : OOB, v2u{FLAG[10], FLAG[11]});
    FLUSH();
    return;
  }

  v4u qf[4];
  u32 tg;
  {
    STAMP(6);
    v4u x4[4];
#pragma unroll
    for (int t = 0; t < 4; ++t) x4[t] = *(const GAS v4u*)(p.x + g * DM + 128 * t + 8 * c);
    const v4u xa = *(const GAS v4u*)(p.x + (c & 3) * DM + 128 * b + 32 * w + 8 * g);
    const rsrc_t rq = mkr(p.wq, DM * NQ * 2);
    v2u wr[8];
#pragma unroll
    for (int r = 0; r < 8; ++r)
      wr[r] = __builtin_amdgcn_raw_buffer_load_b64(rq, g * 8 * 8192 + 8 * c, (128 * b + 32 * w + r) * 8192 + (kvh * 512 + 64 * s) * 2, 2);
    const v2u e = ld2(rw, WS_E);
    wg_barrier();
    STAMP(10);
    const float piv = dppm<0x150>(blo(x4[0][0]));
    float s1 = 0.f, s2 = 0.f;
#pragma unroll
    for (int t = 0; t < 4; ++t)
#pragma unroll
      for (int i = 0; i < 4; ++i) { const float d0 = blo(x4[t][i]) - piv, d1 = bhi(x4[t][i]) - piv; s1 += d0 + d1; s2 = fmaf(d0, d0, fmaf(d1, d1, s2)); }
    const f2 st = ln_stats(piv, rsum16(s1), rsum16(s2));
    const f2 sa = {asf(__builtin_amdgcn_ds_bpermute((c & 3) * 64, asu(st.x))), asf(__builtin_amdgcn_ds_bpermute((c & 3) * 64, asu(st.y)))};
    const v4u a = ln_apply(xa, sa);
    v4u f[4];
    tr84(wr, f);
    f4 acc[4];
#pragma unroll
    for (int i = 0; i < 4; ++i) acc[i] = mfma32(a, f[i], f4{0.f, 0.f, 0.f, 0.f});
    if (lane < 16) {
#pragma unroll
      for (int r = 0; r < 4; ++r) *(LAS f4*)(L + L_QRED + w * 1024 + r * 256 + c * 16) = f4{acc[0][r], acc[1][r], acc[2][r], acc[3][r]};
    }
    const u64 epoch = ((u64)__builtin_amdgcn_readfirstlane(e.y) << 32 | __builtin_amdgcn_readfirstlane(e.x)) + 1;
    const u32 tlo = (u32)epoch, thi = (u32)(epoch >> 32) + tlo * 0x9E3779B1u;
    const u32 dl = tlo - (u32)(epoch >> 32);
    tg = ((tlo + 1u) & 0xffu) | (((tlo >> 8) + 3u * dl + 1u) & 0xffu) << 8 | (((tlo >> 16) + 5u * dl + 1u) & 0xffu) << 16 | ((tlo >> 24) + 7u * dl + 1u) << 24;
    if (w == 0 && lane == 0) { FLAG[10] = tlo; FLAG[11] = (u32)(epoch >> 32); FLAG[12] = tg; }
    asm volatile("s_waitcnt lgkmcnt(0)" ::: "memory");
    STAMP(11);
    u32 old = 0;
    if (lane == 0) old = __hip_atomic_fetch_add((LAS u32*)(L + L_FLAG) + 8, 1u, __ATOMIC_RELAXED, __HIP_MEMORY_SCOPE_WORKGROUP);
    old = __builtin_amdgcn_readfirstlane(old);
    CB();
    if (old == 3) {
      const LAS f2* qr = (const LAS f2*)(L + L_QRED) + g * 32 + c;
      const f2 q0 = (qr[0] + qr[128]) + (qr[256] + qr[384]), q1 = (qr[16] + qr[144]) + (qr[272] + qr[400]);
      const u32 off = WS_QG + (u32)(g * 8 + kvh) * 16384 + b * 4096 + s * 512 + c * 16;
      st4(rw, off, v4u{asu(q0.x), tlo, asu(q0.y), thi});
      st4(rw, off + 256, v4u{asu(q1.x), tlo, asu(q1.y), thi});
    }
    STAMP(1);
    pause<FK_D0>();
    v4u gq[4];
    const u32 qo = WS_QG + grp * 16384 + w * 1024 + lane * 16;
#pragma unroll
    for (int t = 0; t < 4; ++t) gq[t] = ld4(rw, qo, t * 4096);
    for (u32 n = 0;; ++n) {
      bool bad = false;
#pragma unroll
      for (int t = 0; t < 4; ++t) bad = bad || gq[t].y != tlo || gq[t].w != thi;
      if (!__any(bad)) break;
      if (dead || n > SPIN_MEM) { dead = true; break; }
      pause<2>();
#pragma unroll
      for (int t = 0; t < 4; ++t) if (__any(gq[t].y != tlo || gq[t].w != thi)) gq[t] = ld4(rw, qo, t * 4096);
    }
    STAMP(12);
    ((LAS u32*)(L + L_QS))[64 * w + lane] = pk((asf(gq[0].x) + asf(gq[1].x)) + (asf(gq[2].x) + asf(gq[3].x)), (asf(gq[0].z) + asf(gq[1].z)) + (asf(gq[2].z) + asf(gq[3].z)));
    asm volatile("s_waitcnt lgkmcnt(0)" ::: "memory");
    if (lane == 0) __hip_atomic_fetch_add((LAS u32*)(L + L_FLAG) + 9, 1u, __ATOMIC_RELAXED, __HIP_MEMORY_SCOPE_WORKGROUP);
    lds_wait_ge(FLAG + 9, 4, dead);
#pragma unroll
    for (int u = 0; u < 4; ++u) qf[u] = *(const LAS v4u*)(L + L_QS + ((c & 3) * 128 + 32 * u + 8 * g) * 2);
    STAMP(2);
    DIAG(4);
  }
  float m = -INFINITY, l = 0.f;
  f4 acc[8];
#pragma unroll
  for (int i = 0; i < 8; ++i) acc[i] = f4{0.f, 0.f, 0.f, 0.f};
  const LAS unsigned char* ringk = L + L_RING + w * 32768 + c * 256;
  const u32 vrow = 4 * g + (c >> 2);
  const u32 va = (u32)(size_t)(L + L_RING) + w * 32768 + 4096 + vrow * 256 + (vrow & 7) * 32 + 8 * (c & 3);
  u32 seen = 0;
  auto need = [&](u32 n) {
    for (u32 k = 0; seen < n; ++k) {
      seen = __builtin_amdgcn_readfirstlane(FLAG[w]);
      if (seen >= n) break;
      if (dead || k > SPIN_LDS) { dead = true; break; }
      __builtin_amdgcn_s_sleep(1);
    }
    CB();
  };
#pragma unroll 1
  for (int i = 0; i < NCH; ++i) {
    const u32 sl = (i & 3) * 8192;
    need(2 * i + 1);
    v4u kf[4];
#pragma unroll
    for (int u = 0; u < 4; ++u) kf[u] = *(const LAS v4u*)(ringk + sl + (((4 * u + g) ^ c) << 4));
    f4 s0 = mfma32(kf[0], qf[0], f4{0.f, 0.f, 0.f, 0.f}), s1 = mfma32(kf[2], qf[2], f4{0.f, 0.f, 0.f, 0.f});
    s0 = mfma32(kf[1], qf[1], s0); s1 = mfma32(kf[3], qf[3], s1);
    const u32 a01 = pk(s0[0] + s1[0], s0[1] + s1[1]), a23 = pk(s0[2] + s1[2], s0[3] + s1[3]);
    const u32 b01 = pk(blo(a01) * SCALE, bhi(a01) * SCALE), b23 = pk(blo(a23) * SCALE, bhi(a23) * SCALE);
    const float t0 = blo(b01), t1 = bhi(b01), t2 = blo(b23), t3 = bhi(b23);
    const float mx = xg_max(fmaxf(fmaxf(t0, t1), fmaxf(t2, t3)));
    if (__any(mx > m)) {
      const float mn = fmaxf(m, mx), al = __builtin_amdgcn_exp2f((m - mn) * L2E);
      l *= al; m = mn;
#pragma unroll
      for (int d = 0; d < 8; ++d) acc[d] *= al;
    }
    const float p0 = __builtin_amdgcn_exp2f((t0 - m) * L2E), p1 = __builtin_amdgcn_exp2f((t1 - m) * L2E),
                p2 = __builtin_amdgcn_exp2f((t2 - m) * L2E), p3 = __builtin_amdgcn_exp2f((t3 - m) * L2E);
    l += (p0 + p1) + (p2 + p3);
    const v2u pf = {pk(p0, p1), pk(p2, p3)};
    if (i == NCH - 1) {
      const float lt = xg_sum(l);
      if (lane < 4) *(LAS f2*)(L + L_MLS + w * 32 + c * 8) = f2{m, lt};
    }
    need(2 * i + 2);
    s4 vf[8];
#pragma unroll
    for (int d = 0; d < 8; ++d) vf[d] = __builtin_amdgcn_ds_read_tr16_b64_v4i16((LAS s4*)(size_t)((va ^ (d << 5)) + sl));
    asm volatile("s_waitcnt lgkmcnt(0)" ::: "memory");
    FLAG[4 + w] = i + 1;
#pragma unroll
    for (int d = 0; d < 8; ++d) acc[d] = mfma16(vf[d], pf, acc[d]);
  }
  STAMP(4);
  if (c < 4) {
#pragma unroll
    for (int d = 0; d < 8; ++d) *(LAS f4*)(L + L_OM + w * 2048 + c * 512 + (16 * d + 4 * g) * 4) = acc[d];
  }
  wg_barrier();
  {
    const int hd = lane < 32 ? w : (lane & 3);
    f2 ml[4]; f4 ov[4];
#pragma unroll
    for (int k = 0; k < 4; ++k) { ml[k] = *(const LAS f2*)(L + L_MLS + k * 32 + hd * 8); ov[k] = *(const LAS f4*)(L + L_OM + k * 2048 + w * 512 + (lane & 31) * 16); }
    const float M = fmaxf(fmaxf(ml[0].x, ml[1].x), fmaxf(ml[2].x, ml[3].x));
    float Ls = 0.f; f4 O = {0.f, 0.f, 0.f, 0.f};
#pragma unroll
    for (int k = 0; k < 4; ++k) { const float e = __builtin_amdgcn_exp2f((ml[k].x - M) * L2E); Ls = fmaf(e, ml[k].y, Ls); O += ov[k] * e; }
    const u32 wgi = grp * 8 + s;
    st4(rw, lane < 32 ? WS_O + wgi * OREC + w * 512 + lane * 16 : (lane < 40 && w == 0 ? WS_ML + wgi * 128 + (lane - 32) * 16 : OOB), enc4(lane < 32 ? O : f4{M, Ls, M, Ls}, mktag(tg)));
  }
  STAMP(5);
  DIAG(5);
  wg_barrier();
  wg_barrier();
  wg_barrier();
    if (w < 2) {
      const int col = (kvh * 8 + s) * 8 + 4 * w;
      const u32 off = lane < 56 ? WS_D + (u32)(b * 56 + lane) * 2048 + col * 4 : OOB;
      const v2u hv = *(const LAS v2u*)(L + L_HS + col * 2);
      const u32 tgl = lane < 56 ? tg : 0u;
      pause<FK_D3>();
      STAMP(7);
      v4u d0 = ld4(rw, off);
      for (u32 n = 0; __any(bad4(d0, tgl)); ++n) {
        if (dead || n > SPIN_MEM) { dead = true; break; }
        pause<FK_BO>();
        d0 = ld4(rw, off);
      }
      STAMP(8);
      const u32 a0 = pk(wsum63(dec(d0[0])), wsum63(dec(d0[1]))), a1 = pk(wsum63(dec(d0[2])), wsum63(dec(d0[3])));
      if (lane == 63) *(GAS v2u*)(p.out + b * DM + col) = v2u{pk(blo(hv.x) + blo(a0), bhi(hv.x) + bhi(a0)), pk(blo(hv.y) + blo(a1), bhi(hv.y) + bhi(a1))};
      STAMP(9);
    }
  FLUSH();
}
}

extern "C" void kernel_launch(void* const* d_in, const int* in_sizes, int n_in, void* d_out, int out_size, void* d_ws, size_t ws_size,
                              hipStream_t stream) {
  fk::Params p;
  p.x = (const fk::u16*)d_in[0]; p.k = (const fk::u16*)d_in[1]; p.v = (const fk::u16*)d_in[2];
  p.wq = (const fk::u16*)d_in[3]; p.wo = (const fk::u16*)d_in[4];
  p.wg = (const fk::u16*)d_in[6]; p.wu = (const fk::u16*)d_in[7]; p.wd = (const fk::u16*)d_in[8];
  p.out = (fk::u16*)d_out; p.ws = (unsigned char*)d_ws;
  hipLaunchKernelGGL(fk::fused_block, dim3(256), dim3(1024), 0, stream, p);
}
```

```cpp
#include <hip/hip_runtime.h>
#include <stdint.h>

#define FK_W 5
#define FK_WNT 1
#define FK_W0 120
#define FK_P1 18
#define FK_P2 22
#define FK_ORD 1
#define FK_G0 0
#define FK_WB 7
#define FK_WC 10
#define FK_LG 60
#define FK_T0 80
#define FK_TAU 1830
#define FK_DIAG 0
#define FK_DIAGB 0
#define FK_DIAGK 1
#define FK_DB2 0
#define FK_KNT 1
#define FK_VNT 1
#define FK_WB2 0
#define FK_WB2D 0
#define FK_PT 1
#define FK_DIAGW 0
#define FK_BO 1
#define FK_D0 40
#define FK_D1 18
#define FK_D2 16
#define FK_D3 32
#define FK_STAMPS 0

namespace fk {
#define GAS __attribute__((address_space(1)))
#define LAS __attribute__((address_space(3)))
typedef unsigned u32;
typedef unsigned long long u64;
typedef unsigned short u16;
typedef u32 v2u __attribute__((ext_vector_type(2)));
typedef u32 v4u __attribute__((ext_vector_type(4)));
typedef int v4i __attribute__((ext_vector_type(4)));
typedef float f2 __attribute__((ext_vector_type(2)));
typedef float f4 __attribute__((ext_vector_type(4)));
typedef short s4 __attribute__((ext_vector_type(4)));
typedef __bf16 b2 __attribute__((ext_vector_type(2)));
typedef __bf16 b8 __attribute__((ext_vector_type(8)));

constexpr int DM = 512, SK = 16384, FF = 1792, NQ = 4096;
constexpr int NCH = 32;
constexpr float L2E = 1.4426950408889634f, SCALE = 0.08838834764831845f, EPS = 1e-5f;
constexpr u32 WS_E = 0, WS_QG = 4096, WS_O = WS_QG + 32 * 16384, OREC = 2048, WS_ML = WS_O + 256 * OREC  , WS_Y = WS_ML + 256 * 128,
              WS_D = WS_Y + 8 * 65536, WS_END = WS_D + 4 * 56 * 2048, WS_DBG = WS_END;
constexpr u32 OOB = 0x80000000u;
constexpr u32 L_RING = 0, L_FLAG = 131072  , L_QRED = L_FLAG + 256  , L_QS = L_QRED + 4096,
              L_PRIV = L_QS + 1024  , L_OM = L_PRIV + 8192, L_MLS = L_OM + 8192, L_YRED = L_MLS + 128, L_HS = L_YRED + 1024,
              L_GU = L_HS + 1024, L_END = L_GU + 1024;
constexpr u32 SPIN_LDS = 1u << 19, SPIN_MEM = 1u << 16;

struct Params {
  const u16 *x, *k, *v, *wq, *wo, *wg, *wu, *wd;
  u16* out;
  unsigned char* ws;
};

#define CB() asm volatile("" ::: "memory")
__device__ __forceinline__ void wg_barrier() {
  asm volatile("s_waitcnt lgkmcnt(0)\n\ts_barrier" ::: "memory");
  __builtin_amdgcn_sched_barrier(0);
}
__device__ __forceinline__ float asf(u32 x) { return __builtin_bit_cast(float, x); }
__device__ __forceinline__ u32 asu(float x) { return __builtin_bit_cast(u32, x); }
__device__ __forceinline__ u32 pk(float lo, float hi) { f2 z = {lo, hi}; return __builtin_bit_cast(u32, __builtin_convertvector(z, b2)); }
__device__ __forceinline__ float blo(u32 p) { return asf(p << 16); }
__device__ __forceinline__ float bhi(u32 p) { return asf(p & 0xffff0000u); }
__device__ __forceinline__ float rb(float x) { return blo(pk(x, x)); }
template <int CTRL, int RM = 0xf> __device__ __forceinline__ float dpp0(float x) {
  return asf(__builtin_amdgcn_update_dpp(0, asu(x), CTRL, RM, 0xf, true));
}
template <int CTRL> __device__ __forceinline__ float dppm(float x) { return asf(__builtin_amdgcn_mov_dpp(asu(x), CTRL, 0xf, 0xf, true)); }
__device__ __forceinline__ float wsum63(float v) {
  v += dpp0<0x111>(v); v += dpp0<0x112>(v); v += dpp0<0x114>(v); v += dpp0<0x118>(v);
  v += asf(__builtin_amdgcn_update_dpp(0, asu(v), 0x142, 0xa, 0xf, false));
  v += asf(__builtin_amdgcn_update_dpp(0, asu(v), 0x143, 0xc, 0xf, false));
  return v;
}
__device__ __forceinline__ float wsum(float v) { return asf(__builtin_amdgcn_readlane(asu(wsum63(v)), 63)); }
__device__ __forceinline__ float xg_max(float x) {
  auto s = __builtin_amdgcn_permlane16_swap(asu(x), asu(x), false, false);
  x = fmaxf(asf(s[0]), asf(s[1]));
  auto t = __builtin_amdgcn_permlane32_swap(asu(x), asu(x), false, false);
  return fmaxf(asf(t[0]), asf(t[1]));
}
__device__ __forceinline__ float xg_sum(float x) {
  auto s = __builtin_amdgcn_permlane16_swap(asu(x), asu(x), false, false);
  x = asf(s[0]) + asf(s[1]);
  auto t = __builtin_amdgcn_permlane32_swap(asu(x), asu(x), false, false);
  return asf(t[0]) + asf(t[1]);
}
__device__ __forceinline__ float x32_sum(float x) {
  auto t = __builtin_amdgcn_permlane32_swap(asu(x), asu(x), false, false);
  return asf(t[0]) + asf(t[1]);
}
__device__ __forceinline__ f4 mfma32(v4u a, v4u b, f4 c) {
  return __builtin_amdgcn_mfma_f32_16x16x32_bf16(__builtin_bit_cast(b8, a), __builtin_bit_cast(b8, b), c, 0, 0, 0);
}
__device__ __forceinline__ f4 mfma16(s4 a, v2u b, f4 c) {
  return __builtin_amdgcn_mfma_f32_16x16x16bf16_1k(a, __builtin_bit_cast(s4, b), c, 0, 0, 0);
}
__device__ __forceinline__ u32 plo(u32 r1, u32 r0) { return __builtin_amdgcn_perm(r1, r0, 0x05040100u); }
__device__ __forceinline__ u32 phi(u32 r1, u32 r0) { return __builtin_amdgcn_perm(r1, r0, 0x07060302u); }
__device__ __forceinline__ void tr84(const v2u (&r)[8], v4u (&f)[4]) {
#pragma unroll
  for (int t = 0; t < 4; ++t) {
    f[0][t] = plo(r[2 * t + 1].x, r[2 * t].x); f[1][t] = phi(r[2 * t + 1].x, r[2 * t].x);
    f[2][t] = plo(r[2 * t + 1].y, r[2 * t].y); f[3][t] = phi(r[2 * t + 1].y, r[2 * t].y);
  }
}
typedef __amdgpu_buffer_rsrc_t rsrc_t;
__device__ __forceinline__ rsrc_t mkr(const void* p, u32 bytes) { return __builtin_amdgcn_make_buffer_rsrc((void*)p, 0, bytes, 0x00020000); }
__device__ __forceinline__ v4i mkr_s(const void* p, u32 bytes) {
  const u64 a = (u64)p; v4i r;
  r.x = __builtin_amdgcn_readfirstlane((u32)a); r.y = __builtin_amdgcn_readfirstlane((u32)(a >> 32)); r.z = (int)bytes; r.w = 0x00020000;
  return r;
}
constexpr int SC1 = 16, WAUX = FK_WNT ? 2 : 0;
__device__ __forceinline__ v2u ld2(rsrc_t r, u32 vo, u32 so = 0) { return __builtin_amdgcn_raw_buffer_load_b64(r, vo, so, SC1); }
__device__ __forceinline__ v4u ld4(rsrc_t r, u32 vo, u32 so = 0) { return __builtin_amdgcn_raw_buffer_load_b128(r, vo, so, SC1); }
__device__ __forceinline__ void st2(rsrc_t r, u32 vo, v2u v) { __builtin_amdgcn_raw_buffer_store_b64(v, r, vo, 0, SC1); }
__device__ __forceinline__ void st4(rsrc_t r, u32 vo, v4u v) { __builtin_amdgcn_raw_buffer_store_b128(v, r, vo, 0, SC1); }

#define STAMP(i) do {} while (0)
#define FLUSH() do {} while (0)

template <int N> __device__ __forceinline__ void pause() { if (N > 0) asm volatile("s_sleep %0" ::"i"(N) : "memory"); }
__device__ __forceinline__ float rsum16(float v) {
  v += dppm<0x128>(v); v += dppm<0x124>(v); v += dppm<0x122>(v); v += dppm<0x121>(v);
  return v;
}
struct Tag { u32 b[4]; };
__device__ __forceinline__ Tag mktag(u32 t) { return Tag{{t & 0xffu, (t >> 8) & 0xffu, (t >> 16) & 0xffu, t >> 24}}; }
__device__ __forceinline__ u32 enc(float x, u32 tb) { return ((asu(x) + 0x80u) & 0xffffff00u) | tb; }
__device__ __forceinline__ v4u enc4(f4 x, const Tag& t) { return v4u{enc(x[0], t.b[0]), enc(x[1], t.b[1]), enc(x[2], t.b[2]), enc(x[3], t.b[3])}; }
__device__ __forceinline__ float dec(u32 y) { return asf(y & 0xffffff00u); }
__device__ __forceinline__ bool bad2(v2u y, u32 t16) { return (__builtin_amdgcn_perm(y.y, y.x, 0x0c0c0400u)) != t16; }
__device__ __forceinline__ bool bad4(v4u y, u32 t32) {
  return __builtin_amdgcn_perm(__builtin_amdgcn_perm(y.w, y.z, 0x0c0c0400u), __builtin_amdgcn_perm(y.y, y.x, 0x0c0c0400u), 0x05040100u) != t32;
}
__device__ __forceinline__ bool half_any(bool x, int lane) { const u64 m = __ballot(x); return (lane < 32 ? (u32)m : (u32)(m >> 32)) != 0u; }
__device__ __forceinline__ f2 ln_stats(float piv, float s1, float s2) {
  const float md = s1 * (1.f / DM), e2 = s2 * (1.f / DM);
  const float var = rb(fmaxf(fmaf(-md, md, e2), 0.f));
  return f2{rb(piv + md), __builtin_amdgcn_rcpf(rb(__builtin_amdgcn_sqrtf(rb(var + EPS))))};
}
__device__ __forceinline__ v4u ln_apply(v4u xr, f2 st) {
  v4u o;
#pragma unroll
  for (int i = 0; i < 4; ++i) { const u32 d = pk(blo(xr[i]) - st.x, bhi(xr[i]) - st.x); o[i] = pk(blo(d) * st.y, bhi(d) * st.y); }
  return o;
}
__device__ __forceinline__ v4u layernorm8(v4u xr) {
  const float piv = asf(__builtin_amdgcn_readfirstlane(xr[0] << 16));
  float s1 = 0.f, s2 = 0.f;
#pragma unroll
  for (int i = 0; i < 4; ++i) { const float d0 = blo(xr[i]) - piv, d1 = bhi(xr[i]) - piv; s1 += d0 + d1; s2 = fmaf(d0, d0, fmaf(d1, d1, s2)); }
  s1 = wsum63(s1); s2 = wsum63(s2);
  return ln_apply(xr, ln_stats(piv, asf(__builtin_amdgcn_readlane(asu(s1), 63)), asf(__builtin_amdgcn_readlane(asu(s2), 63))));
}

#define FK_PIECE(T, VO) \
  "s_waitcnt vmcnt(%c[wm])\n\t" \
  ".if %c[pt] == " #T "\n\tds_write_b32 %[fa], %[fv]\n\t.endif\n\t" \
  ".if %c[nt]\n\tbuffer_load_dwordx4 " VO ", %[rs], %[so] offen offset:" #T "*1024 nt lds\n\t.else\n\tbuffer_load_dwordx4 " VO ", %[rs], %[so] offen offset:" #T "*1024 lds\n\t.endif\n\t"
template <int W, int NT>
__device__ __forceinline__ void dma_tile(v4i rs, u32 so, u32 la, u32 v0, u32 v1, u32 v2, u32 v3, u32 fa, int tn) {
  const int landed = tn - 1 - (W - 5) / 4;
  asm volatile("s_mov_b32 m0, %[la]\n\t" FK_PIECE(0, "%[v0]") FK_PIECE(1, "%[v1]") FK_PIECE(2, "%[v2]") FK_PIECE(3, "%[v3]")
               :
               : [rs] "s"(rs), [so] "s"(so), [la] "s"(la), [v0] "v"(v0), [v1] "v"(v1), [v2] "v"(v2), [v3] "v"(v3), [fa] "v"(fa),
                 [fv] "v"(landed > 0 ? landed : 0), [wm] "i"(W - 1), [pt] "i"((W - 5) % 4), [nt] "i"(NT)
               : "memory");
}
template <int NT>
__device__ __forceinline__ void dma_adapt(int lag, v4i rs, u32 so, u32 la, u32 v0, u32 v1, u32 v2, u32 v3, u32 fa, int tn) {
  if (lag <= 0) dma_tile<FK_W, NT>(rs, so, la, v0, v1, v2, v3, fa, tn);
  else if (lag <= FK_LG) dma_tile<FK_WB, NT>(rs, so, la, v0, v1, v2, v3, fa, tn);
  else dma_tile<FK_WC, NT>(rs, so, la, v0, v1, v2, v3, fa, tn);
}
__device__ __forceinline__ bool lds_wait_ge(volatile LAS u32* w, u32 need, bool& dead) {
  for (u32 n = 0;; ++n) {
    if ((u32)__builtin_amdgcn_readfirstlane(*w) >= need) break;
    if (dead || n > SPIN_LDS) { dead = true; break; }
    __builtin_amdgcn_s_sleep(1);
  }
  CB();
  return true;
}

__global__ void __launch_bounds__(1024, 4) fused_block(Params p) {
  __shared__ __attribute__((aligned(1024))) unsigned char lds_raw[L_END];
  LAS unsigned char* L = (LAS unsigned char*)lds_raw;
  const int tid = threadIdx.x, lane = tid & 63, wave = __builtin_amdgcn_readfirstlane(tid >> 6), w = wave & 3, role = wave >> 2;
  const int g = lane >> 4, c = lane & 15;
  const int kvh = blockIdx.x & 7, b = blockIdx.x >> 6, s = (blockIdx.x >> 3) & 7, grp = b * 8 + kvh;
  const rsrc_t rw = mkr(p.ws, WS_END);
  volatile LAS u32* FLAG = (volatile LAS u32*)(L + L_FLAG);
  const bool ffn = s < 7;
  const int j = kvh * 7 + s;
  bool dead = false;
  STAMP(0);
#define DIAG(n) do {} while (0)

  if (role == 1) {
    __builtin_amdgcn_s_setprio(3);
    const u32 tb = (u32)__builtin_amdgcn_s_memrealtime() + FK_T0;
    const v4i rk = mkr_s(p.k + (size_t)grp * SK * 128, SK * 256), rv = mkr_s(p.v + (size_t)grp * SK * 128, SK * 256);
    const u32 k0 = g * 256 + ((c ^ g) << 4), k1 = g * 256 + ((c ^ (4 + g)) << 4), k2 = g * 256 + ((c ^ (8 + g)) << 4), k3 = g * 256 + ((c ^ (12 + g)) << 4);
    const u32 u0 = g * 256 + ((c ^ (g << 1)) << 4), u1 = g * 256 + ((c ^ ((4 + g) << 1)) << 4);
    const u32 ring = (u32)(size_t)(L + L_RING) + w * 32768, fa = (u32)(size_t)(FLAG + w);
    STAMP(5);
    if (lane < 4) FLAG[w + 4 * lane] = 0u;
    wg_barrier();
    STAMP(7);
    dma_tile<FK_W, FK_KNT>(rk, (u32)(s * 4 + w) * 4096u, ring, k0, k1, k2, k3, fa, 0);
    STAMP(6);
    dma_tile<FK_W, FK_VNT>(rv, (u32)(s * 4 + w) * 4096u, ring + 4096, u0, u1, u0, u1, fa, 1);
    STAMP(8);
    DIAG(1);
#pragma unroll 1
    for (int i = 1; i < NCH; ++i) {
      const u32 so = (u32)((i * 8 + s) * 4 + w) * 4096u, la = ring + (i & 3) * 8192;
      if (i >= 4) lds_wait_ge(FLAG + 4 + w, i - 3, dead);
      const int lag = (int)((u32)__builtin_amdgcn_s_memrealtime() - tb) - ((i * FK_TAU) >> 4);
      dma_adapt<FK_KNT>(lag, rk, so, la, k0, k1, k2, k3, fa, 2 * i);
      dma_adapt<FK_VNT>(lag, rv, so, la + 4096, u0, u1, u0, u1, fa, 2 * i + 1);
    }
    asm volatile("s_waitcnt vmcnt(8)\n\tds_write_b32 %0, %1\n\ts_waitcnt vmcnt(4)\n\tds_write_b32 %0, %2\n\ts_waitcnt vmcnt(0)\n\tds_write_b32 %0, %3" ::"v"(fa),
                 "v"(2 * NCH - 2), "v"(2 * NCH - 1), "v"(2 * NCH) : "memory");
    STAMP(1);
    DIAG(2);
    __builtin_amdgcn_s_setprio(0);
    v4u dr[8], df[8];
    const rsrc_t rd = mkr(p.wd, FF * DM * 2);
#pragma unroll
    for (int r = 0; r < 8; ++r) dr[r] = __builtin_amdgcn_raw_buffer_load_b128(rd, ffn ? (8 * g) * 1024 + 16 * c : OOB, (32 * j + r) * 1024 + 256 * w, WAUX);
    wg_barrier();
    const Tag T = mktag(FLAG[12]);
    LAS unsigned char* priv = L + L_QRED + w * 1024;
#pragma unroll
    for (int t = 0; t < 4; ++t)
#pragma unroll
      for (int q = 0; q < 4; ++q) { df[2 * q][t] = plo(dr[2 * t + 1][q], dr[2 * t][q]); df[2 * q + 1][t] = phi(dr[2 * t + 1][q], dr[2 * t][q]); }
    asm volatile("" : "+v"(df[0]), "+v"(df[1]), "+v"(df[2]), "+v"(df[3]), "+v"(df[4]), "+v"(df[5]), "+v"(df[6]), "+v"(df[7]));
    STAMP(2);
    wg_barrier();
    wg_barrier();
    wg_barrier();
    STAMP(3);
    if (ffn) {
      const LAS float* gu = (const LAS float*)(L + L_GU) + (lane & 31);
      const float gt = rb((gu[0] + gu[64]) + (gu[128] + gu[192])), up = rb((gu[32] + gu[96]) + (gu[160] + gu[224]));
      const float sg = rb(gt * __builtin_amdgcn_rcpf(1.f + __builtin_amdgcn_exp2f(-gt * L2E)));
      ((LAS u16*)priv)[lane & 31] = (u16)pk(sg * up, 0.f);
      const v4u a = *(const LAS v4u*)(priv + 16 * g);
      f4 o[8];
#pragma unroll
      for (int i = 0; i < 8; ++i) o[i] = mfma32(a, df[i], f4{0.f, 0.f, 0.f, 0.f});
      const bool hi = g & 1;
      const v4u ov = enc4(f4{hi ? o[4][0] : o[0][0], hi ? o[5][0] : o[1][0], hi ? o[6][0] : o[2][0], hi ? o[7][0] : o[3][0]}, T);
      st4(rw, lane < 32 ? WS_D + (u32)(b * 56 + j) * 2048 + (u32)(128 * w + 8 * c + 4 * g) * 4 : OOB, ov);
    }
    STAMP(4);
    FLUSH();
    return;
  }

  if (role == 2) {
    LAS unsigned char* priv = L + L_PRIV + w * 1024;
    wg_barrier();
    pause<FK_W0>(); pause<FK_W0>();
    v2u wor[4][8];
    const rsrc_t ro = mkr(p.wo, NQ * DM * 2);
#pragma unroll
    for (int u = 0; u < 4; ++u)
#pragma unroll
      for (int r = 0; r < 8; ++r) {
        wor[u][r] = __builtin_amdgcn_raw_buffer_load_b64(ro, g * 8 * 1024 + 8 * c, (kvh * 512 + 128 * w + 32 * u + r) * 1024 + 128 * s, WAUX);
        pause<FK_P1>();
      }
    v4u wf[4][4];
#pragma unroll
    for (int u = 0; u < 4; ++u) { tr84(wor[u], wf[u]); asm volatile("" : "+v"(wf[u][0]), "+v"(wf[u][1]), "+v"(wf[u][2]), "+v"(wf[u][3])); }
    lds_wait_ge(FLAG + 9, 4, dead);
    const u32 tg = FLAG[12];
    const Tag T = mktag(tg);
    STAMP(1);
    wg_barrier();
    {
      const u32 ob = WS_O + grp * 8 * OREC, om = WS_ML + (grp * 8 + (lane & 7)) * 128 + w * 16, oo = ob + (lane >> 5) * OREC + w * 512 + (lane & 31) * 16;
      pause<FK_D1>();
      STAMP(2);
      v4u o[4], ml;
      ml = ld4(rw, om);
      for (u32 n = 0; __any(bad4(ml, tg)); ++n) {
        if (dead || n > SPIN_MEM) { dead = true; break; }
        pause<FK_BO>();
        ml = ld4(rw, om);
      }
      STAMP(7);
#pragma unroll
      for (int t = 0; t < 4; ++t) o[t] = ld4(rw, oo, ((t + s) & 3) * 2 * OREC);
      float M = dec(ml.x);
      M = fmaxf(M, dppm<0xB1>(M)); M = fmaxf(M, dppm<0x4E>(M)); M = fmaxf(M, dppm<0x141>(M));
      const float e = __builtin_amdgcn_exp2f((dec(ml.x) - M) * L2E);
      float Ls = e * dec(ml.y);
      Ls += dppm<0xB1>(Ls); Ls += dppm<0x4E>(Ls); Ls += dppm<0x141>(Ls);
      const float en = e * __builtin_amdgcn_rcpf(Ls);
      float et[4];
#pragma unroll
      for (int t = 0; t < 4; ++t) {
        const float e0 = asf(__builtin_amdgcn_readlane(asu(en), 2 * ((t + s) & 3))), e1 = asf(__builtin_amdgcn_readlane(asu(en), 2 * ((t + s) & 3) + 1));
        et[t] = lane < 32 ? e0 : e1;
      }
      asm volatile("" : "+v"(et[0]), "+v"(et[1]), "+v"(et[2]), "+v"(et[3]));
      for (u32 n = 0;; ++n) {
        bool bad = false;
#pragma unroll
        for (int t = 0; t < 4; ++t) bad = bad || bad4(o[t], tg);
        if (!__any(bad)) break;
        if (dead || n > SPIN_MEM) { dead = true; break; }
        pause<FK_BO>();
#pragma unroll
        for (int t = 0; t < 4; ++t) if (half_any(bad4(o[t], tg), lane)) o[t] = ld4(rw, oo, ((t + s) & 3) * 2 * OREC);
      }
      STAMP(3);
      DIAG(6);
      f4 os = {0.f, 0.f, 0.f, 0.f};
#pragma unroll
      for (int t = 0; t < 4; ++t)
#pragma unroll
        for (int i = 0; i < 4; ++i) os[i] = fmaf(et[t], dec(o[t][i]), os[i]);
#pragma unroll
      for (int i = 0; i < 4; ++i) os[i] = x32_sum(os[i]);
      ((LAS v2u*)priv)[lane & 31] = v2u{pk(os[0], os[1]), pk(os[2], os[3])};
      f4 y[4];
#pragma unroll
      for (int i = 0; i < 4; ++i) y[i] = f4{0.f, 0.f, 0.f, 0.f};
#pragma unroll
      for (int u = 0; u < 4; ++u) {
        const v4u a = *(const LAS v4u*)(priv + (32 * u + 8 * g) * 2);
#pragma unroll
        for (int i = 0; i < 4; ++i) y[i] = mfma32(a, wf[u][i], y[i]);
      }
      if (lane < 16) *(LAS f4*)(L + L_YRED + w * 256 + c * 16) = f4{y[0][0], y[1][0], y[2][0], y[3][0]};
    }
    wg_barrier();
    {
      const LAS f4* yr = (const LAS f4*)(L + L_YRED) + c;
      const f4 y = (yr[0] + yr[16]) + (yr[32] + yr[48]);
      st4(rw, lane < 32 ? WS_Y + (u32)(2 * w + g) * 65536 + grp * 2048 + s * 256 + c * 16 : OOB, enc4(y, T));
    }
    STAMP(4);
    wg_barrier();
    wg_barrier();
    STAMP(6);
    FLUSH();
    return;
  }

  if (role == 3) {
    LAS unsigned char* priv = L + L_PRIV + 4096 + w * 1024;
    wg_barrier();
    pause<FK_W0>(); pause<FK_W0>();
    const v2u xp = *(const GAS v2u*)(p.x + b * DM + 128 * w + 4 * (lane & 31));
    v2u gr[4][8];
    v4u gf[4][4];
    if (ffn) {
      const GAS unsigned char* gp = (const GAS unsigned char*)(c < 8 ? p.wg : p.wu) + (size_t)(128 * w + 8 * g) * (FF * 2) + 64 * j + 8 * (c & 7);
#pragma unroll
      for (int u = 0; u < 4; ++u)
#pragma unroll
        for (int r = 0; r < 8; ++r) {
          const GAS v2u* a = (const GAS v2u*)(gp + (size_t)(32 * u + r) * (FF * 2));
          gr[u][r] = FK_WNT ? __builtin_nontemporal_load(a) : *a;
          pause<FK_P2>();
        }
#pragma unroll
      for (int u = 0; u < 4; ++u) { tr84(gr[u], gf[u]); asm volatile("" : "+v"(gf[u][0]), "+v"(gf[u][1]), "+v"(gf[u][2]), "+v"(gf[u][3])); }
    }
    lds_wait_ge(FLAG + 9, 4, dead);
    const u32 tg = FLAG[12];
    if (blockIdx.x == 0) {
      const u32 tlo = FLAG[10];
      for (u32 n = 0;; ++n) {
        const v2u gv = ld2(rw, WS_QG + (u32)(lane >> 3) * 16384 + w * 4096 + (lane & 7) * 512);
        if (__all(gv.y == tlo)) break;
        if (dead || n > SPIN_MEM) { dead = true; break; }
        pause<8>();
      }
    }
    STAMP(1);
    wg_barrier();
    wg_barrier();
    {
      const u32 yb = WS_Y + (u32)kvh * 65536 + (u32)b * 8 * 2048 + (lane >> 5) * 2048 + w * 512 + (lane & 31) * 16;
      pause<FK_D2>();
      STAMP(2);
      v4u y[4];
#pragma unroll
      for (int k = 0; k < 4; ++k) y[k] = ld4(rw, yb, ((k + s) & 3) * 4096);
      for (u32 n = 0;; ++n) {
        bool bad = false;
#pragma unroll
        for (int k = 0; k < 4; ++k) bad = bad || bad4(y[k], tg);
        if (!__any(bad)) break;
        if (dead || n > SPIN_MEM) { dead = true; break; }
        pause<FK_BO>();
#pragma unroll
        for (int k = 0; k < 4; ++k) if (half_any(bad4(y[k], tg), lane)) y[k] = ld4(rw, yb, ((k + s) & 3) * 4096);
      }
      STAMP(3);
      DIAG(7);
      f4 a;
#pragma unroll
      for (int i = 0; i < 4; ++i) {
        const float p0 = dec(y[0][i]), p1 = dec(y[1][i]), p2 = dec(y[2][i]), p3 = dec(y[3][i]);
        a[i] = x32_sum((s & 1) ? (p3 + p0) + (p1 + p2) : (p0 + p1) + (p2 + p3));
      }
      const u32 a0 = pk(a[0], a[1]), a1 = pk(a[2], a[3]);
      ((LAS v2u*)(L + L_HS))[32 * w + (lane & 31)] = v2u{pk(blo(xp.x) + blo(a0), bhi(xp.x) + bhi(a0)), pk(blo(xp.y) + blo(a1), bhi(xp.y) + bhi(a1))};
    }
    wg_barrier();
    STAMP(4);
    if (ffn) {
      const v4u hn = layernorm8(*(const LAS v4u*)(L + L_HS + lane * 16));
      *(LAS v4u*)(priv + lane * 16) = hn;
      f4 acc[4];
#pragma unroll
      for (int i = 0; i < 4; ++i) acc[i] = f4{0.f, 0.f, 0.f, 0.f};
#pragma unroll
      for (int u = 0; u < 4; ++u) {
        const v4u a = *(const LAS v4u*)(priv + (128 * w + 32 * u + 8 * g) * 2);
#pragma unroll
        for (int i = 0; i < 4; ++i) acc[i] = mfma32(a, gf[u][i], acc[i]);
      }
      if (lane < 16) *(LAS f4*)(L + L_GU + w * 256 + c * 16) = f4{acc[0][0], acc[1][0], acc[2][0], acc[3][0]};
    }
    STAMP(5);
    DIAG(8);
    wg_barrier();
    if (blockIdx.x == 0 && w == 0) st2(rw, lane == 0 ? WS_E : OOB, v2u{FLAG[10], FLAG[11]});
    FLUSH();
    return;
  }

  v4u qf[4];
  u32 tg;
  {
    STAMP(6);
    v4u x4[4];
#pragma unroll
    for (int t = 0; t < 4; ++t) x4[t] = *(const GAS v4u*)(p.x + g * DM + 128 * t + 8 * c);
    const v4u xa = *(const GAS v4u*)(p.x + (c & 3) * DM + 128 * b + 32 * w + 8 * g);
    const rsrc_t rq = mkr(p.wq, DM * NQ * 2);
    v2u wr[8];
#pragma unroll
    for (int r = 0; r < 8; ++r)
      wr[r] = __builtin_amdgcn_raw_buffer_load_b64(rq, g * 8 * 8192 + 8 * c, (128 * b + 32 * w + r) * 8192 + (kvh * 512 + 64 * s) * 2, 2);
    const v2u e = ld2(rw, WS_E);
    wg_barrier();
    STAMP(10);
    const float piv = dppm<0x150>(blo(x4[0][0]));
    float s1 = 0.f, s2 = 0.f;
#pragma unroll
    for (int t = 0; t < 4; ++t)
#pragma unroll
      for (int i = 0; i < 4; ++i) { const float d0 = blo(x4[t][i]) - piv, d1 = bhi(x4[t][i]) - piv; s1 += d0 + d1; s2 = fmaf(d0, d0, fmaf(d1, d1, s2)); }
    const f2 st = ln_stats(piv, rsum16(s1), rsum16(s2));
    const f2 sa = {asf(__builtin_amdgcn_ds_bpermute((c & 3) * 64, asu(st.x))), asf(__builtin_amdgcn_ds_bpermute((c & 3) * 64, asu(st.y)))};
    const v4u a = ln_apply(xa, sa);
    v4u f[4];
    tr84(wr, f);
    f4 acc[4];
#pragma unroll
    for (int i = 0; i < 4; ++i) acc[i] = mfma32(a, f[i], f4{0.f, 0.f, 0.f, 0.f});
    if (lane < 16) {
#pragma unroll
      for (int r = 0; r < 4; ++r) *(LAS f4*)(L + L_QRED + w * 1024 + r * 256 + c * 16) = f4{acc[0][r], acc[1][r], acc[2][r], acc[3][r]};
    }
    const u64 epoch = ((u64)__builtin_amdgcn_readfirstlane(e.y) << 32 | __builtin_amdgcn_readfirstlane(e.x)) + 1;
    const u32 tlo = (u32)epoch, thi = (u32)(epoch >> 32) + tlo * 0x9E3779B1u;
    const u32 dl = tlo - (u32)(epoch >> 32);
    tg = ((tlo + 1u) & 0xffu) | (((tlo >> 8) + 3u * dl + 1u) & 0xffu) << 8 | (((tlo >> 16) + 5u * dl + 1u) & 0xffu) << 16 | ((tlo >> 24) + 7u * dl + 1u) << 24;
    if (w == 0 && lane == 0) { FLAG[10] = tlo; FLAG[11] = (u32)(epoch >> 32); FLAG[12] = tg; }
    asm volatile("s_waitcnt lgkmcnt(0)" ::: "memory");
    STAMP(11);
    u32 old = 0;
    if (lane == 0) old = __hip_atomic_fetch_add((LAS u32*)(L + L_FLAG) + 8, 1u, __ATOMIC_RELAXED, __HIP_MEMORY_SCOPE_WORKGROUP);
    old = __builtin_amdgcn_readfirstlane(old);
    CB();
    if (old == 3) {
      const LAS f2* qr = (const LAS f2*)(L + L_QRED) + g * 32 + c;
      const f2 q0 = (qr[0] + qr[128]) + (qr[256] + qr[384]), q1 = (qr[16] + qr[144]) + (qr[272] + qr[400]);
      const u32 off = WS_QG + (u32)(g * 8 + kvh) * 16384 + b * 4096 + s * 512 + c * 16;
      st4(rw, off, v4u{asu(q0.x), tlo, asu(q0.y), thi});
      st4(rw, off + 256, v4u{asu(q1.x), tlo, asu(q1.y), thi});
    }
    STAMP(1);
    pause<FK_D0>();
    v4u gq[4];
    const u32 qo = WS_QG + grp * 16384 + w * 1024 + lane * 16;
#pragma unroll
    for (int t = 0; t < 4; ++t) gq[t] = ld4(rw, qo, t * 4096);
    for (u32 n = 0;; ++n) {
      bool bad = false;
#pragma unroll
      for (int t = 0; t < 4; ++t) bad = bad || gq[t].y != tlo || gq[t].w != thi;
      if (!__any(bad)) break;
      if (dead || n > SPIN_MEM) { dead = true; break; }
      pause<2>();
#pragma unroll
      for (int t = 0; t < 4; ++t) if (__any(gq[t].y != tlo || gq[t].w != thi)) gq[t] = ld4(rw, qo, t * 4096);
    }
    STAMP(12);
    ((LAS u32*)(L + L_QS))[64 * w + lane] = pk((asf(gq[0].x) + asf(gq[1].x)) + (asf(gq[2].x) + asf(gq[3].x)), (asf(gq[0].z) + asf(gq[1].z)) + (asf(gq[2].z) + asf(gq[3].z)));
    asm volatile("s_waitcnt lgkmcnt(0)" ::: "memory");
    if (lane == 0) __hip_atomic_fetch_add((LAS u32*)(L + L_FLAG) + 9, 1u, __ATOMIC_RELAXED, __HIP_MEMORY_SCOPE_WORKGROUP);
    lds_wait_ge(FLAG + 9, 4, dead);
#pragma unroll
    for (int u = 0; u < 4; ++u) qf[u] = *(const LAS v4u*)(L + L_QS + ((c & 3) * 128 + 32 * u + 8 * g) * 2);
    { const u32 t = *(const volatile GAS u32*)(p.out + b * DM + (kvh * 8 + s) * 8); asm volatile("" :: "v"(t)); }
    STAMP(2);
    DIAG(4);
  }
  float m = -INFINITY, l = 0.f;
  f4 acc[8];
#pragma unroll
  for (int i = 0; i < 8; ++i) acc[i] = f4{0.f, 0.f, 0.f, 0.f};
  const LAS unsigned char* ringk = L + L_RING + w * 32768 + c * 256;
  const u32 vrow = 4 * g + (c >> 2);
  const u32 va = (u32)(size_t)(L + L_RING) + w * 32768 + 4096 + vrow * 256 + (vrow & 7) * 32 + 8 * (c & 3);
  u32 seen = 0;
  auto need = [&](u32 n) {
    for (u32 k = 0; seen < n; ++k) {
      seen = __builtin_amdgcn_readfirstlane(FLAG[w]);
      if (seen >= n) break;
      if (dead || k > SPIN_LDS) { dead = true; break; }
      __builtin_amdgcn_s_sleep(1);
    }
    CB();
  };
#pragma unroll 1
  for (int i = 0; i < NCH; ++i) {
    const u32 sl = (i & 3) * 8192;
    need(2 * i + 1);
    v4u kf[4];
#pragma unroll
    for (int u = 0; u < 4; ++u) kf[u] = *(const LAS v4u*)(ringk + sl + (((4 * u + g) ^ c) << 4));
    f4 s0 = mfma32(kf[0], qf[0], f4{0.f, 0.f, 0.f, 0.f}), s1 = mfma32(kf[2], qf[2], f4{0.f, 0.f, 0.f, 0.f});
    s0 = mfma32(kf[1], qf[1], s0); s1 = mfma32(kf[3], qf[3], s1);
    const u32 a01 = pk(s0[0] + s1[0], s0[1] + s1[1]), a23 = pk(s0[2] + s1[2], s0[3] + s1[3]);
    const u32 b01 = pk(blo(a01) * SCALE, bhi(a01) * SCALE), b23 = pk(blo(a23) * SCALE, bhi(a23) * SCALE);
    const float t0 = blo(b01), t1 = bhi(b01), t2 = blo(b23), t3 = bhi(b23);
    const float mx = xg_max(fmaxf(fmaxf(t0, t1), fmaxf(t2, t3)));
    if (__any(mx > m)) {
      const float mn = fmaxf(m, mx), al = __builtin_amdgcn_exp2f((m - mn) * L2E);
      l *= al; m = mn;
#pragma unroll
      for (int d = 0; d < 8; ++d) acc[d] *= al;
    }
    const float p0 = __builtin_amdgcn_exp2f((t0 - m) * L2E), p1 = __builtin_amdgcn_exp2f((t1 - m) * L2E),
                p2 = __builtin_amdgcn_exp2f((t2 - m) * L2E), p3 = __builtin_amdgcn_exp2f((t3 - m) * L2E);
    l += (p0 + p1) + (p2 + p3);
    const v2u pf = {pk(p0, p1), pk(p2, p3)};
    if (i == NCH - 1) {
      const float lt = xg_sum(l);
      if (lane < 4) *(LAS f2*)(L + L_MLS + w * 32 + c * 8) = f2{m, lt};
    }
    need(2 * i + 2);
    s4 vf[8];
#pragma unroll
    for (int d = 0; d < 8; ++d) vf[d] = __builtin_amdgcn_ds_read_tr16_b64_v4i16((LAS s4*)(size_t)((va ^ (d << 5)) + sl));
    asm volatile("s_waitcnt lgkmcnt(0)" ::: "memory");
    FLAG[4 + w] = i + 1;
#pragma unroll
    for (int d = 0; d < 8; ++d) acc[d] = mfma16(vf[d], pf, acc[d]);
  }
  STAMP(4);
  if (c < 4) {
#pragma unroll
    for (int d = 0; d < 8; ++d) *(LAS f4*)(L + L_OM + w * 2048 + c * 512 + (16 * d + 4 * g) * 4) = acc[d];
  }
  wg_barrier();
  {
    const int hd = lane < 32 ? w : (lane & 3);
    f2 ml[4]; f4 ov[4];
#pragma unroll
    for (int k = 0; k < 4; ++k) { ml[k] = *(const LAS f2*)(L + L_MLS + k * 32 + hd * 8); ov[k] = *(const LAS f4*)(L + L_OM + k * 2048 + w * 512 + (lane & 31) * 16); }
    const float M = fmaxf(fmaxf(ml[0].x, ml[1].x), fmaxf(ml[2].x, ml[3].x));
    float Ls = 0.f; f4 O = {0.f, 0.f, 0.f, 0.f};
#pragma unroll
    for (int k = 0; k < 4; ++k) { const float e = __builtin_amdgcn_exp2f((ml[k].x - M) * L2E); Ls = fmaf(e, ml[k].y, Ls); O += ov[k] * e; }
    const u32 wgi = grp * 8 + s;
    st4(rw, lane < 32 ? WS_O + wgi * OREC + w * 512 + lane * 16 : (lane < 40 && w == 0 ? WS_ML + wgi * 128 + (lane - 32) * 16 : OOB), enc4(lane < 32 ? O : f4{M, Ls, M, Ls}, mktag(tg)));
  }
  STAMP(5);
  DIAG(5);
  wg_barrier();
  wg_barrier();
  wg_barrier();
    if (w < 2) {
      const int col = (kvh * 8 + s) * 8 + 4 * w;
      const u32 off = lane < 56 ? WS_D + (u32)(b * 56 + lane) * 2048 + col * 4 : OOB;
      const v2u hv = *(const LAS v2u*)(L + L_HS + col * 2);
      const u32 tgl = lane < 56 ? tg : 0u;
      pause<FK_D3>();
      STAMP(7);
      v4u d0 = ld4(rw, off);
      for (u32 n = 0; __any(bad4(d0, tgl)); ++n) {
        if (dead || n > SPIN_MEM) { dead = true; break; }
        pause<FK_BO>();
        d0 = ld4(rw, off);
      }
      STAMP(8);
      const u32 a0 = pk(wsum63(dec(d0[0])), wsum63(dec(d0[1]))), a1 = pk(wsum63(dec(d0[2])), wsum63(dec(d0[3])));
      if (lane == 63) *(GAS v2u*)(p.out + b * DM + col) = v2u{pk(blo(hv.x) + blo(a0), bhi(hv.x) + bhi(a0)), pk(blo(hv.y) + blo(a1), bhi(hv.y) + bhi(a1))};
      STAMP(9);
    }
  FLUSH();
}
}

extern "C" void kernel_launch(void* const* d_in, const int* in_sizes, int n_in, void* d_out, int out_size, void* d_ws, size_t ws_size,
                              hipStream_t stream) {
  fk::Params p;
  p.x = (const fk::u16*)d_in[0]; p.k = (const fk::u16*)d_in[1]; p.v = (const fk::u16*)d_in[2];
  p.wq = (const fk::u16*)d_in[3]; p.wo = (const fk::u16*)d_in[4];
  p.wg = (const fk::u16*)d_in[6]; p.wu = (const fk::u16*)d_in[7]; p.wd = (const fk::u16*)d_in[8];
  p.out = (fk::u16*)d_out; p.ws = (unsigned char*)d_ws;
  hipLaunchKernelGGL(fk::fused_block, dim3(256), dim3(1024), 0, stream, p);
}
```

```cpp
#include <hip/hip_runtime.h>
#include <stdint.h>

#define FK_W 5
#define FK_WNT 1
#define FK_W0 120
#define FK_P1 18
#define FK_P2 22
#define FK_ORD 1
#define FK_G0 0
#define FK_WB 7
#define FK_WC 10
#define FK_LG 60
#define FK_T0 80
#define FK_TAU 1830
#define FK_DIAG 9
#define FK_DIAGB 0
#define FK_DIAGK 10
#define FK_DB2 0
#define FK_KNT 1
#define FK_VNT 1
#define FK_WB2 0
#define FK_WB2D 0
#define FK_PT 0
#define FK_DIAGW 0
#define FK_BO 1
#define FK_D0 40
#define FK_D1 18
#define FK_D2 16
#define FK_D3 32
#define FK_STAMPS 0

namespace fk {
#define GAS __attribute__((address_space(1)))
#define LAS __attribute__((address_space(3)))
typedef unsigned u32;
typedef unsigned long long u64;
typedef unsigned short u16;
typedef u32 v2u __attribute__((ext_vector_type(2)));
typedef u32 v4u __attribute__((ext_vector_type(4)));
typedef int v4i __attribute__((ext_vector_type(4)));
typedef float f2 __attribute__((ext_vector_type(2)));
typedef float f4 __attribute__((ext_vector_type(4)));
typedef short s4 __attribute__((ext_vector_type(4)));
typedef __bf16 b2 __attribute__((ext_vector_type(2)));
typedef __bf16 b8 __attribute__((ext_vector_type(8)));

constexpr int DM = 512, SK = 16384, FF = 1792, NQ = 4096;
constexpr int NCH = 32;
constexpr float L2E = 1.4426950408889634f, SCALE = 0.08838834764831845f, EPS = 1e-5f;
constexpr u32 WS_E = 0, WS_QG = 4096, WS_O = WS_QG + 32 * 16384, OREC = 2048, WS_ML = WS_O + 256 * OREC  , WS_Y = WS_ML + 256 * 128,
              WS_D = WS_Y + 8 * 65536, WS_END = WS_D + 4 * 56 * 2048, WS_DBG = WS_END;
constexpr u32 OOB = 0x80000000u;
constexpr u32 L_RING = 0, L_FLAG = 131072  , L_QRED = L_FLAG + 256  , L_QS = L_QRED + 4096,
              L_PRIV = L_QS + 1024  , L_OM = L_PRIV + 8192, L_MLS = L_OM + 8192, L_YRED = L_MLS + 128, L_HS = L_YRED + 1024,
              L_GU = L_HS + 1024, L_END = L_GU + 1024;
constexpr u32 SPIN_LDS = 1u << 19, SPIN_MEM = 1u << 16;

struct Params {
  const u16 *x, *k, *v, *wq, *wo, *wg, *wu, *wd;
  u16* out;
  unsigned char* ws;
};

#define CB() asm volatile("" ::: "memory")
__device__ __forceinline__ void wg_barrier() {
  asm volatile("s_waitcnt lgkmcnt(0)\n\ts_barrier" ::: "memory");
  __builtin_amdgcn_sched_barrier(0);
}
__device__ __forceinline__ float asf(u32 x) { return __builtin_bit_cast(float, x); }
__device__ __forceinline__ u32 asu(float x) { return __builtin_bit_cast(u32, x); }
__device__ __forceinline__ u32 pk(float lo, float hi) { f2 z = {lo, hi}; return __builtin_bit_cast(u32, __builtin_convertvector(z, b2)); }
__device__ __forceinline__ float blo(u32 p) { return asf(p << 16); }
__device__ __forceinline__ float bhi(u32 p) { return asf(p & 0xffff0000u); }
__device__ __forceinline__ float rb(float x) { return blo(pk(x, x)); }
template <int CTRL, int RM = 0xf> __device__ __forceinline__ float dpp0(float x) {
  return asf(__builtin_amdgcn_update_dpp(0, asu(x), CTRL, RM, 0xf, true));
}
template <int CTRL> __device__ __forceinline__ float dppm(float x) { return asf(__builtin_amdgcn_mov_dpp(asu(x), CTRL, 0xf, 0xf, true)); }
__device__ __forceinline__ float wsum63(float v) {
  v += dpp0<0x111>(v); v += dpp0<0x112>(v); v += dpp0<0x114>(v); v += dpp0<0x118>(v);
  v += asf(__builtin_amdgcn_update_dpp(0, asu(v), 0x142, 0xa, 0xf, false));
  v += asf(__builtin_amdgcn_update_dpp(0, asu(v), 0x143, 0xc, 0xf, false));
  return v;
}
__device__ __forceinline__ float wsum(float v) { return asf(__builtin_amdgcn_readlane(asu(wsum63(v)), 63)); }
__device__ __forceinline__ float xg_max(float x) {
  auto s = __builtin_amdgcn_permlane16_swap(asu(x), asu(x), false, false);
  x = fmaxf(asf(s[0]), asf(s[1]));
  auto t = __builtin_amdgcn_permlane32_swap(asu(x), asu(x), false, false);
  return fmaxf(asf(t[0]), asf(t[1]));
}
__device__ __forceinline__ float xg_sum(float x) {
  auto s = __builtin_amdgcn_permlane16_swap(asu(x), asu(x), false, false);
  x = asf(s[0]) + asf(s[1]);
  auto t = __builtin_amdgcn_permlane32_swap(asu(x), asu(x), false, false);
  return asf(t[0]) + asf(t[1]);
}
__device__ __forceinline__ float x32_sum(float x) {
  auto t = __builtin_amdgcn_permlane32_swap(asu(x), asu(x), false, false);
  return asf(t[0]) + asf(t[1]);
}
__device__ __forceinline__ f4 mfma32(v4u a, v4u b, f4 c) {
  return __builtin_amdgcn_mfma_f32_16x16x32_bf16(__builtin_bit_cast(b8, a), __builtin_bit_cast(b8, b), c, 0, 0, 0);
}
__device__ __forceinline__ f4 mfma16(s4 a, v2u b, f4 c) {
  return __builtin_amdgcn_mfma_f32_16x16x16bf16_1k(a, __builtin_bit_cast(s4, b), c, 0, 0, 0);
}
__device__ __forceinline__ u32 plo(u32 r1, u32 r0) { return __builtin_amdgcn_perm(r1, r0, 0x05040100u); }
__device__ __forceinline__ u32 phi(u32 r1, u32 r0) { return __builtin_amdgcn_perm(r1, r0, 0x07060302u); }
__device__ __forceinline__ void tr84(const v2u (&r)[8], v4u (&f)[4]) {
#pragma unroll
  for (int t = 0; t < 4; ++t) {
    f[0][t] = plo(r[2 * t + 1].x, r[2 * t].x); f[1][t] = phi(r[2 * t + 1].x, r[2 * t].x);
    f[2][t] = plo(r[2 * t + 1].y, r[2 * t].y); f[3][t] = phi(r[2 * t + 1].y, r[2 * t].y);
  }
}
typedef __amdgpu_buffer_rsrc_t rsrc_t;
__device__ __forceinline__ rsrc_t mkr(const void* p, u32 bytes) { return __builtin_amdgcn_make_buffer_rsrc((void*)p, 0, bytes, 0x00020000); }
__device__ __forceinline__ v4i mkr_s(const void* p, u32 bytes) {
  const u64 a = (u64)p; v4i r;
  r.x = __builtin_amdgcn_readfirstlane((u32)a); r.y = __builtin_amdgcn_readfirstlane((u32)(a >> 32)); r.z = (int)bytes; r.w = 0x00020000;
  return r;
}
constexpr int SC1 = 16, WAUX = FK_WNT ? 2 : 0;
__device__ __forceinline__ v2u ld2(rsrc_t r, u32 vo, u32 so = 0) { return __builtin_amdgcn_raw_buffer_load_b64(r, vo, so, SC1); }
__device__ __forceinline__ v4u ld4(rsrc_t r, u32 vo, u32 so = 0) { return __builtin_amdgcn_raw_buffer_load_b128(r, vo, so, SC1); }
__device__ __forceinline__ void st2(rsrc_t r, u32 vo, v2u v) { __builtin_amdgcn_raw_buffer_store_b64(v, r, vo, 0, SC1); }
__device__ __forceinline__ void st4(rsrc_t r, u32 vo, v4u v) { __builtin_amdgcn_raw_buffer_store_b128(v, r, vo, 0, SC1); }

#define STAMP(i) do {} while (0)
#define FLUSH() do {} while (0)

template <int N> __device__ __forceinline__ void pause() { if (N > 0) asm volatile("s_sleep %0" ::"i"(N) : "memory"); }
__device__ __forceinline__ float rsum16(float v) {
  v += dppm<0x128>(v); v += dppm<0x124>(v); v += dppm<0x122>(v); v += dppm<0x121>(v);
  return v;
}
struct Tag { u32 b[4]; };
__device__ __forceinline__ Tag mktag(u32 t) { return Tag{{t & 0xffu, (t >> 8) & 0xffu, (t >> 16) & 0xffu, t >> 24}}; }
__device__ __forceinline__ u32 enc(float x, u32 tb) { return ((asu(x) + 0x80u) & 0xffffff00u) | tb; }
__device__ __forceinline__ v4u enc4(f4 x, const Tag& t) { return v4u{enc(x[0], t.b[0]), enc(x[1], t.b[1]), enc(x[2], t.b[2]), enc(x[3], t.b[3])}; }
__device__ __forceinline__ float dec(u32 y) { return asf(y & 0xffffff00u); }
__device__ __forceinline__ bool bad2(v2u y, u32 t16) { return (__builtin_amdgcn_perm(y.y, y.x, 0x0c0c0400u)) != t16; }
__device__ __forceinline__ bool bad4(v4u y, u32 t32) {
  return __builtin_amdgcn_perm(__builtin_amdgcn_perm(y.w, y.z, 0x0c0c0400u), __builtin_amdgcn_perm(y.y, y.x, 0x0c0c0400u), 0x05040100u) != t32;
}
__device__ __forceinline__ bool half_any(bool x, int lane) { const u64 m = __ballot(x); return (lane < 32 ? (u32)m : (u32)(m >> 32)) != 0u; }
__device__ __forceinline__ f2 ln_stats(float piv, float s1, float s2) {
  const float md = s1 * (1.f / DM), e2 = s2 * (1.f / DM);
  const float var = rb(fmaxf(fmaf(-md, md, e2), 0.f));
  return f2{rb(piv + md), __builtin_amdgcn_rcpf(rb(__builtin_amdgcn_sqrtf(rb(var + EPS))))};
}
__device__ __forceinline__ v4u ln_apply(v4u xr, f2 st) {
  v4u o;
#pragma unroll
  for (int i = 0; i < 4; ++i) { const u32 d = pk(blo(xr[i]) - st.x, bhi(xr[i]) - st.x); o[i] = pk(blo(d) * st.y, bhi(d) * st.y); }
  return o;
}
__device__ __forceinline__ v4u layernorm8(v4u xr) {
  const float piv = asf(__builtin_amdgcn_readfirstlane(xr[0] << 16));
  float s1 = 0.f, s2 = 0.f;
#pragma unroll
  for (int i = 0; i < 4; ++i) { const float d0 = blo(xr[i]) - piv, d1 = bhi(xr[i]) - piv; s1 += d0 + d1; s2 = fmaf(d0, d0, fmaf(d1, d1, s2)); }
  s1 = wsum63(s1); s2 = wsum63(s2);
  return ln_apply(xr, ln_stats(piv, asf(__builtin_amdgcn_readlane(asu(s1), 63)), asf(__builtin_amdgcn_readlane(asu(s2), 63))));
}

#define FK_PIECE(T, VO) \
  "s_waitcnt vmcnt(%c[wm])\n\t" \
  ".if %c[pt] == " #T "\n\tds_write_b32 %[fa], %[fv]\n\t.endif\n\t" \
  ".if %c[nt]\n\tbuffer_load_dwordx4 " VO ", %[rs], %[so] offen offset:" #T "*1024 nt lds\n\t.else\n\tbuffer_load_dwordx4 " VO ", %[rs], %[so] offen offset:" #T "*1024 lds\n\t.endif\n\t"
template <int W, int NT>
__device__ __forceinline__ void dma_tile(v4i rs, u32 so, u32 la, u32 v0, u32 v1, u32 v2, u32 v3, u32 fa, int tn) {
  const int landed = tn - 1 - (W - 5) / 4;
  asm volatile("s_mov_b32 m0, %[la]\n\t" FK_PIECE(0, "%[v0]") FK_PIECE(1, "%[v1]") FK_PIECE(2, "%[v2]") FK_PIECE(3, "%[v3]")
               :
               : [rs] "s"(rs), [so] "s"(so), [la] "s"(la), [v0] "v"(v0), [v1] "v"(v1), [v2] "v"(v2), [v3] "v"(v3), [fa] "v"(fa),
                 [fv] "v"(landed > 0 ? landed : 0), [wm] "i"(W - 1), [pt] "i"((W - 5) % 4), [nt] "i"(NT)
               : "memory");
}
template <int NT>
__device__ __forceinline__ void dma_adapt(int lag, v4i rs, u32 so, u32 la, u32 v0, u32 v1, u32 v2, u32 v3, u32 fa, int tn) {
  if (lag <= 0) dma_tile<FK_W, NT>(rs, so, la, v0, v1, v2, v3, fa, tn);
  else if (lag <= FK_LG) dma_tile<FK_WB, NT>(rs, so, la, v0, v1, v2, v3, fa, tn);
  else dma_tile<FK_WC, NT>(rs, so, la, v0, v1, v2, v3, fa, tn);
}
__device__ __forceinline__ bool lds_wait_ge(volatile LAS u32* w, u32 need, bool& dead) {
  for (u32 n = 0;; ++n) {
    if ((u32)__builtin_amdgcn_readfirstlane(*w) >= need) break;
    if (dead || n > SPIN_LDS) { dead = true; break; }
    __builtin_amdgcn_s_sleep(1);
  }
  CB();
  return true;
}

__global__ void __launch_bounds__(1024, 4) fused_block(Params p) {
  __shared__ __attribute__((aligned(1024))) unsigned char lds_raw[L_END];
  LAS unsigned char* L = (LAS unsigned char*)lds_raw;
  const int tid = threadIdx.x, lane = tid & 63, wave = __builtin_amdgcn_readfirstlane(tid >> 6), w = wave & 3, role = wave >> 2;
  const int g = lane >> 4, c = lane & 15;
  const int kvh = blockIdx.x & 7, b = blockIdx.x >> 6, s = (blockIdx.x >> 3) & 7, grp = b * 8 + kvh;
  const rsrc_t rw = mkr(p.ws, WS_END);
  volatile LAS u32* FLAG = (volatile LAS u32*)(L + L_FLAG);
  const bool ffn = s < 7;
  const int j = kvh * 7 + s;
  bool dead = false;
  STAMP(0);
  const u32 dg0 = (u32)__builtin_amdgcn_s_memrealtime();
#define DIAG(n) do { if (FK_DIAG == n && w == 0) { FLAG[14] = (u32)__builtin_amdgcn_s_memrealtime(); } } while (0)

  if (role == 1) {
    __builtin_amdgcn_s_setprio(3);
    const u32 tb = (u32)__builtin_amdgcn_s_memrealtime() + FK_T0;
    const v4i rk = mkr_s(p.k + (size_t)grp * SK * 128, SK * 256), rv = mkr_s(p.v + (size_t)grp * SK * 128, SK * 256);
    const u32 k0 = g * 256 + ((c ^ g) << 4), k1 = g * 256 + ((c ^ (4 + g)) << 4), k2 = g * 256 + ((c ^ (8 + g)) << 4), k3 = g * 256 + ((c ^ (12 + g)) << 4);
    const u32 u0 = g * 256 + ((c ^ (g << 1)) << 4), u1 = g * 256 + ((c ^ ((4 + g) << 1)) << 4);
    const u32 ring = (u32)(size_t)(L + L_RING) + w * 32768, fa = (u32)(size_t)(FLAG + w);
    STAMP(5);
    if (lane < 4) FLAG[w + 4 * lane] = 0u;
    wg_barrier();
    STAMP(7);
    dma_tile<FK_W, FK_KNT>(rk, (u32)(s * 4 + w) * 4096u, ring, k0, k1, k2, k3, fa, 0);
    STAMP(6);
    dma_tile<FK_W, FK_VNT>(rv, (u32)(s * 4 + w) * 4096u, ring + 4096, u0, u1, u0, u1, fa, 1);
    STAMP(8);
    DIAG(1);
#pragma unroll 1
    for (int i = 1; i < NCH; ++i) {
      const u32 so = (u32)((i * 8 + s) * 4 + w) * 4096u, la = ring + (i & 3) * 8192;
      if (i >= 4) lds_wait_ge(FLAG + 4 + w, i - 3, dead);
      const int lag = (int)((u32)__builtin_amdgcn_s_memrealtime() - tb) - ((i * FK_TAU) >> 4);
      dma_adapt<FK_KNT>(lag, rk, so, la, k0, k1, k2, k3, fa, 2 * i);
      dma_adapt<FK_VNT>(lag, rv, so, la + 4096, u0, u1, u0, u1, fa, 2 * i + 1);
    }
    asm volatile("s_waitcnt vmcnt(8)\n\tds_write_b32 %0, %1\n\ts_waitcnt vmcnt(4)\n\tds_write_b32 %0, %2\n\ts_waitcnt vmcnt(0)\n\tds_write_b32 %0, %3" ::"v"(fa),
                 "v"(2 * NCH - 2), "v"(2 * NCH - 1), "v"(2 * NCH) : "memory");
    STAMP(1);
    DIAG(2);
    __builtin_amdgcn_s_setprio(0);
    v4u dr[8], df[8];
    const rsrc_t rd = mkr(p.wd, FF * DM * 2);
#pragma unroll
    for (int r = 0; r < 8; ++r) dr[r] = __builtin_amdgcn_raw_buffer_load_b128(rd, ffn ? (8 * g) * 1024 + 16 * c : OOB, (32 * j + r) * 1024 + 256 * w, WAUX);
    wg_barrier();
    const Tag T = mktag(FLAG[12]);
    LAS unsigned char* priv = L + L_QRED + w * 1024;
#pragma unroll
    for (int t = 0; t < 4; ++t)
#pragma unroll
      for (int q = 0; q < 4; ++q) { df[2 * q][t] = plo(dr[2 * t + 1][q], dr[2 * t][q]); df[2 * q + 1][t] = phi(dr[2 * t + 1][q], dr[2 * t][q]); }
    asm volatile("" : "+v"(df[0]), "+v"(df[1]), "+v"(df[2]), "+v"(df[3]), "+v"(df[4]), "+v"(df[5]), "+v"(df[6]), "+v"(df[7]));
    STAMP(2);
    wg_barrier();
    wg_barrier();
    wg_barrier();
    STAMP(3);
    if (ffn) {
      const LAS float* gu = (const LAS float*)(L + L_GU) + (lane & 31);
      const float gt = rb((gu[0] + gu[64]) + (gu[128] + gu[192])), up = rb((gu[32] + gu[96]) + (gu[160] + gu[224]));
      const float sg = rb(gt * __builtin_amdgcn_rcpf(1.f + __builtin_amdgcn_exp2f(-gt * L2E)));
      ((LAS u16*)priv)[lane & 31] = (u16)pk(sg * up, 0.f);
      const v4u a = *(const LAS v4u*)(priv + 16 * g);
      f4 o[8];
#pragma unroll
      for (int i = 0; i < 8; ++i) o[i] = mfma32(a, df[i], f4{0.f, 0.f, 0.f, 0.f});
      const bool hi = g & 1;
      const v4u ov = enc4(f4{hi ? o[4][0] : o[0][0], hi ? o[5][0] : o[1][0], hi ? o[6][0] : o[2][0], hi ? o[7][0] : o[3][0]}, T);
      st4(rw, lane < 32 ? WS_D + (u32)(b * 56 + j) * 2048 + (u32)(128 * w + 8 * c + 4 * g) * 4 : OOB, ov);
    }
    STAMP(4);
    FLUSH();
    return;
  }

  if (role == 2) {
    LAS unsigned char* priv = L + L_PRIV + w * 1024;
    wg_barrier();
    pause<FK_W0>(); pause<FK_W0>();
    v2u wor[4][8];
    const rsrc_t ro = mkr(p.wo, NQ * DM * 2);
#pragma unroll
    for (int u = 0; u < 4; ++u)
#pragma unroll
      for (int r = 0; r < 8; ++r) {
        wor[u][r] = __builtin_amdgcn_raw_buffer_load_b64(ro, g * 8 * 1024 + 8 * c, (kvh * 512 + 128 * w + 32 * u + r) * 1024 + 128 * s, WAUX);
        pause<FK_P1>();
      }
    v4u wf[4][4];
#pragma unroll
    for (int u = 0; u < 4; ++u) { tr84(wor[u], wf[u]); asm volatile("" : "+v"(wf[u][0]), "+v"(wf[u][1]), "+v"(wf[u][2]), "+v"(wf[u][3])); }
    lds_wait_ge(FLAG + 9, 4, dead);
    const u32 tg = FLAG[12];
    const Tag T = mktag(tg);
    STAMP(1);
    wg_barrier();
    {
      const u32 ob = WS_O + grp * 8 * OREC, om = WS_ML + (grp * 8 + (lane & 7)) * 128 + w * 16, oo = ob + (lane >> 5) * OREC + w * 512 + (lane & 31) * 16;
      pause<FK_D1>();
      STAMP(2);
      v4u o[4], ml;
      ml = ld4(rw, om);
      for (u32 n = 0; __any(bad4(ml, tg)); ++n) {
        if (dead || n > SPIN_MEM) { dead = true; break; }
        pause<FK_BO>();
        ml = ld4(rw, om);
      }
      STAMP(7);
#pragma unroll
      for (int t = 0; t < 4; ++t) o[t] = ld4(rw, oo, ((t + s) & 3) * 2 * OREC);
      float M = dec(ml.x);
      M = fmaxf(M, dppm<0xB1>(M)); M = fmaxf(M, dppm<0x4E>(M)); M = fmaxf(M, dppm<0x141>(M));
      const float e = __builtin_amdgcn_exp2f((dec(ml.x) - M) * L2E);
      float Ls = e * dec(ml.y);
      Ls += dppm<0xB1>(Ls); Ls += dppm<0x4E>(Ls); Ls += dppm<0x141>(Ls);
      const float en = e * __builtin_amdgcn_rcpf(Ls);
      float et[4];
#pragma unroll
      for (int t = 0; t < 4; ++t) {
        const float e0 = asf(__builtin_amdgcn_readlane(asu(en), 2 * ((t + s) & 3))), e1 = asf(__builtin_amdgcn_readlane(asu(en), 2 * ((t + s) & 3) + 1));
        et[t] = lane < 32 ? e0 : e1;
      }
      asm volatile("" : "+v"(et[0]), "+v"(et[1]), "+v"(et[2]), "+v"(et[3]));
      for (u32 n = 0;; ++n) {
        bool bad = false;
#pragma unroll
        for (int t = 0; t < 4; ++t) bad = bad || bad4(o[t], tg);
        if (!__any(bad)) break;
        if (dead || n > SPIN_MEM) { dead = true; break; }
        pause<FK_BO>();
#pragma unroll
        for (int t = 0; t < 4; ++t) if (half_any(bad4(o[t], tg), lane)) o[t] = ld4(rw, oo, ((t + s) & 3) * 2 * OREC);
      }
      STAMP(3);
      DIAG(6);
      f4 os = {0.f, 0.f, 0.f, 0.f};
#pragma unroll
      for (int t = 0; t < 4; ++t)
#pragma unroll
        for (int i = 0; i < 4; ++i) os[i] = fmaf(et[t], dec(o[t][i]), os[i]);
#pragma unroll
      for (int i = 0; i < 4; ++i) os[i] = x32_sum(os[i]);
      ((LAS v2u*)priv)[lane & 31] = v2u{pk(os[0], os[1]), pk(os[2], os[3])};
      f4 y[4];
#pragma unroll
      for (int i = 0; i < 4; ++i) y[i] = f4{0.f, 0.f, 0.f, 0.f};
#pragma unroll
      for (int u = 0; u < 4; ++u) {
        const v4u a = *(const LAS v4u*)(priv + (32 * u + 8 * g) * 2);
#pragma unroll
        for (int i = 0; i < 4; ++i) y[i] = mfma32(a, wf[u][i], y[i]);
      }
      if (lane < 16) *(LAS f4*)(L + L_YRED + w * 256 + c * 16) = f4{y[0][0], y[1][0], y[2][0], y[3][0]};
    }
    wg_barrier();
    {
      const LAS f4* yr = (const LAS f4*)(L + L_YRED) + c;
      const f4 y = (yr[0] + yr[16]) + (yr[32] + yr[48]);
      st4(rw, lane < 32 ? WS_Y + (u32)(2 * w + g) * 65536 + grp * 2048 + s * 256 + c * 16 : OOB, enc4(y, T));
    }
    STAMP(4);
    wg_barrier();
    wg_barrier();
    STAMP(6);
    FLUSH();
    return;
  }

  if (role == 3) {
    LAS unsigned char* priv = L + L_PRIV + 4096 + w * 1024;
    wg_barrier();
    pause<FK_W0>(); pause<FK_W0>();
    const v2u xp = *(const GAS v2u*)(p.x + b * DM + 128 * w + 4 * (lane & 31));
    v2u gr[4][8];
    v4u gf[4][4];
    if (ffn) {
      const GAS unsigned char* gp = (const GAS unsigned char*)(c < 8 ? p.wg : p.wu) + (size_t)(128 * w + 8 * g) * (FF * 2) + 64 * j + 8 * (c & 7);
#pragma unroll
      for (int u = 0; u < 4; ++u)
#pragma unroll
        for (int r = 0; r < 8; ++r) {
          const GAS v2u* a = (const GAS v2u*)(gp + (size_t)(32 * u + r) * (FF * 2));
          gr[u][r] = FK_WNT ? __builtin_nontemporal_load(a) : *a;
          pause<FK_P2>();
        }
#pragma unroll
      for (int u = 0; u < 4; ++u) { tr84(gr[u], gf[u]); asm volatile("" : "+v"(gf[u][0]), "+v"(gf[u][1]), "+v"(gf[u][2]), "+v"(gf[u][3])); }
    }
    lds_wait_ge(FLAG + 9, 4, dead);
    const u32 tg = FLAG[12];
    if (blockIdx.x == 0) {
      const u32 tlo = FLAG[10];
      for (u32 n = 0;; ++n) {
        const v2u gv = ld2(rw, WS_QG + (u32)(lane >> 3) * 16384 + w * 4096 + (lane & 7) * 512);
        if (__all(gv.y == tlo)) break;
        if (dead || n > SPIN_MEM) { dead = true; break; }
        pause<8>();
      }
    }
    STAMP(1);
    wg_barrier();
    wg_barrier();
    {
      const u32 yb = WS_Y + (u32)kvh * 65536 + (u32)b * 8 * 2048 + (lane >> 5) * 2048 + w * 512 + (lane & 31) * 16;
      pause<FK_D2>();
      STAMP(2);
      v4u y[4];
#pragma unroll
      for (int k = 0; k < 4; ++k) y[k] = ld4(rw, yb, ((k + s) & 3) * 4096);
      for (u32 n = 0;; ++n) {
        bool bad = false;
#pragma unroll
        for (int k = 0; k < 4; ++k) bad = bad || bad4(y[k], tg);
        if (!__any(bad)) break;
        if (dead || n > SPIN_MEM) { dead = true; break; }
        pause<FK_BO>();
#pragma unroll
        for (int k = 0; k < 4; ++k) if (half_any(bad4(y[k], tg), lane)) y[k] = ld4(rw, yb, ((k + s) & 3) * 4096);
      }
      STAMP(3);
      DIAG(7);
      f4 a;
#pragma unroll
      for (int i = 0; i < 4; ++i) {
        const float p0 = dec(y[0][i]), p1 = dec(y[1][i]), p2 = dec(y[2][i]), p3 = dec(y[3][i]);
        a[i] = x32_sum((s & 1) ? (p3 + p0) + (p1 + p2) : (p0 + p1) + (p2 + p3));
      }
      const u32 a0 = pk(a[0], a[1]), a1 = pk(a[2], a[3]);
      ((LAS v2u*)(L + L_HS))[32 * w + (lane & 31)] = v2u{pk(blo(xp.x) + blo(a0), bhi(xp.x) + bhi(a0)), pk(blo(xp.y) + blo(a1), bhi(xp.y) + bhi(a1))};
    }
    wg_barrier();
    STAMP(4);
    if (ffn) {
      const v4u hn = layernorm8(*(const LAS v4u*)(L + L_HS + lane * 16));
      *(LAS v4u*)(priv + lane * 16) = hn;
      f4 acc[4];
#pragma unroll
      for (int i = 0; i < 4; ++i) acc[i] = f4{0.f, 0.f, 0.f, 0.f};
#pragma unroll
      for (int u = 0; u < 4; ++u) {
        const v4u a = *(const LAS v4u*)(priv + (128 * w + 32 * u + 8 * g) * 2);
#pragma unroll
        for (int i = 0; i < 4; ++i) acc[i] = mfma32(a, gf[u][i], acc[i]);
      }
      if (lane < 16) *(LAS f4*)(L + L_GU + w * 256 + c * 16) = f4{acc[0][0], acc[1][0], acc[2][0], acc[3][0]};
    }
    STAMP(5);
    DIAG(8);
    wg_barrier();
    if (blockIdx.x == 0 && w == 0) st2(rw, lane == 0 ? WS_E : OOB, v2u{FLAG[10], FLAG[11]});
    FLUSH();
    return;
  }

  v4u qf[4];
  u32 tg;
  {
    STAMP(6);
    v4u x4[4];
#pragma unroll
    for (int t = 0; t < 4; ++t) x4[t] = *(const GAS v4u*)(p.x + g * DM + 128 * t + 8 * c);
    const v4u xa = *(const GAS v4u*)(p.x + (c & 3) * DM + 128 * b + 32 * w + 8 * g);
    const rsrc_t rq = mkr(p.wq, DM * NQ * 2);
    v2u wr[8];
#pragma unroll
    for (int r = 0; r < 8; ++r)
      wr[r] = __builtin_amdgcn_raw_buffer_load_b64(rq, g * 8 * 8192 + 8 * c, (128 * b + 32 * w + r) * 8192 + (kvh * 512 + 64 * s) * 2, 2);
    const v2u e = ld2(rw, WS_E);
    wg_barrier();
    STAMP(10);
    const float piv = dppm<0x150>(blo(x4[0][0]));
    float s1 = 0.f, s2 = 0.f;
#pragma unroll
    for (int t = 0; t < 4; ++t)
#pragma unroll
      for (int i = 0; i < 4; ++i) { const float d0 = blo(x4[t][i]) - piv, d1 = bhi(x4[t][i]) - piv; s1 += d0 + d1; s2 = fmaf(d0, d0, fmaf(d1, d1, s2)); }
    const f2 st = ln_stats(piv, rsum16(s1), rsum16(s2));
    const f2 sa = {asf(__builtin_amdgcn_ds_bpermute((c & 3) * 64, asu(st.x))), asf(__builtin_amdgcn_ds_bpermute((c & 3) * 64, asu(st.y)))};
    const v4u a = ln_apply(xa, sa);
    v4u f[4];
    tr84(wr, f);
    f4 acc[4];
#pragma unroll
    for (int i = 0; i < 4; ++i) acc[i] = mfma32(a, f[i], f4{0.f, 0.f, 0.f, 0.f});
    if (lane < 16) {
#pragma unroll
      for (int r = 0; r < 4; ++r) *(LAS f4*)(L + L_QRED + w * 1024 + r * 256 + c * 16) = f4{acc[0][r], acc[1][r], acc[2][r], acc[3][r]};
    }
    const u64 epoch = ((u64)__builtin_amdgcn_readfirstlane(e.y) << 32 | __builtin_amdgcn_readfirstlane(e.x)) + 1;
    const u32 tlo = (u32)epoch, thi = (u32)(epoch >> 32) + tlo * 0x9E3779B1u;
    const u32 dl = tlo - (u32)(epoch >> 32);
    tg = ((tlo + 1u) & 0xffu) | (((tlo >> 8) + 3u * dl + 1u) & 0xffu) << 8 | (((tlo >> 16) + 5u * dl + 1u) & 0xffu) << 16 | ((tlo >> 24) + 7u * dl + 1u) << 24;
    if (w == 0 && lane == 0) { FLAG[10] = tlo; FLAG[11] = (u32)(epoch >> 32); FLAG[12] = tg; }
    asm volatile("s_waitcnt lgkmcnt(0)" ::: "memory");
    STAMP(11);
    u32 old = 0;
    if (lane == 0) old = __hip_atomic_fetch_add((LAS u32*)(L + L_FLAG) + 8, 1u, __ATOMIC_RELAXED, __HIP_MEMORY_SCOPE_WORKGROUP);
    old = __builtin_amdgcn_readfirstlane(old);
    CB();
    if (old == 3) {
      const LAS f2* qr = (const LAS f2*)(L + L_QRED) + g * 32 + c;
      const f2 q0 = (qr[0] + qr[128]) + (qr[256] + qr[384]), q1 = (qr[16] + qr[144]) + (qr[272] + qr[400]);
      const u32 off = WS_QG + (u32)(g * 8 + kvh) * 16384 + b * 4096 + s * 512 + c * 16;
      st4(rw, off, v4u{asu(q0.x), tlo, asu(q0.y), thi});
      st4(rw, off + 256, v4u{asu(q1.x), tlo, asu(q1.y), thi});
    }
    STAMP(1);
    pause<FK_D0>();
    v4u gq[4];
    const u32 qo = WS_QG + grp * 16384 + w * 1024 + lane * 16;
#pragma unroll
    for (int t = 0; t < 4; ++t) gq[t] = ld4(rw, qo, t * 4096);
    for (u32 n = 0;; ++n) {
      bool bad = false;
#pragma unroll
      for (int t = 0; t < 4; ++t) bad = bad || gq[t].y != tlo || gq[t].w != thi;
      if (!__any(bad)) break;
      if (dead || n > SPIN_MEM) { dead = true; break; }
      pause<2>();
#pragma unroll
      for (int t = 0; t < 4; ++t) if (__any(gq[t].y != tlo || gq[t].w != thi)) gq[t] = ld4(rw, qo, t * 4096);
    }
    STAMP(12);
    ((LAS u32*)(L + L_QS))[64 * w + lane] = pk((asf(gq[0].x) + asf(gq[1].x)) + (asf(gq[2].x) + asf(gq[3].x)), (asf(gq[0].z) + asf(gq[1].z)) + (asf(gq[2].z) + asf(gq[3].z)));
    asm volatile("s_waitcnt lgkmcnt(0)" ::: "memory");
    if (lane == 0) __hip_atomic_fetch_add((LAS u32*)(L + L_FLAG) + 9, 1u, __ATOMIC_RELAXED, __HIP_MEMORY_SCOPE_WORKGROUP);
    lds_wait_ge(FLAG + 9, 4, dead);
#pragma unroll
    for (int u = 0; u < 4; ++u) qf[u] = *(const LAS v4u*)(L + L_QS + ((c & 3) * 128 + 32 * u + 8 * g) * 2);
    STAMP(2);
    DIAG(4);
  }
  float m = -INFINITY, l = 0.f;
  f4 acc[8];
#pragma unroll
  for (int i = 0; i < 8; ++i) acc[i] = f4{0.f, 0.f, 0.f, 0.f};
  const LAS unsigned char* ringk = L + L_RING + w * 32768 + c * 256;
  const u32 vrow = 4 * g + (c >> 2);
  const u32 va = (u32)(size_t)(L + L_RING) + w * 32768 + 4096 + vrow * 256 + (vrow & 7) * 32 + 8 * (c & 3);
  u32 seen = 0;
  auto need = [&](u32 n) {
    for (u32 k = 0; seen < n; ++k) {
      seen = __builtin_amdgcn_readfirstlane(FLAG[w]);
      if (seen >= n) break;
      if (dead || k > SPIN_LDS) { dead = true; break; }
      __builtin_amdgcn_s_sleep(1);
    }
    CB();
  };
#pragma unroll 1
  for (int i = 0; i < NCH; ++i) {
    const u32 sl = (i & 3) * 8192;
    need(2 * i + 1);
    v4u kf[4];
#pragma unroll
    for (int u = 0; u < 4; ++u) kf[u] = *(const LAS v4u*)(ringk + sl + (((4 * u + g) ^ c) << 4));
    f4 s0 = mfma32(kf[0], qf[0], f4{0.f, 0.f, 0.f, 0.f}), s1 = mfma32(kf[2], qf[2], f4{0.f, 0.f, 0.f, 0.f});
    s0 = mfma32(kf[1], qf[1], s0); s1 = mfma32(kf[3], qf[3], s1);
    const u32 a01 = pk(s0[0] + s1[0], s0[1] + s1[1]), a23 = pk(s0[2] + s1[2], s0[3] + s1[3]);
    const u32 b01 = pk(blo(a01) * SCALE, bhi(a01) * SCALE), b23 = pk(blo(a23) * SCALE, bhi(a23) * SCALE);
    const float t0 = blo(b01), t1 = bhi(b01), t2 = blo(b23), t3 = bhi(b23);
    const float mx = xg_max(fmaxf(fmaxf(t0, t1), fmaxf(t2, t3)));
    if (__any(mx > m)) {
      const float mn = fmaxf(m, mx), al = __builtin_amdgcn_exp2f((m - mn) * L2E);
      l *= al; m = mn;
#pragma unroll
      for (int d = 0; d < 8; ++d) acc[d] *= al;
    }
    const float p0 = __builtin_amdgcn_exp2f((t0 - m) * L2E), p1 = __builtin_amdgcn_exp2f((t1 - m) * L2E),
                p2 = __builtin_amdgcn_exp2f((t2 - m) * L2E), p3 = __builtin_amdgcn_exp2f((t3 - m) * L2E);
    l += (p0 + p1) + (p2 + p3);
    const v2u pf = {pk(p0, p1), pk(p2, p3)};
    if (i == NCH - 1) {
      const float lt = xg_sum(l);
      if (lane < 4) *(LAS f2*)(L + L_MLS + w * 32 + c * 8) = f2{m, lt};
    }
    need(2 * i + 2);
    s4 vf[8];
#pragma unroll
    for (int d = 0; d < 8; ++d) vf[d] = __builtin_amdgcn_ds_read_tr16_b64_v4i16((LAS s4*)(size_t)((va ^ (d << 5)) + sl));
    asm volatile("s_waitcnt lgkmcnt(0)" ::: "memory");
    FLAG[4 + w] = i + 1;
#pragma unroll
    for (int d = 0; d < 8; ++d) acc[d] = mfma16(vf[d], pf, acc[d]);
  }
  STAMP(4);
  if (c < 4) {
#pragma unroll
    for (int d = 0; d < 8; ++d) *(LAS f4*)(L + L_OM + w * 2048 + c * 512 + (16 * d + 4 * g) * 4) = acc[d];
  }
  wg_barrier();
  {
    const int hd = lane < 32 ? w : (lane & 3);
    f2 ml[4]; f4 ov[4];
#pragma unroll
    for (int k = 0; k < 4; ++k) { ml[k] = *(const LAS f2*)(L + L_MLS + k * 32 + hd * 8); ov[k] = *(const LAS f4*)(L + L_OM + k * 2048 + w * 512 + (lane & 31) * 16); }
    const float M = fmaxf(fmaxf(ml[0].x, ml[1].x), fmaxf(ml[2].x, ml[3].x));
    float Ls = 0.f; f4 O = {0.f, 0.f, 0.f, 0.f};
#pragma unroll
    for (int k = 0; k < 4; ++k) { const float e = __builtin_amdgcn_exp2f((ml[k].x - M) * L2E); Ls = fmaf(e, ml[k].y, Ls); O += ov[k] * e; }
    const u32 wgi = grp * 8 + s;
    st4(rw, lane < 32 ? WS_O + wgi * OREC + w * 512 + lane * 16 : (lane < 40 && w == 0 ? WS_ML + wgi * 128 + (lane - 32) * 16 : OOB), enc4(lane < 32 ? O : f4{M, Ls, M, Ls}, mktag(tg)));
  }
  STAMP(5);
  DIAG(5);
  wg_barrier();
  wg_barrier();
  wg_barrier();
    if (w < 2) {
      const int col = (kvh * 8 + s) * 8 + 4 * w;
      const u32 off = lane < 56 ? WS_D + (u32)(b * 56 + lane) * 2048 + col * 4 : OOB;
      const v2u hv = *(const LAS v2u*)(L + L_HS + col * 2);
      const u32 tgl = lane < 56 ? tg : 0u;
      pause<FK_D3>();
      STAMP(7);
      v4u d0 = ld4(rw, off);
      for (u32 n = 0; __any(bad4(d0, tgl)); ++n) {
        if (dead || n > SPIN_MEM) { dead = true; break; }
        pause<FK_BO>();
        d0 = ld4(rw, off);
      }
      STAMP(8);
      const u32 a0 = pk(wsum63(dec(d0[0])), wsum63(dec(d0[1]))), a1 = pk(wsum63(dec(d0[2])), wsum63(dec(d0[3])));
      if (lane == 63) *(GAS v2u*)(p.out + b * DM + col) = v2u{pk(blo(hv.x) + blo(a0), bhi(hv.x) + bhi(a0)), pk(blo(hv.y) + blo(a1), bhi(hv.y) + bhi(a1))};
      STAMP(9);
      if (FK_DIAGW) asm volatile("s_waitcnt vmcnt(0)" ::: "memory");
      if (FK_DIAG == 9 && blockIdx.x == FK_DIAGB && w == 0) {
        GAS u32* pe = (GAS u32*)(p.ws + WS_DBG);
        const u32 prev = __hip_atomic_load(pe, __ATOMIC_RELAXED, __HIP_MEMORY_SCOPE_AGENT);
        const u32 now = (u32)__builtin_amdgcn_s_memrealtime(); u32 x = dg0 - __builtin_amdgcn_readfirstlane(prev); x = x < 5000 ? x * FK_DIAGK : 0;
        while ((u32)__builtin_amdgcn_s_memrealtime() - now < x) __builtin_amdgcn_s_sleep(1);
        __hip_atomic_store(pe, (u32)__builtin_amdgcn_s_memrealtime(), __ATOMIC_RELAXED, __HIP_MEMORY_SCOPE_AGENT);
      } else
      if (blockIdx.x == FK_DIAGB && w == 0) { const u32 now = (u32)__builtin_amdgcn_s_memrealtime(), x = ((FK_DIAG == 3 ? now : FLAG[14]) - dg0) * FK_DIAGK;
        while ((u32)__builtin_amdgcn_s_memrealtime() - now < x && x < 100000) __builtin_amdgcn_s_sleep(1); }
    }
  FLUSH();
}
}

extern "C" void kernel_launch(void* const* d_in, const int* in_sizes, int n_in, void* d_out, int out_size, void* d_ws, size_t ws_size,
                              hipStream_t stream) {
  fk::Params p;
  p.x = (const fk::u16*)d_in[0]; p.k = (const fk::u16*)d_in[1]; p.v = (const fk::u16*)d_in[2];
  p.wq = (const fk::u16*)d_in[3]; p.wo = (const fk::u16*)d_in[4];
  p.wg = (const fk::u16*)d_in[6]; p.wu = (const fk::u16*)d_in[7]; p.wd = (const fk::u16*)d_in[8];
  p.out = (fk::u16*)d_out; p.ws = (unsigned char*)d_ws;
  hipLaunchKernelGGL(fk::fused_block, dim3(256), dim3(1024), 0, stream, p);
}
```

```cpp
#include <hip/hip_runtime.h>
#include <stdint.h>

#define FK_W 5
#define FK_WNT 1
#define FK_W0 120
#define FK_P1 18
#define FK_P2 22
#define FK_ORD 1
#define FK_G0 0
#define FK_WB 7
#define FK_WC 10
#define FK_LG 60
#define FK_T0 80
#define FK_TAU 1830
#define FK_DIAG 9
#define FK_DIAGB 0
#define FK_DIAGK 0
#define FK_DB2 0
#define FK_KNT 1
#define FK_VNT 1
#define FK_WB2 0
#define FK_WB2D 0
#define FK_PT 0
#define FK_DIAGW 0
#define FK_TINY 2
#define FK_TINYK 0
#define FK_BO 1
#define FK_D0 40
#define FK_D1 18
#define FK_D2 16
#define FK_D3 32
#define FK_STAMPS 0

namespace fk {
#define GAS __attribute__((address_space(1)))
#define LAS __attribute__((address_space(3)))
typedef unsigned u32;
typedef unsigned long long u64;
typedef unsigned short u16;
typedef u32 v2u __attribute__((ext_vector_type(2)));
typedef u32 v4u __attribute__((ext_vector_type(4)));
typedef int v4i __attribute__((ext_vector_type(4)));
typedef float f2 __attribute__((ext_vector_type(2)));
typedef float f4 __attribute__((ext_vector_type(4)));
typedef short s4 __attribute__((ext_vector_type(4)));
typedef __bf16 b2 __attribute__((ext_vector_type(2)));
typedef __bf16 b8 __attribute__((ext_vector_type(8)));

constexpr int DM = 512, SK = 16384, FF = 1792, NQ = 4096;
constexpr int NCH = 32;
constexpr float L2E = 1.4426950408889634f, SCALE = 0.08838834764831845f, EPS = 1e-5f;
constexpr u32 WS_E = 0, WS_QG = 4096, WS_O = WS_QG + 32 * 16384, OREC = 2048, WS_ML = WS_O + 256 * OREC  , WS_Y = WS_ML + 256 * 128,
              WS_D = WS_Y + 8 * 65536, WS_END = WS_D + 4 * 56 * 2048, WS_DBG = WS_END;
constexpr u32 OOB = 0x80000000u;
constexpr u32 L_RING = 0, L_FLAG = 131072  , L_QRED = L_FLAG + 256  , L_QS = L_QRED + 4096,
              L_PRIV = L_QS + 1024  , L_OM = L_PRIV + 8192, L_MLS = L_OM + 8192, L_YRED = L_MLS + 128, L_HS = L_YRED + 1024,
              L_GU = L_HS + 1024, L_END = L_GU + 1024;
constexpr u32 SPIN_LDS = 1u << 19, SPIN_MEM = 1u << 16;

struct Params {
  const u16 *x, *k, *v, *wq, *wo, *wg, *wu, *wd;
  u16* out;
  unsigned char* ws;
};

#define CB() asm volatile("" ::: "memory")
__device__ __forceinline__ void wg_barrier() {
  asm volatile("s_waitcnt lgkmcnt(0)\n\ts_barrier" ::: "memory");
  __builtin_amdgcn_sched_barrier(0);
}
__device__ __forceinline__ float asf(u32 x) { return __builtin_bit_cast(float, x); }
__device__ __forceinline__ u32 asu(float x) { return __builtin_bit_cast(u32, x); }
__device__ __forceinline__ u32 pk(float lo, float hi) { f2 z = {lo, hi}; return __builtin_bit_cast(u32, __builtin_convertvector(z, b2)); }
__device__ __forceinline__ float blo(u32 p) { return asf(p << 16); }
__device__ __forceinline__ float bhi(u32 p) { return asf(p & 0xffff0000u); }
__device__ __forceinline__ float rb(float x) { return blo(pk(x, x)); }
template <int CTRL, int RM = 0xf> __device__ __forceinline__ float dpp0(float x) {
  return asf(__builtin_amdgcn_update_dpp(0, asu(x), CTRL, RM, 0xf, true));
}
template <int CTRL> __device__ __forceinline__ float dppm(float x) { return asf(__builtin_amdgcn_mov_dpp(asu(x), CTRL, 0xf, 0xf, true)); }
__device__ __forceinline__ float wsum63(float v) {
  v += dpp0<0x111>(v); v += dpp0<0x112>(v); v += dpp0<0x114>(v); v += dpp0<0x118>(v);
  v += asf(__builtin_amdgcn_update_dpp(0, asu(v), 0x142, 0xa, 0xf, false));
  v += asf(__builtin_amdgcn_update_dpp(0, asu(v), 0x143, 0xc, 0xf, false));
  return v;
}
__device__ __forceinline__ float wsum(float v) { return asf(__builtin_amdgcn_readlane(asu(wsum63(v)), 63)); }
__device__ __forceinline__ float xg_max(float x) {
  auto s = __builtin_amdgcn_permlane16_swap(asu(x), asu(x), false, false);
  x = fmaxf(asf(s[0]), asf(s[1]));
  auto t = __builtin_amdgcn_permlane32_swap(asu(x), asu(x), false, false);
  return fmaxf(asf(t[0]), asf(t[1]));
}
__device__ __forceinline__ float xg_sum(float x) {
  auto s = __builtin_amdgcn_permlane16_swap(asu(x), asu(x), false, false);
  x = asf(s[0]) + asf(s[1]);
  auto t = __builtin_amdgcn_permlane32_swap(asu(x), asu(x), false, false);
  return asf(t[0]) + asf(t[1]);
}
__device__ __forceinline__ float x32_sum(float x) {
  auto t = __builtin_amdgcn_permlane32_swap(asu(x), asu(x), false, false);
  return asf(t[0]) + asf(t[1]);
}
__device__ __forceinline__ f4 mfma32(v4u a, v4u b, f4 c) {
  return __builtin_amdgcn_mfma_f32_16x16x32_bf16(__builtin_bit_cast(b8, a), __builtin_bit_cast(b8, b), c, 0, 0, 0);
}
__device__ __forceinline__ f4 mfma16(s4 a, v2u b, f4 c) {
  return __builtin_amdgcn_mfma_f32_16x16x16bf16_1k(a, __builtin_bit_cast(s4, b), c, 0, 0, 0);
}
__device__ __forceinline__ u32 plo(u32 r1, u32 r0) { return __builtin_amdgcn_perm(r1, r0, 0x05040100u); }
__device__ __forceinline__ u32 phi(u32 r1, u32 r0) { return __builtin_amdgcn_perm(r1, r0, 0x07060302u); }
__device__ __forceinline__ void tr84(const v2u (&r)[8], v4u (&f)[4]) {
#pragma unroll
  for (int t = 0; t < 4; ++t) {
    f[0][t] = plo(r[2 * t + 1].x, r[2 * t].x); f[1][t] = phi(r[2 * t + 1].x, r[2 * t].x);
    f[2][t] = plo(r[2 * t + 1].y, r[2 * t].y); f[3][t] = phi(r[2 * t + 1].y, r[2 * t].y);
  }
}
typedef __amdgpu_buffer_rsrc_t rsrc_t;
__device__ __forceinline__ rsrc_t mkr(const void* p, u32 bytes) { return __builtin_amdgcn_make_buffer_rsrc((void*)p, 0, bytes, 0x00020000); }
__device__ __forceinline__ v4i mkr_s(const void* p, u32 bytes) {
  const u64 a = (u64)p; v4i r;
  r.x = __builtin_amdgcn_readfirstlane((u32)a); r.y = __builtin_amdgcn_readfirstlane((u32)(a >> 32)); r.z = (int)bytes; r.w = 0x00020000;
  return r;
}
constexpr int SC1 = 16, WAUX = FK_WNT ? 2 : 0;
__device__ __forceinline__ v2u ld2(rsrc_t r, u32 vo, u32 so = 0) { return __builtin_amdgcn_raw_buffer_load_b64(r, vo, so, SC1); }
__device__ __forceinline__ v4u ld4(rsrc_t r, u32 vo, u32 so = 0) { return __builtin_amdgcn_raw_buffer_load_b128(r, vo, so, SC1); }
__device__ __forceinline__ void st2(rsrc_t r, u32 vo, v2u v) { __builtin_amdgcn_raw_buffer_store_b64(v, r, vo, 0, SC1); }
__device__ __forceinline__ void st4(rsrc_t r, u32 vo, v4u v) { __builtin_amdgcn_raw_buffer_store_b128(v, r, vo, 0, SC1); }

#define STAMP(i) do {} while (0)
#define FLUSH() do {} while (0)

template <int N> __device__ __forceinline__ void pause() { if (N > 0) asm volatile("s_sleep %0" ::"i"(N) : "memory"); }
__device__ __forceinline__ float rsum16(float v) {
  v += dppm<0x128>(v); v += dppm<0x124>(v); v += dppm<0x122>(v); v += dppm<0x121>(v);
  return v;
}
struct Tag { u32 b[4]; };
__device__ __forceinline__ Tag mktag(u32 t) { return Tag{{t & 0xffu, (t >> 8) & 0xffu, (t >> 16) & 0xffu, t >> 24}}; }
__device__ __forceinline__ u32 enc(float x, u32 tb) { return ((asu(x) + 0x80u) & 0xffffff00u) | tb; }
__device__ __forceinline__ v4u enc4(f4 x, const Tag& t) { return v4u{enc(x[0], t.b[0]), enc(x[1], t.b[1]), enc(x[2], t.b[2]), enc(x[3], t.b[3])}; }
__device__ __forceinline__ float dec(u32 y) { return asf(y & 0xffffff00u); }
__device__ __forceinline__ bool bad2(v2u y, u32 t16) { return (__builtin_amdgcn_perm(y.y, y.x, 0x0c0c0400u)) != t16; }
__device__ __forceinline__ bool bad4(v4u y, u32 t32) {
  return __builtin_amdgcn_perm(__builtin_amdgcn_perm(y.w, y.z, 0x0c0c0400u), __builtin_amdgcn_perm(y.y, y.x, 0x0c0c0400u), 0x05040100u) != t32;
}
__device__ __forceinline__ bool half_any(bool x, int lane) { const u64 m = __ballot(x); return (lane < 32 ? (u32)m : (u32)(m >> 32)) != 0u; }
__device__ __forceinline__ f2 ln_stats(float piv, float s1, float s2) {
  const float md = s1 * (1.f / DM), e2 = s2 * (1.f / DM);
  const float var = rb(fmaxf(fmaf(-md, md, e2), 0.f));
  return f2{rb(piv + md), __builtin_amdgcn_rcpf(rb(__builtin_amdgcn_sqrtf(rb(var + EPS))))};
}
__device__ __forceinline__ v4u ln_apply(v4u xr, f2 st) {
  v4u o;
#pragma unroll
  for (int i = 0; i < 4; ++i) { const u32 d = pk(blo(xr[i]) - st.x, bhi(xr[i]) - st.x); o[i] = pk(blo(d) * st.y, bhi(d) * st.y); }
  return o;
}
__device__ __forceinline__ v4u layernorm8(v4u xr) {
  const float piv = asf(__builtin_amdgcn_readfirstlane(xr[0] << 16));
  float s1 = 0.f, s2 = 0.f;
#pragma unroll
  for (int i = 0; i < 4; ++i) { const float d0 = blo(xr[i]) - piv, d1 = bhi(xr[i]) - piv; s1 += d0 + d1; s2 = fmaf(d0, d0, fmaf(d1, d1, s2)); }
  s1 = wsum63(s1); s2 = wsum63(s2);
  return ln_apply(xr, ln_stats(piv, asf(__builtin_amdgcn_readlane(asu(s1), 63)), asf(__builtin_amdgcn_readlane(asu(s2), 63))));
}

#define FK_PIECE(T, VO) \
  "s_waitcnt vmcnt(%c[wm])\n\t" \
  ".if %c[pt] == " #T "\n\tds_write_b32 %[fa], %[fv]\n\t.endif\n\t" \
  ".if %c[nt]\n\tbuffer_load_dwordx4 " VO ", %[rs], %[so] offen offset:" #T "*1024 nt lds\n\t.else\n\tbuffer_load_dwordx4 " VO ", %[rs], %[so] offen offset:" #T "*1024 lds\n\t.endif\n\t"
template <int W, int NT>
__device__ __forceinline__ void dma_tile(v4i rs, u32 so, u32 la, u32 v0, u32 v1, u32 v2, u32 v3, u32 fa, int tn) {
  const int landed = tn - 1 - (W - 5) / 4;
  asm volatile("s_mov_b32 m0, %[la]\n\t" FK_PIECE(0, "%[v0]") FK_PIECE(1, "%[v1]") FK_PIECE(2, "%[v2]") FK_PIECE(3, "%[v3]")
               :
               : [rs] "s"(rs), [so] "s"(so), [la] "s"(la), [v0] "v"(v0), [v1] "v"(v1), [v2] "v"(v2), [v3] "v"(v3), [fa] "v"(fa),
                 [fv] "v"(landed > 0 ? landed : 0), [wm] "i"(W - 1), [pt] "i"((W - 5) % 4), [nt] "i"(NT)
               : "memory");
}
template <int NT>
__device__ __forceinline__ void dma_adapt(int lag, v4i rs, u32 so, u32 la, u32 v0, u32 v1, u32 v2, u32 v3, u32 fa, int tn) {
  if (lag <= 0) dma_tile<FK_W, NT>(rs, so, la, v0, v1, v2, v3, fa, tn);
  else if (lag <= FK_LG) dma_tile<FK_WB, NT>(rs, so, la, v0, v1, v2, v3, fa, tn);
  else dma_tile<FK_WC, NT>(rs, so, la, v0, v1, v2, v3, fa, tn);
}
__device__ __forceinline__ bool lds_wait_ge(volatile LAS u32* w, u32 need, bool& dead) {
  for (u32 n = 0;; ++n) {
    if ((u32)__builtin_amdgcn_readfirstlane(*w) >= need) break;
    if (dead || n > SPIN_LDS) { dead = true; break; }
    __builtin_amdgcn_s_sleep(1);
  }
  CB();
  return true;
}

__global__ void __launch_bounds__(1024, 4) fused_block(Params p) {
  __shared__ __attribute__((aligned(1024))) unsigned char lds_raw[L_END];
  LAS unsigned char* L = (LAS unsigned char*)lds_raw;
  const int tid = threadIdx.x, lane = tid & 63, wave = __builtin_amdgcn_readfirstlane(tid >> 6), w = wave & 3, role = wave >> 2;
  const int g = lane >> 4, c = lane & 15;
  const int kvh = blockIdx.x & 7, b = blockIdx.x >> 6, s = (blockIdx.x >> 3) & 7, grp = b * 8 + kvh;
  const rsrc_t rw = mkr(p.ws, WS_END);
  volatile LAS u32* FLAG = (volatile LAS u32*)(L + L_FLAG);
  const bool ffn = s < 7;
  const int j = kvh * 7 + s;
  bool dead = false;
  STAMP(0);
  const u32 dg0 = (u32)__builtin_amdgcn_s_memrealtime();
#define DIAG(n) do { if (FK_DIAG == n && w == 0) { FLAG[14] = (u32)__builtin_amdgcn_s_memrealtime(); } } while (0)

  if (role == 1) {
    __builtin_amdgcn_s_setprio(3);
    const u32 tb = (u32)__builtin_amdgcn_s_memrealtime() + FK_T0;
    const v4i rk = mkr_s(p.k + (size_t)grp * SK * 128, SK * 256), rv = mkr_s(p.v + (size_t)grp * SK * 128, SK * 256);
    const u32 k0 = g * 256 + ((c ^ g) << 4), k1 = g * 256 + ((c ^ (4 + g)) << 4), k2 = g * 256 + ((c ^ (8 + g)) << 4), k3 = g * 256 + ((c ^ (12 + g)) << 4);
    const u32 u0 = g * 256 + ((c ^ (g << 1)) << 4), u1 = g * 256 + ((c ^ ((4 + g) << 1)) << 4);
    const u32 ring = (u32)(size_t)(L + L_RING) + w * 32768, fa = (u32)(size_t)(FLAG + w);
    STAMP(5);
    if (lane < 4) FLAG[w + 4 * lane] = 0u;
    wg_barrier();
    STAMP(7);
    dma_tile<FK_W, FK_KNT>(rk, (u32)(s * 4 + w) * 4096u, ring, k0, k1, k2, k3, fa, 0);
    STAMP(6);
    dma_tile<FK_W, FK_VNT>(rv, (u32)(s * 4 + w) * 4096u, ring + 4096, u0, u1, u0, u1, fa, 1);
    STAMP(8);
    DIAG(1);
#pragma unroll 1
    for (int i = 1; i < NCH; ++i) {
      const u32 so = (u32)((i * 8 + s) * 4 + w) * 4096u, la = ring + (i & 3) * 8192;
      if (i >= 4) lds_wait_ge(FLAG + 4 + w, i - 3, dead);
      const int lag = (int)((u32)__builtin_amdgcn_s_memrealtime() - tb) - ((i * FK_TAU) >> 4);
      dma_adapt<FK_KNT>(lag, rk, so, la, k0, k1, k2, k3, fa, 2 * i);
      dma_adapt<FK_VNT>(lag, rv, so, la + 4096, u0, u1, u0, u1, fa, 2 * i + 1);
    }
    asm volatile("s_waitcnt vmcnt(8)\n\tds_write_b32 %0, %1\n\ts_waitcnt vmcnt(4)\n\tds_write_b32 %0, %2\n\ts_waitcnt vmcnt(0)\n\tds_write_b32 %0, %3" ::"v"(fa),
                 "v"(2 * NCH - 2), "v"(2 * NCH - 1), "v"(2 * NCH) : "memory");
    STAMP(1);
    DIAG(2);
    __builtin_amdgcn_s_setprio(0);
    v4u dr[8], df[8];
    const rsrc_t rd = mkr(p.wd, FF * DM * 2);
#pragma unroll
    for (int r = 0; r < 8; ++r) dr[r] = __builtin_amdgcn_raw_buffer_load_b128(rd, ffn ? (8 * g) * 1024 + 16 * c : OOB, (32 * j + r) * 1024 + 256 * w, WAUX);
    wg_barrier();
    const Tag T = mktag(FLAG[12]);
    LAS unsigned char* priv = L + L_QRED + w * 1024;
#pragma unroll
    for (int t = 0; t < 4; ++t)
#pragma unroll
      for (int q = 0; q < 4; ++q) { df[2 * q][t] = plo(dr[2 * t + 1][q], dr[2 * t][q]); df[2 * q + 1][t] = phi(dr[2 * t + 1][q], dr[2 * t][q]); }
    asm volatile("" : "+v"(df[0]), "+v"(df[1]), "+v"(df[2]), "+v"(df[3]), "+v"(df[4]), "+v"(df[5]), "+v"(df[6]), "+v"(df[7]));
    STAMP(2);
    wg_barrier();
    wg_barrier();
    wg_barrier();
    STAMP(3);
    if (ffn) {
      const LAS float* gu = (const LAS float*)(L + L_GU) + (lane & 31);
      const float gt = rb((gu[0] + gu[64]) + (gu[128] + gu[192])), up = rb((gu[32] + gu[96]) + (gu[160] + gu[224]));
      const float sg = rb(gt * __builtin_amdgcn_rcpf(1.f + __builtin_amdgcn_exp2f(-gt * L2E)));
      ((LAS u16*)priv)[lane & 31] = (u16)pk(sg * up, 0.f);
      const v4u a = *(const LAS v4u*)(priv + 16 * g);
      f4 o[8];
#pragma unroll
      for (int i = 0; i < 8; ++i) o[i] = mfma32(a, df[i], f4{0.f, 0.f, 0.f, 0.f});
      const bool hi = g & 1;
      const v4u ov = enc4(f4{hi ? o[4][0] : o[0][0], hi ? o[5][0] : o[1][0], hi ? o[6][0] : o[2][0], hi ? o[7][0] : o[3][0]}, T);
      st4(rw, lane < 32 ? WS_D + (u32)(b * 56 + j) * 2048 + (u32)(128 * w + 8 * c + 4 * g) * 4 : OOB, ov);
    }
    STAMP(4);
    FLUSH();
    return;
  }

  if (role == 2) {
    LAS unsigned char* priv = L + L_PRIV + w * 1024;
    wg_barrier();
    pause<FK_W0>(); pause<FK_W0>();
    v2u wor[4][8];
    const rsrc_t ro = mkr(p.wo, NQ * DM * 2);
#pragma unroll
    for (int u = 0; u < 4; ++u)
#pragma unroll
      for (int r = 0; r < 8; ++r) {
        wor[u][r] = __builtin_amdgcn_raw_buffer_load_b64(ro, g * 8 * 1024 + 8 * c, (kvh * 512 + 128 * w + 32 * u + r) * 1024 + 128 * s, WAUX);
        pause<FK_P1>();
      }
    v4u wf[4][4];
#pragma unroll
    for (int u = 0; u < 4; ++u) { tr84(wor[u], wf[u]); asm volatile("" : "+v"(wf[u][0]), "+v"(wf[u][1]), "+v"(wf[u][2]), "+v"(wf[u][3])); }
    lds_wait_ge(FLAG + 9, 4, dead);
    const u32 tg = FLAG[12];
    const Tag T = mktag(tg);
    STAMP(1);
    wg_barrier();
    {
      const u32 ob = WS_O + grp * 8 * OREC, om = WS_ML + (grp * 8 + (lane & 7)) * 128 + w * 16, oo = ob + (lane >> 5) * OREC + w * 512 + (lane & 31) * 16;
      pause<FK_D1>();
      STAMP(2);
      v4u o[4], ml;
      ml = ld4(rw, om);
      for (u32 n = 0; __any(bad4(ml, tg)); ++n) {
        if (dead || n > SPIN_MEM) { dead = true; break; }
        pause<FK_BO>();
        ml = ld4(rw, om);
      }
      STAMP(7);
#pragma unroll
      for (int t = 0; t < 4; ++t) o[t] = ld4(rw, oo, ((t + s) & 3) * 2 * OREC);
      float M = dec(ml.x);
      M = fmaxf(M, dppm<0xB1>(M)); M = fmaxf(M, dppm<0x4E>(M)); M = fmaxf(M, dppm<0x141>(M));
      const float e = __builtin_amdgcn_exp2f((dec(ml.x) - M) * L2E);
      float Ls = e * dec(ml.y);
      Ls += dppm<0xB1>(Ls); Ls += dppm<0x4E>(Ls); Ls += dppm<0x141>(Ls);
      const float en = e * __builtin_amdgcn_rcpf(Ls);
      float et[4];
#pragma unroll
      for (int t = 0; t < 4; ++t) {
        const float e0 = asf(__builtin_amdgcn_readlane(asu(en), 2 * ((t + s) & 3))), e1 = asf(__builtin_amdgcn_readlane(asu(en), 2 * ((t + s) & 3) + 1));
        et[t] = lane < 32 ? e0 : e1;
      }
      asm volatile("" : "+v"(et[0]), "+v"(et[1]), "+v"(et[2]), "+v"(et[3]));
      for (u32 n = 0;; ++n) {
        bool bad = false;
#pragma unroll
        for (int t = 0; t < 4; ++t) bad = bad || bad4(o[t], tg);
        if (!__any(bad)) break;
        if (dead || n > SPIN_MEM) { dead = true; break; }
        pause<FK_BO>();
#pragma unroll
        for (int t = 0; t < 4; ++t) if (half_any(bad4(o[t], tg), lane)) o[t] = ld4(rw, oo, ((t + s) & 3) * 2 * OREC);
      }
      STAMP(3);
      DIAG(6);
      f4 os = {0.f, 0.f, 0.f, 0.f};
#pragma unroll
      for (int t = 0; t < 4; ++t)
#pragma unroll
        for (int i = 0; i < 4; ++i) os[i] = fmaf(et[t], dec(o[t][i]), os[i]);
#pragma unroll
      for (int i = 0; i < 4; ++i) os[i] = x32_sum(os[i]);
      ((LAS v2u*)priv)[lane & 31] = v2u{pk(os[0], os[1]), pk(os[2], os[3])};
      f4 y[4];
#pragma unroll
      for (int i = 0; i < 4; ++i) y[i] = f4{0.f, 0.f, 0.f, 0.f};
#pragma unroll
      for (int u = 0; u < 4; ++u) {
        const v4u a = *(const LAS v4u*)(priv + (32 * u + 8 * g) * 2);
#pragma unroll
        for (int i = 0; i < 4; ++i) y[i] = mfma32(a, wf[u][i], y[i]);
      }
      if (lane < 16) *(LAS f4*)(L + L_YRED + w * 256 + c * 16) = f4{y[0][0], y[1][0], y[2][0], y[3][0]};
    }
    wg_barrier();
    {
      const LAS f4* yr = (const LAS f4*)(L + L_YRED) + c;
      const f4 y = (yr[0] + yr[16]) + (yr[32] + yr[48]);
      st4(rw, lane < 32 ? WS_Y + (u32)(2 * w + g) * 65536 + grp * 2048 + s * 256 + c * 16 : OOB, enc4(y, T));
    }
    STAMP(4);
    wg_barrier();
    wg_barrier();
    STAMP(6);
    FLUSH();
    return;
  }

  if (role == 3) {
    LAS unsigned char* priv = L + L_PRIV + 4096 + w * 1024;
    wg_barrier();
    pause<FK_W0>(); pause<FK_W0>();
    const v2u xp = *(const GAS v2u*)(p.x + b * DM + 128 * w + 4 * (lane & 31));
    v2u gr[4][8];
    v4u gf[4][4];
    if (ffn) {
      const GAS unsigned char* gp = (const GAS unsigned char*)(c < 8 ? p.wg : p.wu) + (size_t)(128 * w + 8 * g) * (FF * 2) + 64 * j + 8 * (c & 7);
#pragma unroll
      for (int u = 0; u < 4; ++u)
#pragma unroll
        for (int r = 0; r < 8; ++r) {
          const GAS v2u* a = (const GAS v2u*)(gp + (size_t)(32 * u + r) * (FF * 2));
          gr[u][r] = FK_WNT ? __builtin_nontemporal_load(a) : *a;
          pause<FK_P2>();
        }
#pragma unroll
      for (int u = 0; u < 4; ++u) { tr84(gr[u], gf[u]); asm volatile("" : "+v"(gf[u][0]), "+v"(gf[u][1]), "+v"(gf[u][2]), "+v"(gf[u][3])); }
    }
    lds_wait_ge(FLAG + 9, 4, dead);
    const u32 tg = FLAG[12];
    if (blockIdx.x == 0) {
      const u32 tlo = FLAG[10];
      for (u32 n = 0;; ++n) {
        const v2u gv = ld2(rw, WS_QG + (u32)(lane >> 3) * 16384 + w * 4096 + (lane & 7) * 512);
        if (__all(gv.y == tlo)) break;
        if (dead || n > SPIN_MEM) { dead = true; break; }
        pause<8>();
      }
    }
    STAMP(1);
    wg_barrier();
    wg_barrier();
    {
      const u32 yb = WS_Y + (u32)kvh * 65536 + (u32)b * 8 * 2048 + (lane >> 5) * 2048 + w * 512 + (lane & 31) * 16;
      pause<FK_D2>();
      STAMP(2);
      v4u y[4];
#pragma unroll
      for (int k = 0; k < 4; ++k) y[k] = ld4(rw, yb, ((k + s) & 3) * 4096);
      for (u32 n = 0;; ++n) {
        bool bad = false;
#pragma unroll
        for (int k = 0; k < 4; ++k) bad = bad || bad4(y[k], tg);
        if (!__any(bad)) break;
        if (dead || n > SPIN_MEM) { dead = true; break; }
        pause<FK_BO>();
#pragma unroll
        for (int k = 0; k < 4; ++k) if (half_any(bad4(y[k], tg), lane)) y[k] = ld4(rw, yb, ((k + s) & 3) * 4096);
      }
      STAMP(3);
      DIAG(7);
      f4 a;
#pragma unroll
      for (int i = 0; i < 4; ++i) {
        const float p0 = dec(y[0][i]), p1 = dec(y[1][i]), p2 = dec(y[2][i]), p3 = dec(y[3][i]);
        a[i] = x32_sum((s & 1) ? (p3 + p0) + (p1 + p2) : (p0 + p1) + (p2 + p3));
      }
      const u32 a0 = pk(a[0], a[1]), a1 = pk(a[2], a[3]);
      ((LAS v2u*)(L + L_HS))[32 * w + (lane & 31)] = v2u{pk(blo(xp.x) + blo(a0), bhi(xp.x) + bhi(a0)), pk(blo(xp.y) + blo(a1), bhi(xp.y) + bhi(a1))};
    }
    wg_barrier();
    STAMP(4);
    if (ffn) {
      const v4u hn = layernorm8(*(const LAS v4u*)(L + L_HS + lane * 16));
      *(LAS v4u*)(priv + lane * 16) = hn;
      f4 acc[4];
#pragma unroll
      for (int i = 0; i < 4; ++i) acc[i] = f4{0.f, 0.f, 0.f, 0.f};
#pragma unroll
      for (int u = 0; u < 4; ++u) {
        const v4u a = *(const LAS v4u*)(priv + (128 * w + 32 * u + 8 * g) * 2);
#pragma unroll
        for (int i = 0; i < 4; ++i) acc[i] = mfma32(a, gf[u][i], acc[i]);
      }
      if (lane < 16) *(LAS f4*)(L + L_GU + w * 256 + c * 16) = f4{acc[0][0], acc[1][0], acc[2][0], acc[3][0]};
    }
    STAMP(5);
    DIAG(8);
    wg_barrier();
    if (blockIdx.x == 0 && w == 0) st2(rw, lane == 0 ? WS_E : OOB, v2u{FLAG[10], FLAG[11]});
    FLUSH();
    return;
  }

  v4u qf[4];
  u32 tg;
  {
    STAMP(6);
    v4u x4[4];
#pragma unroll
    for (int t = 0; t < 4; ++t) x4[t] = *(const GAS v4u*)(p.x + g * DM + 128 * t + 8 * c);
    const v4u xa = *(const GAS v4u*)(p.x + (c & 3) * DM + 128 * b + 32 * w + 8 * g);
    const rsrc_t rq = mkr(p.wq, DM * NQ * 2);
    v2u wr[8];
#pragma unroll
    for (int r = 0; r < 8; ++r)
      wr[r] = __builtin_amdgcn_raw_buffer_load_b64(rq, g * 8 * 8192 + 8 * c, (128 * b + 32 * w + r) * 8192 + (kvh * 512 + 64 * s) * 2, 2);
    const v2u e = ld2(rw, WS_E);
    wg_barrier();
    STAMP(10);
    const float piv = dppm<0x150>(blo(x4[0][0]));
    float s1 = 0.f, s2 = 0.f;
#pragma unroll
    for (int t = 0; t < 4; ++t)
#pragma unroll
      for (int i = 0; i < 4; ++i) { const float d0 = blo(x4[t][i]) - piv, d1 = bhi(x4[t][i]) - piv; s1 += d0 + d1; s2 = fmaf(d0, d0, fmaf(d1, d1, s2)); }
    const f2 st = ln_stats(piv, rsum16(s1), rsum16(s2));
    const f2 sa = {asf(__builtin_amdgcn_ds_bpermute((c & 3) * 64, asu(st.x))), asf(__builtin_amdgcn_ds_bpermute((c & 3) * 64, asu(st.y)))};
    const v4u a = ln_apply(xa, sa);
    v4u f[4];
    tr84(wr, f);
    f4 acc[4];
#pragma unroll
    for (int i = 0; i < 4; ++i) acc[i] = mfma32(a, f[i], f4{0.f, 0.f, 0.f, 0.f});
    if (lane < 16) {
#pragma unroll
      for (int r = 0; r < 4; ++r) *(LAS f4*)(L + L_QRED + w * 1024 + r * 256 + c * 16) = f4{acc[0][r], acc[1][r], acc[2][r], acc[3][r]};
    }
    const u64 epoch = ((u64)__builtin_amdgcn_readfirstlane(e.y) << 32 | __builtin_amdgcn_readfirstlane(e.x)) + 1;
    const u32 tlo = (u32)epoch, thi = (u32)(epoch >> 32) + tlo * 0x9E3779B1u;
    const u32 dl = tlo - (u32)(epoch >> 32);
    tg = ((tlo + 1u) & 0xffu) | (((tlo >> 8) + 3u * dl + 1u) & 0xffu) << 8 | (((tlo >> 16) + 5u * dl + 1u) & 0xffu) << 16 | ((tlo >> 24) + 7u * dl + 1u) << 24;
    if (w == 0 && lane == 0) { FLAG[10] = tlo; FLAG[11] = (u32)(epoch >> 32); FLAG[12] = tg; }
    asm volatile("s_waitcnt lgkmcnt(0)" ::: "memory");
    STAMP(11);
    u32 old = 0;
    if (lane == 0) old = __hip_atomic_fetch_add((LAS u32*)(L + L_FLAG) + 8, 1u, __ATOMIC_RELAXED, __HIP_MEMORY_SCOPE_WORKGROUP);
    old = __builtin_amdgcn_readfirstlane(old);
    CB();
    if (old == 3) {
      const LAS f2* qr = (const LAS f2*)(L + L_QRED) + g * 32 + c;
      const f2 q0 = (qr[0] + qr[128]) + (qr[256] + qr[384]), q1 = (qr[16] + qr[144]) + (qr[272] + qr[400]);
      const u32 off = WS_QG + (u32)(g * 8 + kvh) * 16384 + b * 4096 + s * 512 + c * 16;
      st4(rw, off, v4u{asu(q0.x), tlo, asu(q0.y), thi});
      st4(rw, off + 256, v4u{asu(q1.x), tlo, asu(q1.y), thi});
    }
    STAMP(1);
    pause<FK_D0>();
    v4u gq[4];
    const u32 qo = WS_QG + grp * 16384 + w * 1024 + lane * 16;
#pragma unroll
    for (int t = 0; t < 4; ++t) gq[t] = ld4(rw, qo, t * 4096);
    for (u32 n = 0;; ++n) {
      bool bad = false;
#pragma unroll
      for (int t = 0; t < 4; ++t) bad = bad || gq[t].y != tlo || gq[t].w != thi;
      if (!__any(bad)) break;
      if (dead || n > SPIN_MEM) { dead = true; break; }
      pause<2>();
#pragma unroll
      for (int t = 0; t < 4; ++t) if (__any(gq[t].y != tlo || gq[t].w != thi)) gq[t] = ld4(rw, qo, t * 4096);
    }
    STAMP(12);
    ((LAS u32*)(L + L_QS))[64 * w + lane] = pk((asf(gq[0].x) + asf(gq[1].x)) + (asf(gq[2].x) + asf(gq[3].x)), (asf(gq[0].z) + asf(gq[1].z)) + (asf(gq[2].z) + asf(gq[3].z)));
    asm volatile("s_waitcnt lgkmcnt(0)" ::: "memory");
    if (lane == 0) __hip_atomic_fetch_add((LAS u32*)(L + L_FLAG) + 9, 1u, __ATOMIC_RELAXED, __HIP_MEMORY_SCOPE_WORKGROUP);
    lds_wait_ge(FLAG + 9, 4, dead);
#pragma unroll
    for (int u = 0; u < 4; ++u) qf[u] = *(const LAS v4u*)(L + L_QS + ((c & 3) * 128 + 32 * u + 8 * g) * 2);
    STAMP(2);
    DIAG(4);
  }
  float m = -INFINITY, l = 0.f;
  f4 acc[8];
#pragma unroll
  for (int i = 0; i < 8; ++i) acc[i] = f4{0.f, 0.f, 0.f, 0.f};
  const LAS unsigned char* ringk = L + L_RING + w * 32768 + c * 256;
  const u32 vrow = 4 * g + (c >> 2);
  const u32 va = (u32)(size_t)(L + L_RING) + w * 32768 + 4096 + vrow * 256 + (vrow & 7) * 32 + 8 * (c & 3);
  u32 seen = 0;
  auto need = [&](u32 n) {
    for (u32 k = 0; seen < n; ++k) {
      seen = __builtin_amdgcn_readfirstlane(FLAG[w]);
      if (seen >= n) break;
      if (dead || k > SPIN_LDS) { dead = true; break; }
      __builtin_amdgcn_s_sleep(1);
    }
    CB();
  };
#pragma unroll 1
  for (int i = 0; i < NCH; ++i) {
    const u32 sl = (i & 3) * 8192;
    need(2 * i + 1);
    v4u kf[4];
#pragma unroll
    for (int u = 0; u < 4; ++u) kf[u] = *(const LAS v4u*)(ringk + sl + (((4 * u + g) ^ c) << 4));
    f4 s0 = mfma32(kf[0], qf[0], f4{0.f, 0.f, 0.f, 0.f}), s1 = mfma32(kf[2], qf[2], f4{0.f, 0.f, 0.f, 0.f});
    s0 = mfma32(kf[1], qf[1], s0); s1 = mfma32(kf[3], qf[3], s1);
    const u32 a01 = pk(s0[0] + s1[0], s0[1] + s1[1]), a23 = pk(s0[2] + s1[2], s0[3] + s1[3]);
    const u32 b01 = pk(blo(a01) * SCALE, bhi(a01) * SCALE), b23 = pk(blo(a23) * SCALE, bhi(a23) * SCALE);
    const float t0 = blo(b01), t1 = bhi(b01), t2 = blo(b23), t3 = bhi(b23);
    const float mx = xg_max(fmaxf(fmaxf(t0, t1), fmaxf(t2, t3)));
    if (__any(mx > m)) {
      const float mn = fmaxf(m, mx), al = __builtin_amdgcn_exp2f((m - mn) * L2E);
      l *= al; m = mn;
#pragma unroll
      for (int d = 0; d < 8; ++d) acc[d] *= al;
    }
    const float p0 = __builtin_amdgcn_exp2f((t0 - m) * L2E), p1 = __builtin_amdgcn_exp2f((t1 - m) * L2E),
                p2 = __builtin_amdgcn_exp2f((t2 - m) * L2E), p3 = __builtin_amdgcn_exp2f((t3 - m) * L2E);
    l += (p0 + p1) + (p2 + p3);
    const v2u pf = {pk(p0, p1), pk(p2, p3)};
    if (i == NCH - 1) {
      const float lt = xg_sum(l);
      if (lane < 4) *(LAS f2*)(L + L_MLS + w * 32 + c * 8) = f2{m, lt};
    }
    need(2 * i + 2);
    s4 vf[8];
#pragma unroll
    for (int d = 0; d < 8; ++d) vf[d] = __builtin_amdgcn_ds_read_tr16_b64_v4i16((LAS s4*)(size_t)((va ^ (d << 5)) + sl));
    asm volatile("s_waitcnt lgkmcnt(0)" ::: "memory");
    FLAG[4 + w] = i + 1;
#pragma unroll
    for (int d = 0; d < 8; ++d) acc[d] = mfma16(vf[d], pf, acc[d]);
  }
  STAMP(4);
  if (c < 4) {
#pragma unroll
    for (int d = 0; d < 8; ++d) *(LAS f4*)(L + L_OM + w * 2048 + c * 512 + (16 * d + 4 * g) * 4) = acc[d];
  }
  wg_barrier();
  {
    const int hd = lane < 32 ? w : (lane & 3);
    f2 ml[4]; f4 ov[4];
#pragma unroll
    for (int k = 0; k < 4; ++k) { ml[k] = *(const LAS f2*)(L + L_MLS + k * 32 + hd * 8); ov[k] = *(const LAS f4*)(L + L_OM + k * 2048 + w * 512 + (lane & 31) * 16); }
    const float M = fmaxf(fmaxf(ml[0].x, ml[1].x), fmaxf(ml[2].x, ml[3].x));
    float Ls = 0.f; f4 O = {0.f, 0.f, 0.f, 0.f};
#pragma unroll
    for (int k = 0; k < 4; ++k) { const float e = __builtin_amdgcn_exp2f((ml[k].x - M) * L2E); Ls = fmaf(e, ml[k].y, Ls); O += ov[k] * e; }
    const u32 wgi = grp * 8 + s;
    st4(rw, lane < 32 ? WS_O + wgi * OREC + w * 512 + lane * 16 : (lane < 40 && w == 0 ? WS_ML + wgi * 128 + (lane - 32) * 16 : OOB), enc4(lane < 32 ? O : f4{M, Ls, M, Ls}, mktag(tg)));
  }
  STAMP(5);
  DIAG(5);
  wg_barrier();
  wg_barrier();
  wg_barrier();
    if (w < 2) {
      const int col = (kvh * 8 + s) * 8 + 4 * w;
      const u32 off = lane < 56 ? WS_D + (u32)(b * 56 + lane) * 2048 + col * 4 : OOB;
      const v2u hv = *(const LAS v2u*)(L + L_HS + col * 2);
      const u32 tgl = lane < 56 ? tg : 0u;
      pause<FK_D3>();
      STAMP(7);
      v4u d0 = ld4(rw, off);
      for (u32 n = 0; __any(bad4(d0, tgl)); ++n) {
        if (dead || n > SPIN_MEM) { dead = true; break; }
        pause<FK_BO>();
        d0 = ld4(rw, off);
      }
      STAMP(8);
      const u32 a0 = pk(wsum63(dec(d0[0])), wsum63(dec(d0[1]))), a1 = pk(wsum63(dec(d0[2])), wsum63(dec(d0[3])));
      if (lane == 63) *(GAS v2u*)(p.out + b * DM + col) = v2u{pk(blo(hv.x) + blo(a0), bhi(hv.x) + bhi(a0)), pk(blo(hv.y) + blo(a1), bhi(hv.y) + bhi(a1))};
      STAMP(9);
      if (FK_DIAGW) asm volatile("s_waitcnt vmcnt(0)" ::: "memory");
      if (FK_DIAG == 9 && blockIdx.x == FK_DIAGB && w == 0) {
        GAS u32* pe = (GAS u32*)(p.ws + WS_DBG);
        const u32 prev = __hip_atomic_load(pe, __ATOMIC_RELAXED, __HIP_MEMORY_SCOPE_AGENT);
        const u32 now = (u32)__builtin_amdgcn_s_memrealtime(); u32 x = dg0 - __builtin_amdgcn_readfirstlane(prev); x = x < 5000 ? x * FK_DIAGK : 0;
        while ((u32)__builtin_amdgcn_s_memrealtime() - now < x) __builtin_amdgcn_s_sleep(1);
        __hip_atomic_store(pe, (u32)__builtin_amdgcn_s_memrealtime(), __ATOMIC_RELAXED, __HIP_MEMORY_SCOPE_AGENT);
      } else
      if (blockIdx.x == FK_DIAGB && w == 0) { const u32 now = (u32)__builtin_amdgcn_s_memrealtime(), x = ((FK_DIAG == 3 ? now : FLAG[14]) - dg0) * FK_DIAGK;
        while ((u32)__builtin_amdgcn_s_memrealtime() - now < x && x < 100000) __builtin_amdgcn_s_sleep(1); }
    }
  FLUSH();
}
__global__ void tiny(unsigned char* ws) {
  const u32 t0 = (u32)__builtin_amdgcn_s_memrealtime();
  GAS u32* pe = (GAS u32*)(ws + WS_DBG);
  const u32 prev = __hip_atomic_load(pe, __ATOMIC_RELAXED, __HIP_MEMORY_SCOPE_AGENT);
  u32 x = t0 - __builtin_amdgcn_readfirstlane(prev); x = x < 5000 ? x * FK_TINYK : 0;
  while ((u32)__builtin_amdgcn_s_memrealtime() - t0 < x) __builtin_amdgcn_s_sleep(1);
  __hip_atomic_store(pe, (u32)__builtin_amdgcn_s_memrealtime(), __ATOMIC_RELAXED, __HIP_MEMORY_SCOPE_AGENT);
}
}

extern "C" void kernel_launch(void* const* d_in, const int* in_sizes, int n_in, void* d_out, int out_size, void* d_ws, size_t ws_size,
                              hipStream_t stream) {
  fk::Params p;
  p.x = (const fk::u16*)d_in[0]; p.k = (const fk::u16*)d_in[1]; p.v = (const fk::u16*)d_in[2];
  p.wq = (const fk::u16*)d_in[3]; p.wo = (const fk::u16*)d_in[4];
  p.wg = (const fk::u16*)d_in[6]; p.wu = (const fk::u16*)d_in[7]; p.wd = (const fk::u16*)d_in[8];
  p.out = (fk::u16*)d_out; p.ws = (unsigned char*)d_ws;
  hipLaunchKernelGGL(fk::fused_block, dim3(256), dim3(1024), 0, stream, p);
  hipLaunchKernelGGL(fk::tiny, dim3(1), dim3(64), 0, stream, p.ws);
}
```

```cpp
#include <hip/hip_runtime.h>
#include <stdint.h>

#define FK_W 5
#define FK_WNT 1
#define FK_W0 120
#define FK_P1 18
#define FK_P2 22
#define FK_ORD 1
#define FK_G0 0
#define FK_WB 7
#define FK_WC 10
#define FK_LG 60
#define FK_T0 80
#define FK_TAU 1830
#define FK_DIAG 0
#define FK_DIAGB 0
#define FK_DIAGK 1
#define FK_DB2 0
#define FK_KNT 1
#define FK_VNT 1
#define FK_BO 1
#define FK_D0 40
#define FK_D1 18
#define FK_D2 16
#define FK_D3 32
#define FK_STAMPS 0

namespace fk {
#define GAS __attribute__((address_space(1)))
#define LAS __attribute__((address_space(3)))
typedef unsigned u32;
typedef unsigned long long u64;
typedef unsigned short u16;
typedef u32 v2u __attribute__((ext_vector_type(2)));
typedef u32 v4u __attribute__((ext_vector_type(4)));
typedef int v4i __attribute__((ext_vector_type(4)));
typedef float f2 __attribute__((ext_vector_type(2)));
typedef float f4 __attribute__((ext_vector_type(4)));
typedef short s4 __attribute__((ext_vector_type(4)));
typedef __bf16 b2 __attribute__((ext_vector_type(2)));
typedef __bf16 b8 __attribute__((ext_vector_type(8)));

constexpr int DM = 512, SK = 16384, FF = 1792, NQ = 4096;
constexpr int NCH = 32;
constexpr float L2E = 1.4426950408889634f, SCALE = 0.08838834764831845f, EPS = 1e-5f;
constexpr u32 WS_E = 0, WS_QG = 4096, WS_O = WS_QG + 32 * 16384, OREC = 2048, WS_ML = WS_O + 256 * OREC  , WS_Y = WS_ML + 256 * 128,
              WS_D = WS_Y + 8 * 65536, WS_END = WS_D + 4 * 56 * 2048, WS_DBG = WS_END;
constexpr u32 OOB = 0x80000000u;
constexpr u32 L_RING = 0, L_FLAG = 131072  , L_QRED = L_FLAG + 256  , L_QS = L_QRED + 4096,
              L_PRIV = L_QS + 1024  , L_OM = L_PRIV + 8192, L_MLS = L_OM + 8192, L_YRED = L_MLS + 128, L_HS = L_YRED + 1024,
              L_GU = L_HS + 1024, L_END = L_GU + 1024;
constexpr u32 SPIN_LDS = 1u << 19, SPIN_MEM = 1u << 16;

struct Params {
  const u16 *x, *k, *v, *wq, *wo, *wg, *wu, *wd;
  u16* out;
  unsigned char* ws;
};

#define CB() asm volatile("" ::: "memory")
__device__ __forceinline__ void wg_barrier() {
  asm volatile("s_waitcnt lgkmcnt(0)\n\ts_barrier" ::: "memory");
  __builtin_amdgcn_sched_barrier(0);
}
__device__ __forceinline__ float asf(u32 x) { return __builtin_bit_cast(float, x); }
__device__ __forceinline__ u32 asu(float x) { return __builtin_bit_cast(u32, x); }
__device__ __forceinline__ u32 pk(float lo, float hi) { f2 z = {lo, hi}; return __builtin_bit_cast(u32, __builtin_convertvector(z, b2)); }
__device__ __forceinline__ float blo(u32 p) { return asf(p << 16); }
__device__ __forceinline__ float bhi(u32 p) { return asf(p & 0xffff0000u); }
__device__ __forceinline__ float rb(float x) { return blo(pk(x, x)); }
template <int CTRL, int RM = 0xf> __device__ __forceinline__ float dpp0(float x) {
  return asf(__builtin_amdgcn_update_dpp(0, asu(x), CTRL, RM, 0xf, true));
}
template <int CTRL> __device__ __forceinline__ float dppm(float x) { return asf(__builtin_amdgcn_mov_dpp(asu(x), CTRL, 0xf, 0xf, true)); }
__device__ __forceinline__ float wsum63(float v) {
  v += dpp0<0x111>(v); v += dpp0<0x112>(v); v += dpp0<0x114>(v); v += dpp0<0x118>(v);
  v += asf(__builtin_amdgcn_update_dpp(0, asu(v), 0x142, 0xa, 0xf, false));
  v += asf(__builtin_amdgcn_update_dpp(0, asu(v), 0x143, 0xc, 0xf, false));
  return v;
}
__device__ __forceinline__ float wsum(float v) { return asf(__builtin_amdgcn_readlane(asu(wsum63(v)), 63)); }
__device__ __forceinline__ float xg_max(float x) {
  auto s = __builtin_amdgcn_permlane16_swap(asu(x), asu(x), false, false);
  x = fmaxf(asf(s[0]), asf(s[1]));
  auto t = __builtin_amdgcn_permlane32_swap(asu(x), asu(x), false, false);
  return fmaxf(asf(t[0]), asf(t[1]));
}
__device__ __forceinline__ float xg_sum(float x) {
  auto s = __builtin_amdgcn_permlane16_swap(asu(x), asu(x), false, false);
  x = asf(s[0]) + asf(s[1]);
  auto t = __builtin_amdgcn_permlane32_swap(asu(x), asu(x), false, false);
  return asf(t[0]) + asf(t[1]);
}
__device__ __forceinline__ float x32_sum(float x) {
  auto t = __builtin_amdgcn_permlane32_swap(asu(x), asu(x), false, false);
  return asf(t[0]) + asf(t[1]);
}
__device__ __forceinline__ f4 mfma32(v4u a, v4u b, f4 c) {
  return __builtin_amdgcn_mfma_f32_16x16x32_bf16(__builtin_bit_cast(b8, a), __builtin_bit_cast(b8, b), c, 0, 0, 0);
}
__device__ __forceinline__ f4 mfma16(s4 a, v2u b, f4 c) {
  return __builtin_amdgcn_mfma_f32_16x16x16bf16_1k(a, __builtin_bit_cast(s4, b), c, 0, 0, 0);
}
__device__ __forceinline__ u32 plo(u32 r1, u32 r0) { return __builtin_amdgcn_perm(r1, r0, 0x05040100u); }
__device__ __forceinline__ u32 phi(u32 r1, u32 r0) { return __builtin_amdgcn_perm(r1, r0, 0x07060302u); }
__device__ __forceinline__ void tr84(const v2u (&r)[8], v4u (&f)[4]) {
#pragma unroll
  for (int t = 0; t < 4; ++t) {
    f[0][t] = plo(r[2 * t + 1].x, r[2 * t].x); f[1][t] = phi(r[2 * t + 1].x, r[2 * t].x);
    f[2][t] = plo(r[2 * t + 1].y, r[2 * t].y); f[3][t] = phi(r[2 * t + 1].y, r[2 * t].y);
  }
}
typedef __amdgpu_buffer_rsrc_t rsrc_t;
__device__ __forceinline__ rsrc_t mkr(const void* p, u32 bytes) { return __builtin_amdgcn_make_buffer_rsrc((void*)p, 0, bytes, 0x00020000); }
__device__ __forceinline__ v4i mkr_s(const void* p, u32 bytes) {
  const u64 a = (u64)p; v4i r;
  r.x = __builtin_amdgcn_readfirstlane((u32)a); r.y = __builtin_amdgcn_readfirstlane((u32)(a >> 32)); r.z = (int)bytes; r.w = 0x00020000;
  return r;
}
constexpr int SC1 = 16, WAUX = FK_WNT ? 2 : 0;
__device__ __forceinline__ v2u ld2(rsrc_t r, u32 vo, u32 so = 0) { return __builtin_amdgcn_raw_buffer_load_b64(r, vo, so, SC1); }
__device__ __forceinline__ v4u ld4(rsrc_t r, u32 vo, u32 so = 0) { return __builtin_amdgcn_raw_buffer_load_b128(r, vo, so, SC1); }
__device__ __forceinline__ void st2(rsrc_t r, u32 vo, v2u v) { __builtin_amdgcn_raw_buffer_store_b64(v, r, vo, 0, SC1); }
__device__ __forceinline__ void st4(rsrc_t r, u32 vo, v4u v) { __builtin_amdgcn_raw_buffer_store_b128(v, r, vo, 0, SC1); }

#define STAMP(i) do {} while (0)
#define FLUSH() do {} while (0)

template <int N> __device__ __forceinline__ void pause() { if (N > 0) asm volatile("s_sleep %0" ::"i"(N) : "memory"); }
__device__ __forceinline__ float rsum16(float v) {
  v += dppm<0x128>(v); v += dppm<0x124>(v); v += dppm<0x122>(v); v += dppm<0x121>(v);
  return v;
}
struct Tag { u32 b[4]; };
__device__ __forceinline__ Tag mktag(u32 t) { return Tag{{t & 0xffu, (t >> 8) & 0xffu, (t >> 16) & 0xffu, t >> 24}}; }
__device__ __forceinline__ u32 enc(float x, u32 tb) { return ((asu(x) + 0x80u) & 0xffffff00u) | tb; }
__device__ __forceinline__ v4u enc4(f4 x, const Tag& t) { return v4u{enc(x[0], t.b[0]), enc(x[1], t.b[1]), enc(x[2], t.b[2]), enc(x[3], t.b[3])}; }
__device__ __forceinline__ float dec(u32 y) { return asf(y & 0xffffff00u); }
__device__ __forceinline__ bool bad2(v2u y, u32 t16) { return (__builtin_amdgcn_perm(y.y, y.x, 0x0c0c0400u)) != t16; }
__device__ __forceinline__ bool bad4(v4u y, u32 t32) {
  return __builtin_amdgcn_perm(__builtin_amdgcn_perm(y.w, y.z, 0x0c0c0400u), __builtin_amdgcn_perm(y.y, y.x, 0x0c0c0400u), 0x05040100u) != t32;
}
__device__ __forceinline__ bool half_any(bool x, int lane) { const u64 m = __ballot(x); return (lane < 32 ? (u32)m : (u32)(m >> 32)) != 0u; }
__device__ __forceinline__ f2 ln_stats(float piv, float s1, float s2) {
  const float md = s1 * (1.f / DM), e2 = s2 * (1.f / DM);
  const float var = rb(fmaxf(fmaf(-md, md, e2), 0.f));
  return f2{rb(piv + md), __builtin_amdgcn_rcpf(rb(__builtin_amdgcn_sqrtf(rb(var + EPS))))};
}
__device__ __forceinline__ v4u ln_apply(v4u xr, f2 st) {
  v4u o;
#pragma unroll
  for (int i = 0; i < 4; ++i) { const u32 d = pk(blo(xr[i]) - st.x, bhi(xr[i]) - st.x); o[i] = pk(blo(d) * st.y, bhi(d) * st.y); }
  return o;
}
__device__ __forceinline__ v4u layernorm8(v4u xr) {
  const float piv = asf(__builtin_amdgcn_readfirstlane(xr[0] << 16));
  float s1 = 0.f, s2 = 0.f;
#pragma unroll
  for (int i = 0; i < 4; ++i) { const float d0 = blo(xr[i]) - piv, d1 = bhi(xr[i]) - piv; s1 += d0 + d1; s2 = fmaf(d0, d0, fmaf(d1, d1, s2)); }
  s1 = wsum63(s1); s2 = wsum63(s2);
  return ln_apply(xr, ln_stats(piv, asf(__builtin_amdgcn_readlane(asu(s1), 63)), asf(__builtin_amdgcn_readlane(asu(s2), 63))));
}

#define FK_PIECE(T, VO) \
  "s_waitcnt vmcnt(%c[wm])\n\t" \
  ".if %c[pt] == " #T "\n\tds_write_b32 %[fa], %[fv]\n\t.endif\n\t" \
  ".if %c[nt]\n\tbuffer_load_dwordx4 " VO ", %[rs], %[so] offen offset:" #T "*1024 nt lds\n\t.else\n\tbuffer_load_dwordx4 " VO ", %[rs], %[so] offen offset:" #T "*1024 lds\n\t.endif\n\t"
template <int W, int NT>
__device__ __forceinline__ void dma_tile(v4i rs, u32 so, u32 la, u32 v0, u32 v1, u32 v2, u32 v3, u32 fa, int tn) {
  const int landed = tn - 1 - (W - 5) / 4;
  asm volatile("s_mov_b32 m0, %[la]\n\t" FK_PIECE(0, "%[v0]") FK_PIECE(1, "%[v1]") FK_PIECE(2, "%[v2]") FK_PIECE(3, "%[v3]")
               :
               : [rs] "s"(rs), [so] "s"(so), [la] "s"(la), [v0] "v"(v0), [v1] "v"(v1), [v2] "v"(v2), [v3] "v"(v3), [fa] "v"(fa),
                 [fv] "v"(landed > 0 ? landed : 0), [wm] "i"(W - 1), [pt] "i"((W - 5) % 4), [nt] "i"(NT)
               : "memory");
}
template <int NT>
__device__ __forceinline__ void dma_adapt(int lag, v4i rs, u32 so, u32 la, u32 v0, u32 v1, u32 v2, u32 v3, u32 fa, int tn) {
  if (lag <= 0) dma_tile<FK_W, NT>(rs, so, la, v0, v1, v2, v3, fa, tn);
  else if (lag <= FK_LG) dma_tile<FK_WB, NT>(rs, so, la, v0, v1, v2, v3, fa, tn);
  else dma_tile<FK_WC, NT>(rs, so, la, v0, v1, v2, v3, fa, tn);
}
__device__ __forceinline__ bool lds_wait_ge(volatile LAS u32* w, u32 need, bool& dead) {
  for (u32 n = 0;; ++n) {
    if ((u32)__builtin_amdgcn_readfirstlane(*w) >= need) break;
    if (dead || n > SPIN_LDS) { dead = true; break; }
    __builtin_amdgcn_s_sleep(1);
  }
  CB();
  return true;
}

__global__ void __launch_bounds__(1024, 4) fused_block(Params p) {
  __shared__ __attribute__((aligned(1024))) unsigned char lds_raw[L_END];
  LAS unsigned char* L = (LAS unsigned char*)lds_raw;
  const int tid = threadIdx.x, lane = tid & 63, wave = __builtin_amdgcn_readfirstlane(tid >> 6), w = wave & 3, role = wave >> 2;
  const int g = lane >> 4, c = lane & 15;
  const int kvh = blockIdx.x & 7, b = blockIdx.x >> 6, s = (blockIdx.x >> 3) & 7, grp = b * 8 + kvh;
  const rsrc_t rw = mkr(p.ws, WS_END);
  volatile LAS u32* FLAG = (volatile LAS u32*)(L + L_FLAG);
  const bool ffn = s < 7;
  const int j = kvh * 7 + s;
  bool dead = false;
  STAMP(0);
#define DIAG(n) do {} while (0)

  if (role == 1) {
    __builtin_amdgcn_s_setprio(3);
    const u32 tb = (u32)__builtin_amdgcn_s_memrealtime() + FK_T0;
    const v4i rk = mkr_s(p.k + (size_t)grp * SK * 128, SK * 256), rv = mkr_s(p.v + (size_t)grp * SK * 128, SK * 256);
    const u32 k0 = g * 256 + ((c ^ g) << 4), k1 = g * 256 + ((c ^ (4 + g)) << 4), k2 = g * 256 + ((c ^ (8 + g)) << 4), k3 = g * 256 + ((c ^ (12 + g)) << 4);
    const u32 u0 = g * 256 + ((c ^ (g << 1)) << 4), u1 = g * 256 + ((c ^ ((4 + g) << 1)) << 4);
    const u32 ring = (u32)(size_t)(L + L_RING) + w * 32768, fa = (u32)(size_t)(FLAG + w);
    STAMP(5);
    if (lane < 4) FLAG[w + 4 * lane] = 0u;
    wg_barrier();
    STAMP(7);
    dma_tile<FK_W, FK_KNT>(rk, (u32)(s * 4 + w) * 4096u, ring, k0, k1, k2, k3, fa, 0);
    STAMP(6);
    dma_tile<FK_W, FK_VNT>(rv, (u32)(s * 4 + w) * 4096u, ring + 4096, u0, u1, u0, u1, fa, 1);
    STAMP(8);
    DIAG(1);
#pragma unroll 1
    for (int i = 1; i < NCH; ++i) {
      const u32 so = (u32)((i * 8 + s) * 4 + w) * 4096u, la = ring + (i & 3) * 8192;
      if (i >= 4) lds_wait_ge(FLAG + 4 + w, i - 3, dead);
      const int lag = (int)((u32)__builtin_amdgcn_s_memrealtime() - tb) - ((i * FK_TAU) >> 4);
      dma_adapt<FK_KNT>(lag, rk, so, la, k0, k1, k2, k3, fa, 2 * i);
      dma_adapt<FK_VNT>(lag, rv, so, la + 4096, u0, u1, u0, u1, fa, 2 * i + 1);
    }
    asm volatile("s_waitcnt vmcnt(8)\n\tds_write_b32 %0, %1\n\ts_waitcnt vmcnt(4)\n\tds_write_b32 %0, %2\n\ts_waitcnt vmcnt(0)\n\tds_write_b32 %0, %3" ::"v"(fa),
                 "v"(2 * NCH - 2), "v"(2 * NCH - 1), "v"(2 * NCH) : "memory");
    STAMP(1);
    DIAG(2);
    __builtin_amdgcn_s_setprio(0);
    v4u dr[8], df[8];
    const rsrc_t rd = mkr(p.wd, FF * DM * 2);
#pragma unroll
    for (int r = 0; r < 8; ++r) dr[r] = __builtin_amdgcn_raw_buffer_load_b128(rd, ffn ? (8 * g) * 1024 + 16 * c : OOB, (32 * j + r) * 1024 + 256 * w, WAUX);
    wg_barrier();
    const Tag T = mktag(FLAG[12]);
    LAS unsigned char* priv = L + L_QRED + w * 1024;
#pragma unroll
    for (int t = 0; t < 4; ++t)
#pragma unroll
      for (int q = 0; q < 4; ++q) { df[2 * q][t] = plo(dr[2 * t + 1][q], dr[2 * t][q]); df[2 * q + 1][t] = phi(dr[2 * t + 1][q], dr[2 * t][q]); }
    asm volatile("" : "+v"(df[0]), "+v"(df[1]), "+v"(df[2]), "+v"(df[3]), "+v"(df[4]), "+v"(df[5]), "+v"(df[6]), "+v"(df[7]));
    STAMP(2);
    wg_barrier();
    wg_barrier();
    wg_barrier();
    STAMP(3);
    if (ffn) {
      const LAS float* gu = (const LAS float*)(L + L_GU) + (lane & 31);
      const float gt = rb((gu[0] + gu[64]) + (gu[128] + gu[192])), up = rb((gu[32] + gu[96]) + (gu[160] + gu[224]));
      const float sg = rb(gt * __builtin_amdgcn_rcpf(1.f + __builtin_amdgcn_exp2f(-gt * L2E)));
      ((LAS u16*)priv)[lane & 31] = (u16)pk(sg * up, 0.f);
      const v4u a = *(const LAS v4u*)(priv + 16 * g);
      f4 o[8];
#pragma unroll
      for (int i = 0; i < 8; ++i) o[i] = mfma32(a, df[i], f4{0.f, 0.f, 0.f, 0.f});
      const bool hi = g & 1;
      const v4u ov = enc4(f4{hi ? o[4][0] : o[0][0], hi ? o[5][0] : o[1][0], hi ? o[6][0] : o[2][0], hi ? o[7][0] : o[3][0]}, T);
      st4(rw, lane < 32 ? WS_D + (u32)(b * 56 + j) * 2048 + (u32)(128 * w + 8 * c + 4 * g) * 4 : OOB, ov);
    }
    STAMP(4);
    FLUSH();
    return;
  }

  if (role == 2) {
    LAS unsigned char* priv = L + L_PRIV + w * 1024;
    wg_barrier();
    pause<FK_W0>(); pause<FK_W0>();
    v2u wor[4][8];
    const rsrc_t ro = mkr(p.wo, NQ * DM * 2);
#pragma unroll
    for (int u = 0; u < 4; ++u)
#pragma unroll
      for (int r = 0; r < 8; ++r) {
        wor[u][r] = __builtin_amdgcn_raw_buffer_load_b64(ro, g * 8 * 1024 + 8 * c, (kvh * 512 + 128 * w + 32 * u + r) * 1024 + 128 * s, WAUX);
        pause<FK_P1>();
      }
    v4u wf[4][4];
#pragma unroll
    for (int u = 0; u < 4; ++u) { tr84(wor[u], wf[u]); asm volatile("" : "+v"(wf[u][0]), "+v"(wf[u][1]), "+v"(wf[u][2]), "+v"(wf[u][3])); }
    lds_wait_ge(FLAG + 9, 4, dead);
    const u32 tg = FLAG[12];
    const Tag T = mktag(tg);
    STAMP(1);
    wg_barrier();
    {
      const u32 ob = WS_O + grp * 8 * OREC, om = WS_ML + (grp * 8 + (lane & 7)) * 128 + w * 16, oo = ob + (lane >> 5) * OREC + w * 512 + (lane & 31) * 16;
      pause<FK_D1>();
      STAMP(2);
      v4u o[4], ml;
      ml = ld4(rw, om);
      for (u32 n = 0; __any(bad4(ml, tg)); ++n) {
        if (dead || n > SPIN_MEM) { dead = true; break; }
        pause<FK_BO>();
        ml = ld4(rw, om);
      }
      STAMP(7);
#pragma unroll
      for (int t = 0; t < 4; ++t) o[t] = ld4(rw, oo, ((t + s) & 3) * 2 * OREC);
      float M = dec(ml.x);
      M = fmaxf(M, dppm<0xB1>(M)); M = fmaxf(M, dppm<0x4E>(M)); M = fmaxf(M, dppm<0x141>(M));
      const float e = __builtin_amdgcn_exp2f((dec(ml.x) - M) * L2E);
      float Ls = e * dec(ml.y);
      Ls += dppm<0xB1>(Ls); Ls += dppm<0x4E>(Ls); Ls += dppm<0x141>(Ls);
      const float en = e * __builtin_amdgcn_rcpf(Ls);
      float et[4];
#pragma unroll
      for (int t = 0; t < 4; ++t) {
        const float e0 = asf(__builtin_amdgcn_readlane(asu(en), 2 * ((t + s) & 3))), e1 = asf(__builtin_amdgcn_readlane(asu(en), 2 * ((t + s) & 3) + 1));
        et[t] = lane < 32 ? e0 : e1;
      }
      asm volatile("" : "+v"(et[0]), "+v"(et[1]), "+v"(et[2]), "+v"(et[3]));
      for (u32 n = 0;; ++n) {
        bool bad = false;
#pragma unroll
        for (int t = 0; t < 4; ++t) bad = bad || bad4(o[t], tg);
        if (!__any(bad)) break;
        if (dead || n > SPIN_MEM) { dead = true; break; }
        pause<FK_BO>();
#pragma unroll
        for (int t = 0; t < 4; ++t) if (half_any(bad4(o[t], tg), lane)) o[t] = ld4(rw, oo, ((t + s) & 3) * 2 * OREC);
      }
      STAMP(3);
      DIAG(6);
      f4 os = {0.f, 0.f, 0.f, 0.f};
#pragma unroll
      for (int t = 0; t < 4; ++t)
#pragma unroll
        for (int i = 0; i < 4; ++i) os[i] = fmaf(et[t], dec(o[t][i]), os[i]);
#pragma unroll
      for (int i = 0; i < 4; ++i) os[i] = x32_sum(os[i]);
      ((LAS v2u*)priv)[lane & 31] = v2u{pk(os[0], os[1]), pk(os[2], os[3])};
      f4 y[4];
#pragma unroll
      for (int i = 0; i < 4; ++i) y[i] = f4{0.f, 0.f, 0.f, 0.f};
#pragma unroll
      for (int u = 0; u < 4; ++u) {
        const v4u a = *(const LAS v4u*)(priv + (32 * u + 8 * g) * 2);
#pragma unroll
        for (int i = 0; i < 4; ++i) y[i] = mfma32(a, wf[u][i], y[i]);
      }
      if (lane < 16) *(LAS f4*)(L + L_YRED + w * 256 + c * 16) = f4{y[0][0], y[1][0], y[2][0], y[3][0]};
    }
    wg_barrier();
    {
      const LAS f4* yr = (const LAS f4*)(L + L_YRED) + c;
      const f4 y = (yr[0] + yr[16]) + (yr[32] + yr[48]);
      st4(rw, lane < 32 ? WS_Y + (u32)(2 * w + g) * 65536 + grp * 2048 + s * 256 + c * 16 : OOB, enc4(y, T));
    }
    STAMP(4);
    wg_barrier();
    wg_barrier();
    STAMP(6);
    FLUSH();
    return;
  }

  if (role == 3) {
    LAS unsigned char* priv = L + L_PRIV + 4096 + w * 1024;
    wg_barrier();
    pause<FK_W0>(); pause<FK_W0>();
    const v2u xp = *(const GAS v2u*)(p.x + b * DM + 128 * w + 4 * (lane & 31));
    v2u gr[4][8];
    v4u gf[4][4];
    if (ffn) {
      const GAS unsigned char* gp = (const GAS unsigned char*)(c < 8 ? p.wg : p.wu) + (size_t)(128 * w + 8 * g) * (FF * 2) + 64 * j + 8 * (c & 7);
#pragma unroll
      for (int u = 0; u < 4; ++u)
#pragma unroll
        for (int r = 0; r < 8; ++r) {
          const GAS v2u* a = (const GAS v2u*)(gp + (size_t)(32 * u + r) * (FF * 2));
          gr[u][r] = FK_WNT ? __builtin_nontemporal_load(a) : *a;
          pause<FK_P2>();
        }
#pragma unroll
      for (int u = 0; u < 4; ++u) { tr84(gr[u], gf[u]); asm volatile("" : "+v"(gf[u][0]), "+v"(gf[u][1]), "+v"(gf[u][2]), "+v"(gf[u][3])); }
    }
    lds_wait_ge(FLAG + 9, 4, dead);
    const u32 tg = FLAG[12];
    if (blockIdx.x == 0) {
      const u32 tlo = FLAG[10];
      for (u32 n = 0;; ++n) {
        const v2u gv = ld2(rw, WS_QG + (u32)(lane >> 3) * 16384 + w * 4096 + (lane & 7) * 512);
        if (__all(gv.y == tlo)) break;
        if (dead || n > SPIN_MEM) { dead = true; break; }
        pause<8>();
      }
    }
    STAMP(1);
    wg_barrier();
    wg_barrier();
    {
      const u32 yb = WS_Y + (u32)kvh * 65536 + (u32)b * 8 * 2048 + (lane >> 5) * 2048 + w * 512 + (lane & 31) * 16;
      pause<FK_D2>();
      STAMP(2);
      v4u y[4];
#pragma unroll
      for (int k = 0; k < 4; ++k) y[k] = ld4(rw, yb, ((k + s) & 3) * 4096);
      for (u32 n = 0;; ++n) {
        bool bad = false;
#pragma unroll
        for (int k = 0; k < 4; ++k) bad = bad || bad4(y[k], tg);
        if (!__any(bad)) break;
        if (dead || n > SPIN_MEM) { dead = true; break; }
        pause<FK_BO>();
#pragma unroll
        for (int k = 0; k < 4; ++k) if (half_any(bad4(y[k], tg), lane)) y[k] = ld4(rw, yb, ((k + s) & 3) * 4096);
      }
      STAMP(3);
      DIAG(7);
      f4 a;
#pragma unroll
      for (int i = 0; i < 4; ++i) {
        const float p0 = dec(y[0][i]), p1 = dec(y[1][i]), p2 = dec(y[2][i]), p3 = dec(y[3][i]);
        a[i] = x32_sum((s & 1) ? (p3 + p0) + (p1 + p2) : (p0 + p1) + (p2 + p3));
      }
      const u32 a0 = pk(a[0], a[1]), a1 = pk(a[2], a[3]);
      ((LAS v2u*)(L + L_HS))[32 * w + (lane & 31)] = v2u{pk(blo(xp.x) + blo(a0), bhi(xp.x) + bhi(a0)), pk(blo(xp.y) + blo(a1), bhi(xp.y) + bhi(a1))};
    }
    wg_barrier();
    STAMP(4);
    if (ffn) {
      const v4u hn = layernorm8(*(const LAS v4u*)(L + L_HS + lane * 16));
      *(LAS v4u*)(priv + lane * 16) = hn;
      f4 acc[4];
#pragma unroll
      for (int i = 0; i < 4; ++i) acc[i] = f4{0.f, 0.f, 0.f, 0.f};
#pragma unroll
      for (int u = 0; u < 4; ++u) {
        const v4u a = *(const LAS v4u*)(priv + (128 * w + 32 * u + 8 * g) * 2);
#pragma unroll
        for (int i = 0; i < 4; ++i) acc[i] = mfma32(a, gf[u][i], acc[i]);
      }
      if (lane < 16) *(LAS f4*)(L + L_GU + w * 256 + c * 16) = f4{acc[0][0], acc[1][0], acc[2][0], acc[3][0]};
    }
    STAMP(5);
    DIAG(8);
    wg_barrier();
    if (blockIdx.x == 0 && w == 0) st2(rw, lane == 0 ? WS_E : OOB, v2u{FLAG[10], FLAG[11]});
    FLUSH();
    return;
  }

  v4u qf[4];
  u32 tg;
  {
    STAMP(6);
    v4u x4[4];
#pragma unroll
    for (int t = 0; t < 4; ++t) x4[t] = *(const GAS v4u*)(p.x + g * DM + 128 * t + 8 * c);
    const v4u xa = *(const GAS v4u*)(p.x + (c & 3) * DM + 128 * b + 32 * w + 8 * g);
    const rsrc_t rq = mkr(p.wq, DM * NQ * 2);
    v2u wr[8];
#pragma unroll
    for (int r = 0; r < 8; ++r)
      wr[r] = __builtin_amdgcn_raw_buffer_load_b64(rq, g * 8 * 8192 + 8 * c, (128 * b + 32 * w + r) * 8192 + (kvh * 512 + 64 * s) * 2, 2);
    const v2u e = ld2(rw, WS_E);
    wg_barrier();
    STAMP(10);
    const float piv = dppm<0x150>(blo(x4[0][0]));
    float s1 = 0.f, s2 = 0.f;
#pragma unroll
    for (int t = 0; t < 4; ++t)
#pragma unroll
      for (int i = 0; i < 4; ++i) { const float d0 = blo(x4[t][i]) - piv, d1 = bhi(x4[t][i]) - piv; s1 += d0 + d1; s2 = fmaf(d0, d0, fmaf(d1, d1, s2)); }
    const f2 st = ln_stats(piv, rsum16(s1), rsum16(s2));
    const f2 sa = {asf(__builtin_amdgcn_ds_bpermute((c & 3) * 64, asu(st.x))), asf(__builtin_amdgcn_ds_bpermute((c & 3) * 64, asu(st.y)))};
    const v4u a = ln_apply(xa, sa);
    v4u f[4];
    tr84(wr, f);
    f4 acc[4];
#pragma unroll
    for (int i = 0; i < 4; ++i) acc[i] = mfma32(a, f[i], f4{0.f, 0.f, 0.f, 0.f});
    if (lane < 16) {
#pragma unroll
      for (int r = 0; r < 4; ++r) *(LAS f4*)(L + L_QRED + w * 1024 + r * 256 + c * 16) = f4{acc[0][r], acc[1][r], acc[2][r], acc[3][r]};
    }
    const u64 epoch = ((u64)__builtin_amdgcn_readfirstlane(e.y) << 32 | __builtin_amdgcn_readfirstlane(e.x)) + 1;
    const u32 tlo = (u32)epoch, thi = (u32)(epoch >> 32) + tlo * 0x9E3779B1u;
    const u32 dl = tlo - (u32)(epoch >> 32);
    tg = ((tlo + 1u) & 0xffu) | (((tlo >> 8) + 3u * dl + 1u) & 0xffu) << 8 | (((tlo >> 16) + 5u * dl + 1u) & 0xffu) << 16 | ((tlo >> 24) + 7u * dl + 1u) << 24;
    if (w == 0 && lane == 0) { FLAG[10] = tlo; FLAG[11] = (u32)(epoch >> 32); FLAG[12] = tg; }
    asm volatile("s_waitcnt lgkmcnt(0)" ::: "memory");
    STAMP(11);
    u32 old = 0;
    if (lane == 0) old = __hip_atomic_fetch_add((LAS u32*)(L + L_FLAG) + 8, 1u, __ATOMIC_RELAXED, __HIP_MEMORY_SCOPE_WORKGROUP);
    old = __builtin_amdgcn_readfirstlane(old);
    CB();
    if (old == 3) {
      const LAS f2* qr = (const LAS f2*)(L + L_QRED) + g * 32 + c;
      const f2 q0 = (qr[0] + qr[128]) + (qr[256] + qr[384]), q1 = (qr[16] + qr[144]) + (qr[272] + qr[400]);
      const u32 off = WS_QG + (u32)(g * 8 + kvh) * 16384 + b * 4096 + s * 512 + c * 16;
      st4(rw, off, v4u{asu(q0.x), tlo, asu(q0.y), thi});
      st4(rw, off + 256, v4u{asu(q1.x), tlo, asu(q1.y), thi});
    }
    STAMP(1);
    pause<FK_D0>();
    v4u gq[4];
    const u32 qo = WS_QG + grp * 16384 + w * 1024 + lane * 16;
#pragma unroll
    for (int t = 0; t < 4; ++t) gq[t] = ld4(rw, qo, t * 4096);
    for (u32 n = 0;; ++n) {
      bool bad = false;
#pragma unroll
      for (int t = 0; t < 4; ++t) bad = bad || gq[t].y != tlo || gq[t].w != thi;
      if (!__any(bad)) break;
      if (dead || n > SPIN_MEM) { dead = true; break; }
      pause<2>();
#pragma unroll
      for (int t = 0; t < 4; ++t) if (__any(gq[t].y != tlo || gq[t].w != thi)) gq[t] = ld4(rw, qo, t * 4096);
    }
    STAMP(12);
    ((LAS u32*)(L + L_QS))[64 * w + lane] = pk((asf(gq[0].x) + asf(gq[1].x)) + (asf(gq[2].x) + asf(gq[3].x)), (asf(gq[0].z) + asf(gq[1].z)) + (asf(gq[2].z) + asf(gq[3].z)));
    asm volatile("s_waitcnt lgkmcnt(0)" ::: "memory");
    if (lane == 0) __hip_atomic_fetch_add((LAS u32*)(L + L_FLAG) + 9, 1u, __ATOMIC_RELAXED, __HIP_MEMORY_SCOPE_WORKGROUP);
    lds_wait_ge(FLAG + 9, 4, dead);
#pragma unroll
    for (int u = 0; u < 4; ++u) qf[u] = *(const LAS v4u*)(L + L_QS + ((c & 3) * 128 + 32 * u + 8 * g) * 2);
    STAMP(2);
    DIAG(4);
  }
  float m = -INFINITY, l = 0.f;
  f4 acc[8];
#pragma unroll
  for (int i = 0; i < 8; ++i) acc[i] = f4{0.f, 0.f, 0.f, 0.f};
  const LAS unsigned char* ringk = L + L_RING + w * 32768 + c * 256;
  const u32 vrow = 4 * g + (c >> 2);
  const u32 va = (u32)(size_t)(L + L_RING) + w * 32768 + 4096 + vrow * 256 + (vrow & 7) * 32 + 8 * (c & 3);
  u32 seen = 0;
  auto need = [&](u32 n) {
    for (u32 k = 0; seen < n; ++k) {
      seen = __builtin_amdgcn_readfirstlane(FLAG[w]);
      if (seen >= n) break;
      if (dead || k > SPIN_LDS) { dead = true; break; }
      __builtin_amdgcn_s_sleep(1);
    }
    CB();
  };
#pragma unroll 1
  for (int i = 0; i < NCH; ++i) {
    const u32 sl = (i & 3) * 8192;
    need(2 * i + 1);
    v4u kf[4];
#pragma unroll
    for (int u = 0; u < 4; ++u) kf[u] = *(const LAS v4u*)(ringk + sl + (((4 * u + g) ^ c) << 4));
    f4 s0 = mfma32(kf[0], qf[0], f4{0.f, 0.f, 0.f, 0.f}), s1 = mfma32(kf[2], qf[2], f4{0.f, 0.f, 0.f, 0.f});
    s0 = mfma32(kf[1], qf[1], s0); s1 = mfma32(kf[3], qf[3], s1);
    const u32 a01 = pk(s0[0] + s1[0], s0[1] + s1[1]), a23 = pk(s0[2] + s1[2], s0[3] + s1[3]);
    const u32 b01 = pk(blo(a01) * SCALE, bhi(a01) * SCALE), b23 = pk(blo(a23) * SCALE, bhi(a23) * SCALE);
    const float t0 = blo(b01), t1 = bhi(b01), t2 = blo(b23), t3 = bhi(b23);
    const float mx = xg_max(fmaxf(fmaxf(t0, t1), fmaxf(t2, t3)));
    if (__any(mx > m)) {
      const float mn = fmaxf(m, mx), al = __builtin_amdgcn_exp2f((m - mn) * L2E);
      l *= al; m = mn;
#pragma unroll
      for (int d = 0; d < 8; ++d) acc[d] *= al;
    }
    const float p0 = __builtin_amdgcn_exp2f((t0 - m) * L2E), p1 = __builtin_amdgcn_exp2f((t1 - m) * L2E),
                p2 = __builtin_amdgcn_exp2f((t2 - m) * L2E), p3 = __builtin_amdgcn_exp2f((t3 - m) * L2E);
    l += (p0 + p1) + (p2 + p3);
    const v2u pf = {pk(p0, p1), pk(p2, p3)};
    if (i == NCH - 1) {
      const float lt = xg_sum(l);
      if (lane < 4) *(LAS f2*)(L + L_MLS + w * 32 + c * 8) = f2{m, lt};
    }
    need(2 * i + 2);
    s4 vf[8];
#pragma unroll
    for (int d = 0; d < 8; ++d) vf[d] = __builtin_amdgcn_ds_read_tr16_b64_v4i16((LAS s4*)(size_t)((va ^ (d << 5)) + sl));
    asm volatile("s_waitcnt lgkmcnt(0)" ::: "memory");
    FLAG[4 + w] = i + 1;
#pragma unroll
    for (int d = 0; d < 8; ++d) acc[d] = mfma16(vf[d], pf, acc[d]);
  }
  STAMP(4);
  if (c < 4) {
#pragma unroll
    for (int d = 0; d < 8; ++d) *(LAS f4*)(L + L_OM + w * 2048 + c * 512 + (16 * d + 4 * g) * 4) = acc[d];
  }
  wg_barrier();
  {
    const int hd = lane < 32 ? w : (lane & 3);
    f2 ml[4]; f4 ov[4];
#pragma unroll
    for (int k = 0; k < 4; ++k) { ml[k] = *(const LAS f2*)(L + L_MLS + k * 32 + hd * 8); ov[k] = *(const LAS f4*)(L + L_OM + k * 2048 + w * 512 + (lane & 31) * 16); }
    const float M = fmaxf(fmaxf(ml[0].x, ml[1].x), fmaxf(ml[2].x, ml[3].x));
    float Ls = 0.f; f4 O = {0.f, 0.f, 0.f, 0.f};
#pragma unroll
    for (int k = 0; k < 4; ++k) { const float e = __builtin_amdgcn_exp2f((ml[k].x - M) * L2E); Ls = fmaf(e, ml[k].y, Ls); O += ov[k] * e; }
    const u32 wgi = grp * 8 + s;
    st4(rw, lane < 32 ? WS_O + wgi * OREC + w * 512 + lane * 16 : (lane < 40 && w == 0 ? WS_ML + wgi * 128 + (lane - 32) * 16 : OOB), enc4(lane < 32 ? O : f4{M, Ls, M, Ls}, mktag(tg)));
  }
  STAMP(5);
  DIAG(5);
  wg_barrier();
  wg_barrier();
  wg_barrier();
    if (w < 2) {
      const int col = (kvh * 8 + s) * 8 + 4 * w;
      const u32 off = lane < 56 ? WS_D + (u32)(b * 56 + lane) * 2048 + col * 4 : OOB;
      const v2u hv = *(const LAS v2u*)(L + L_HS + col * 2);
      const u32 tgl = lane < 56 ? tg : 0u;
      pause<FK_D3>();
      STAMP(7);
      v4u d0 = ld4(rw, off);
      for (u32 n = 0; __any(bad4(d0, tgl)); ++n) {
        if (dead || n > SPIN_MEM) { dead = true; break; }
        pause<FK_BO>();
        d0 = ld4(rw, off);
      }
      STAMP(8);
      const u32 a0 = pk(wsum63(dec(d0[0])), wsum63(dec(d0[1]))), a1 = pk(wsum63(dec(d0[2])), wsum63(dec(d0[3])));
      if (lane == 63) *(GAS v2u*)(p.out + b * DM + col) = v2u{pk(blo(hv.x) + blo(a0), bhi(hv.x) + bhi(a0)), pk(blo(hv.y) + blo(a1), bhi(hv.y) + bhi(a1))};
      STAMP(9);
    }
  FLUSH();
}
}

extern "C" void kernel_launch(void* const* d_in, const int* in_sizes, int n_in, void* d_out, int out_size, void* d_ws, size_t ws_size,
                              hipStream_t stream) {
  fk::Params p;
  p.x = (const fk::u16*)d_in[0]; p.k = (const fk::u16*)d_in[1]; p.v = (const fk::u16*)d_in[2];
  p.wq = (const fk::u16*)d_in[3]; p.wo = (const fk::u16*)d_in[4];
  p.wg = (const fk::u16*)d_in[6]; p.wu = (const fk::u16*)d_in[7]; p.wd = (const fk::u16*)d_in[8];
  p.out = (fk::u16*)d_out; p.ws = (unsigned char*)d_ws;
  hipLaunchKernelGGL(fk::fused_block, dim3(256), dim3(1024), 0, stream, p);
}
```
